```python
import torch

def _softmax(x, axis=-1):
    return torch.softmax(x, dim=axis)
def _gelu(x):
    return torch.nn.functional.gelu(x, approximate="tanh")
def _silu(x):
    return torch.nn.functional.silu(x)
def _sigmoid(x):
    return torch.sigmoid(x)
def _layernorm(x, axis=-1, eps=1e-5):
    m = x.mean(dim=axis, keepdim=True)
    v = x.var(dim=axis, keepdim=True, unbiased=False)
    return (x - m) / torch.sqrt(v + eps)
def _scatter_add(tgt, idx, src, axis=0):
    return tgt.clone().index_add_(axis, idx, src)
def _segment_sum(src, seg, n):
    out = torch.zeros((n,) + src.shape[1:], dtype=src.dtype)
    return out.index_add_(0, seg, src)
def _pad(x, pw):
    flat = tuple(v for pair in reversed(pw) for v in pair)
    return torch.nn.functional.pad(x, flat)

kernel_name = 'standalone__mha_block_cached__near_ridge'

def setup_inputs():

    _w = (lambda a: (lambda t: t.to(torch.bfloat16) if t.is_floating_point() else t)(torch.as_tensor(a)))
    B, Sq, D = 8, 208, 512
    H, Sk, Dh = 32, 512, 128
    return {
        "x": _w((lambda *s: torch.randn(*s, dtype=torch.bfloat16) * 0.8)(B, Sq, D)),
        "k_cache": _w((lambda *s: torch.randn(*s, dtype=torch.bfloat16) * 0.8)(B, H, Sk, Dh)),
        "v_cache": _w((lambda *s: torch.randn(*s, dtype=torch.bfloat16) * 0.05)(B, H, Sk, Dh)),
        "wq": _w((lambda *s: torch.randn(*s, dtype=torch.bfloat16) * 0.8)(D, H * Dh) * (D ** -0.5)),
        "wo": _w((lambda *s: torch.randn(*s, dtype=torch.bfloat16) * 0.05)(H * Dh, D) * ((H * Dh) ** -0.5)),
    }

def reference(x, k_cache, v_cache, wq, wo):
    B, Sq, D = x.shape
    H, Dh = 32, 128
    q = (lambda x, *d: x.permute(*d))((x @ wq).reshape(B, Sq, H, Dh), 0, 2, 1, 3)
    scores = torch.einsum("bhqd,bhkd->bhqk", q, k_cache) * (Dh ** -0.5)
    p = _softmax(scores, axis=-1)
    o = torch.einsum("bhqk,bhkd->bhqd", p, v_cache)
    return (lambda x, *d: x.permute(*d))(o, 0, 2, 1, 3).reshape(B, Sq, H * Dh) @ wo
```

```cpp
#include <hip/hip_runtime.h>
#include <hip/hip_bf16.h>

typedef __bf16   bf16x8 __attribute__((ext_vector_type(8)));
typedef __bf16   bf16x2 __attribute__((ext_vector_type(2)));
typedef short    s16x4  __attribute__((ext_vector_type(4)));
typedef short    s16x8  __attribute__((ext_vector_type(8)));
typedef float    f32x4  __attribute__((ext_vector_type(4)));
typedef float    f32x2  __attribute__((ext_vector_type(2)));
typedef unsigned u32x4  __attribute__((ext_vector_type(4)));
typedef unsigned u32x2  __attribute__((ext_vector_type(2)));
#define DI __device__ __forceinline__
#define LAS __attribute__((address_space(3)))
typedef LAS char* lptr;

constexpr int NB = 8, SQ = 208, DM = 512, NH = 32, SK = 512, DH = 128;
constexpr int LDS_BYTES = 163840;
constexpr unsigned KS0 = 0, VS0 = 65536, SPARE = 131072;
constexpr unsigned QST0 = 32768, QSTB = 43008, QX = 16384;
constexpr size_t WS_O = 0, WS_P = 16u << 20;

DI unsigned rfl(unsigned x) { return __builtin_amdgcn_readfirstlane(x); }
DI unsigned cvtpk(float lo, float hi) { f32x2 v = {lo, hi}; bf16x2 b = __builtin_convertvector(v, bf16x2); return __builtin_bit_cast(unsigned, b); }
DI f32x4 mfma(bf16x8 a, bf16x8 b, f32x4 c) { return __builtin_amdgcn_mfma_f32_16x16x32_bf16(a, b, c, 0, 0, 0); }
DI void dma16(__amdgpu_buffer_rsrc_t rs, unsigned voff, unsigned soff, unsigned ldsaddr) {
    asm volatile("s_mov_b32 m0, %0\n\ts_nop 0\n\tbuffer_load_dwordx4 %1, %2, %3 offen lds" :: "s"(ldsaddr), "v"(voff), "s"(rs), "s"(soff) : "memory");
}
DI void dma16_lo32(__amdgpu_buffer_rsrc_t rs, unsigned voff, unsigned soff, unsigned ldsaddr) {
    asm volatile("s_mov_b32 m0, %0\n\ts_mov_b32 exec_hi, 0\n\tbuffer_load_dwordx4 %1, %2, %3 offen lds\n\ts_mov_b32 exec_hi, -1" :: "s"(ldsaddr), "v"(voff), "s"(rs), "s"(soff) : "memory");
}
DI bf16x8 lds128(unsigned addr) { return *(const LAS bf16x8*)(size_t)addr; }
DI s16x4 ldstr(unsigned addr) { return __builtin_amdgcn_ds_read_tr16_b64_v4i16((LAS s16x4*)(size_t)addr); }
DI bf16x8 ldstr2(unsigned a0, unsigned a1) { s16x4 lo = ldstr(a0), hi = ldstr(a1); s16x8 v = __builtin_shufflevector(lo, hi, 0, 1, 2, 3, 4, 5, 6, 7); return __builtin_bit_cast(bf16x8, v); }
#define WAITV(n) asm volatile("s_waitcnt vmcnt(" #n ")" ::: "memory")
#define BAR() asm volatile("s_waitcnt lgkmcnt(0)\n\ts_barrier" ::: "memory")

DI void decode_bh(unsigned blk, unsigned& b, unsigned& h) {
    const unsigned xcd = blk & 7, r = blk >> 3; h = xcd * 4 + (r & 3); b = r >> 2; }

__global__ __launch_bounds__(256, 1) void attn_kernel(const unsigned short* __restrict__ x, const unsigned short* __restrict__ kc, const unsigned short* __restrict__ vc,
                                                      const unsigned short* __restrict__ wq, u32x4* __restrict__ ofr) {
    extern __shared__ __attribute__((aligned(16))) char lds[];
    const unsigned tid = threadIdx.x, l = tid & 63, w = rfl(tid >> 6), c = l & 15, g = l >> 4, q = (l >> 2) & 3, p = l & 3;
    unsigned b, h; decode_bh(blockIdx.x, b, h); const unsigned bh = b * NH + h;
    const unsigned L0 = (unsigned)(size_t)lds;
    const __amdgpu_buffer_rsrc_t rX = __builtin_amdgcn_make_buffer_rsrc((void*)(x + (size_t)b * SQ * DM), (short)0, SQ * DM * 2, 0x00020000);
    const __amdgpu_buffer_rsrc_t rW = __builtin_amdgcn_make_buffer_rsrc((void*)(wq + h * DH), (short)0, DM * NH * DH * 2 - h * DH * 2, 0x00020000);
    const __amdgpu_buffer_rsrc_t rK = __builtin_amdgcn_make_buffer_rsrc((void*)(kc + (size_t)bh * SK * DH), (short)0, SK * DH * 2, 0x00020000);
    const __amdgpu_buffer_rsrc_t rV = __builtin_amdgcn_make_buffer_rsrc((void*)(vc + (size_t)bh * SK * DH), (short)0, SK * DH * 2, 0x00020000);
    unsigned voffW[2], voffX[2], voffK[4], voffV[2];
    for (int v = 0; v < 2; ++v) { voffW[v] = g * 8192 + ((c ^ ((g << 2) | v)) * 16); voffX[v] = (l >> 3) * 1024 + (((l & 7) ^ ((4 * v + g) & 7)) * 16); voffV[v] = g * 256 + ((c ^ (((4 * v + g) & 7) << 1)) * 16); }
    for (int v = 0; v < 4; ++v) voffK[v] = g * 256 + ((c ^ (4 * v + g)) * 16);
    const unsigned voffXS = (l >> 3) * 1024 + (((l & 7) ^ ((2 * w + g) & 7)) * 16);
    auto stage_q = [&](int st) { const unsigned base = L0 + QST0 + (st % 3) * QSTB;
#pragma unroll
        for (int i = 0; i < 4; ++i) dma16(rW, voffW[(i >> 1) & 1], (st * 64 + 4 * (4 * w + i)) * 8192, base + (4 * w + i) * 1024);
#pragma unroll
        for (int i = 0; i < 6; ++i) dma16(rX, voffX[i & 1], (8 * (6 * w + i)) * 1024 + st * 128, base + QX + (6 * w + i) * 1024);
        dma16_lo32(rX, voffXS, (192 + 4 * w) * 1024 + st * 128, base + QX + (192 + 4 * w) * 128); };
    auto stage_k = [&](int t) {
#pragma unroll
        for (int i = 0; i < 8; ++i) dma16(rK, voffK[i & 3], t * 32768 + (8 * w + i) * 1024, L0 + KS0 + (t & 1) * 32768 + (8 * w + i) * 1024); };
    auto stage_v = [&](int t) {
#pragma unroll
        for (int i = 0; i < 8; ++i) dma16(rV, voffV[i & 1], t * 32768 + (8 * w + i) * 1024, L0 + VS0 + (t & 1) * 32768 + (8 * w + i) * 1024); };

    stage_q(0); stage_q(1); stage_k(0);
    f32x4 qa[3][4][2], qs[2];
#pragma unroll
    for (int i = 0; i < 3; ++i) for (int j = 0; j < 4; ++j) for (int a = 0; a < 2; ++a) qa[i][j][a] = f32x4{0, 0, 0, 0};
    qs[0] = qs[1] = f32x4{0, 0, 0, 0};
    unsigned waddr[4][2], xaddr[2];
#pragma unroll
    for (int j = 0; j < 4; ++j) for (int a = 0; a < 2; ++a) waddr[j][a] = (8 * g + q) * 256 + (((4 * (w ^ j) + p) ^ ((q << 2) | (g & 1))) * 16) + ((a ^ (p & 1)) * 8);
#pragma unroll
    for (int ks = 0; ks < 2; ++ks) xaddr[ks] = c * 128 + (((4 * ks + g) ^ ((c >> 1) & 7)) * 16);
#pragma unroll
    for (int st = 0; st < 8; ++st) {
        if (st == 0) WAITV(19); else if (st == 1) WAITV(19); else if (st < 7) WAITV(11); else WAITV(0);
        BAR();
        if (st + 2 < 8) stage_q(st + 2);
        const unsigned base = L0 + QST0 + (st % 3) * QSTB;
#pragma unroll
        for (int ks = 0; ks < 2; ++ks) {
            bf16x8 xf[4];
#pragma unroll
            for (int i = 0; i < 3; ++i) xf[i] = lds128(base + QX + xaddr[ks] + (3 * w + i) * 2048);
            xf[3] = lds128(base + QX + xaddr[ks] + 12 * 2048);
#pragma unroll
            for (int j = 0; j < 4; ++j)
#pragma unroll
                for (int a = 0; a < 2; ++a) {
                    const bf16x8 wf = ldstr2(base + waddr[j][a] + ks * 8192, base + waddr[j][a] + ks * 8192 + 1024);
#pragma unroll
                    for (int i = 0; i < 3; ++i) qa[i][j][a] = mfma(wf, xf[i], qa[i][j][a]);
                    if (j == 0) qs[a] = mfma(wf, xf[3], qs[a]);
                }
        }
    }
    BAR();
    stage_v(0); stage_k(1); stage_v(1);
    const float C = 0.12751743082f;
    bf16x8 qf[4][4];
    auto packq = [&](f32x4 t0, f32x4 t1) { const unsigned a0 = cvtpk(t0[0] * C, t0[1] * C), a1 = cvtpk(t0[2] * C, t0[3] * C), b0 = cvtpk(t1[0] * C, t1[1] * C), b1 = cvtpk(t1[2] * C, t1[3] * C);
        const bool odd = g & 1; u32x4 r = {odd ? b0 : a0, odd ? b1 : a1, odd ? a0 : b0, odd ? a1 : b1}; return __builtin_bit_cast(bf16x8, r); };
#pragma unroll
    for (int i = 0; i < 3; ++i) for (int j = 0; j < 4; ++j) qf[i][j] = packq(qa[i][j][0], qa[i][j][1]);
    *(LAS bf16x8*)(size_t)(L0 + SPARE + w * 1024 + l * 16) = packq(qs[0], qs[1]);
    BAR();
#pragma unroll
    for (int j = 0; j < 4; ++j) qf[3][j] = lds128(L0 + SPARE + (w ^ j) * 1024 + l * 16);

    f32x4 o[4][8];
#pragma unroll
    for (int i = 0; i < 4; ++i) for (int u = 0; u < 8; ++u) o[i][u] = f32x4{0, 0, 0, 0};
    float m[4], ls[4];
#pragma unroll
    for (int i = 0; i < 4; ++i) { m[i] = -1e30f; ls[i] = 0.f; }
    unsigned kaddr[4], vaddr[8];
#pragma unroll
    for (int j = 0; j < 4; ++j) kaddr[j] = L0 + KS0 + c * 256 + (((4 * (w ^ j) + g) ^ c) * 16);
#pragma unroll
    for (int u = 0; u < 8; ++u) vaddr[u] = L0 + VS0 + (4 * g + q) * 256 + ((u ^ ((4 * g + q) & 7)) * 32) + p * 8;
    for (int t = 0; t < 4; ++t) {
        if (t < 3) WAITV(16); else WAITV(0);
        BAR();
        const unsigned slot = (t & 1) * 32768;
#pragma unroll
        for (int jj = 0; jj < 4; ++jj) {
            const unsigned go = slot + (w ^ jj) * 8192;
            constexpr int NBLK = 4;
            f32x4 s[4][2];
#pragma unroll
            for (int i = 0; i < 4; ++i) s[i][0] = s[i][1] = f32x4{0, 0, 0, 0};
#pragma unroll
            for (int a = 0; a < 2; ++a)
#pragma unroll
                for (int j = 0; j < 4; ++j) {
                    const bf16x8 kf = lds128(kaddr[j] + go + a * 4096);
#pragma unroll
                    for (int i = 0; i < NBLK; ++i) if (i < 3 || jj == 0) s[i][a] = mfma(kf, qf[i][j], s[i][a]);
                }
            bf16x8 pf[4];
#pragma unroll
            for (int i = 0; i < NBLK; ++i) if (i < 3 || jj == 0) {
                float mx = fmaxf(fmaxf(fmaxf(s[i][0][0], s[i][0][1]), fmaxf(s[i][0][2], s[i][0][3])), fmaxf(fmaxf(s[i][1][0], s[i][1][1]), fmaxf(s[i][1][2], s[i][1][3])));
                if (__any(mx - m[i] > 8.f)) {
                    mx = fmaxf(mx, __shfl_xor(mx, 16)); mx = fmaxf(mx, __shfl_xor(mx, 32));
                    const float mn = fmaxf(m[i], mx), al = __builtin_amdgcn_exp2f(m[i] - mn); m[i] = mn; ls[i] *= al;
#pragma unroll
                    for (int u = 0; u < 8; ++u) o[i][u] *= al;
                }
                float e[8];
#pragma unroll
                for (int r = 0; r < 8; ++r) { e[r] = __builtin_amdgcn_exp2f(s[i][r >> 2][r & 3] - m[i]); ls[i] += e[r]; }
                u32x4 pk = {cvtpk(e[0], e[1]), cvtpk(e[2], e[3]), cvtpk(e[4], e[5]), cvtpk(e[6], e[7])};
                pf[i] = __builtin_bit_cast(bf16x8, pk);
            }
#pragma unroll
            for (int u = 0; u < 8; ++u) {
                const bf16x8 vf = ldstr2(vaddr[u] + go, vaddr[u] + go + 4096);
#pragma unroll
                for (int i = 0; i < NBLK; ++i) if (i < 3 || jj == 0) o[i][u] = mfma(vf, pf[i], o[i][u]);
            }
        }
        BAR();
        if (t + 2 < 4) { stage_k(t + 2); stage_v(t + 2); }
    }
#pragma unroll
    for (int i = 0; i < 4; ++i) { ls[i] += __shfl_xor(ls[i], 16); ls[i] += __shfl_xor(ls[i], 32); }
    u32x4* ob = ofr + (size_t)bh * 13 * 4 * 64 + l;
#pragma unroll
    for (int i = 0; i < 3; ++i) { const float il = __builtin_amdgcn_rcpf(ls[i]);
#pragma unroll
        for (int s2 = 0; s2 < 4; ++s2) { const f32x4 t0 = o[i][2 * s2] * il, t1 = o[i][2 * s2 + 1] * il;
            ob[((3 * w + i) * 4 + s2) * 64] = u32x4{cvtpk(t0[0], t0[1]), cvtpk(t0[2], t0[3]), cvtpk(t1[0], t1[1]), cvtpk(t1[2], t1[3])}; } }
    LAS float* ml = (LAS float*)(size_t)(L0 + SPARE + 8192);
    if (g == 0) { ml[w * 32 + c] = m[3]; ml[w * 32 + 16 + c] = ls[3]; }
    BAR();
    { float M = ml[c]; for (int k = 1; k < 4; ++k) M = fmaxf(M, ml[k * 32 + c]);
      float Lt = 0; for (int k = 0; k < 4; ++k) Lt += ml[k * 32 + 16 + c] * __builtin_amdgcn_exp2f(ml[k * 32 + c] - M);
      const float f = __builtin_amdgcn_exp2f(m[3] - M) * __builtin_amdgcn_rcpf(Lt);
#pragma unroll
      for (int u = 0; u < 8; ++u) *(LAS f32x4*)(size_t)(L0 + (w * 8 + u) * 1024 + l * 16) = o[3][u] * f; }
    BAR();
    { f32x4 t0 = {0, 0, 0, 0}, t1 = {0, 0, 0, 0};
#pragma unroll
      for (int k = 0; k < 4; ++k) { t0 += *(LAS f32x4*)(size_t)(L0 + (k * 8 + 2 * w) * 1024 + l * 16); t1 += *(LAS f32x4*)(size_t)(L0 + (k * 8 + 2 * w + 1) * 1024 + l * 16); }
      ob[(12 * 4 + w) * 64] = u32x4{cvtpk(t0[0], t0[1]), cvtpk(t0[2], t0[3]), cvtpk(t1[0], t1[1]), cvtpk(t1[2], t1[3])}; }
}

__global__ __launch_bounds__(256, 1) void oproj_kernel(const u32x4* __restrict__ ofr, const unsigned short* __restrict__ wo, f32x4* __restrict__ part) {
    extern __shared__ __attribute__((aligned(16))) char lds[];
    const unsigned tid = threadIdx.x, l = tid & 63, w = rfl(tid >> 6), c = l & 15, g = l >> 4, q = (l >> 2) & 3, p = l & 3;
    const unsigned hq = blockIdx.x & 7, nq = (blockIdx.x >> 3) & 3, b = blockIdx.x >> 5;
    const unsigned L0 = (unsigned)(size_t)lds;
    const __amdgpu_buffer_rsrc_t rW = __builtin_amdgcn_make_buffer_rsrc((void*)(wo + (size_t)hq * 512 * DM + nq * 128), (short)0, 512 * DM * 2 - nq * 256, 0x00020000);
    unsigned voffV[2];
    for (int v = 0; v < 2; ++v) voffV[v] = g * 1024 + ((c ^ (((4 * v + g) & 7) << 1)) * 16);
#pragma unroll
    for (int hh = 0; hh < 4; ++hh)
#pragma unroll
        for (int i = 0; i < 8; ++i) dma16(rW, voffV[i & 1], (hh * 128 + (8 * w + i) * 4) * 1024, L0 + hh * 32768 + (8 * w + i) * 1024);
    f32x4 acc[3][8], accs[2];
#pragma unroll
    for (int i = 0; i < 3; ++i) for (int u = 0; u < 8; ++u) acc[i][u] = f32x4{0, 0, 0, 0};
    accs[0] = accs[1] = f32x4{0, 0, 0, 0};
    unsigned vaddr[8];
#pragma unroll
    for (int j = 0; j < 8; ++j) vaddr[j] = L0 + (4 * g + q) * 256 + (((j ^ (2 * w)) ^ ((4 * g + q) & 7)) * 32) + p * 8;
    WAITV(0); BAR();
#pragma unroll
    for (int hh = 0; hh < 4; ++hh) {
        const u32x4* ob = ofr + (size_t)(b * NH + hq * 4 + hh) * 13 * 4 * 64 + l;
#pragma unroll
        for (int s2 = 0; s2 < 4; ++s2) {
            bf16x8 of[4];
#pragma unroll
            for (int i = 0; i < 3; ++i) of[i] = __builtin_bit_cast(bf16x8, ob[((3 * w + i) * 4 + s2) * 64]);
            of[3] = __builtin_bit_cast(bf16x8, ob[(12 * 4 + s2) * 64]);
#pragma unroll
            for (int j = 0; j < 8; ++j) {
                const unsigned a0 = vaddr[j] + hh * 32768 + s2 * 8192;
                const bf16x8 wf = ldstr2(a0, a0 + 4096);
#pragma unroll
                for (int i = 0; i < 3; ++i) acc[i][j] = mfma(wf, of[i], acc[i][j]);
                if (j < 2) accs[j] = mfma(wf, of[3], accs[j]);
            }
        }
    }
    f32x4* pb = part + (size_t)((b * 4 + nq) * 8 + hq) * 13 * 8 * 64 + l;
#pragma unroll
    for (int i = 0; i < 3; ++i)
#pragma unroll
        for (int j = 0; j < 8; ++j) pb[((3 * w + i) * 8 + (j ^ (2 * w))) * 64] = acc[i][j];
#pragma unroll
    for (int j = 0; j < 2; ++j) pb[(12 * 8 + (j ^ (2 * w))) * 64] = accs[j];
}

__global__ __launch_bounds__(256) void reduce_kernel(const f32x4* __restrict__ part, u32x2* __restrict__ out) {
    const unsigned t = blockIdx.x * 256 + threadIdx.x;
    const unsigned l = t & 63, r = t >> 6, iu = r % 104, bn = r / 104, u = iu & 7, i = iu >> 3, nq = bn & 3, b = bn >> 2;
    const f32x4* pb = part + (size_t)bn * 8 * 104 * 64 + iu * 64 + l;
    f32x4 a = pb[0];
#pragma unroll
    for (int k = 1; k < 8; ++k) a += pb[(size_t)k * 104 * 64];
    out[((size_t)(b * SQ + 16 * i + (l & 15)) * DM + nq * 128 + u * 16 + (l >> 4) * 4) / 4] = u32x2{cvtpk(a[0], a[1]), cvtpk(a[2], a[3])};
}

extern "C" void kernel_launch(void* const* d_in, const int* in_sizes, int n_in, void* d_out, int out_size, void* d_ws, size_t ws_size, hipStream_t stream) {
    (void)hipFuncSetAttribute((const void*)attn_kernel, hipFuncAttributeMaxDynamicSharedMemorySize, LDS_BYTES);
    (void)hipFuncSetAttribute((const void*)oproj_kernel, hipFuncAttributeMaxDynamicSharedMemorySize, 131072);
    u32x4* ofr = (u32x4*)((char*)d_ws + WS_O); f32x4* part = (f32x4*)((char*)d_ws + WS_P);
    attn_kernel<<<256, 256, LDS_BYTES, stream>>>((const unsigned short*)d_in[0], (const unsigned short*)d_in[1], (const unsigned short*)d_in[2], (const unsigned short*)d_in[3], ofr);
    oproj_kernel<<<256, 256, 131072, stream>>>(ofr, (const unsigned short*)d_in[4], part);
    reduce_kernel<<<832, 256, 0, stream>>>(part, (u32x2*)d_out);
}
```

```cpp
#include <hip/hip_runtime.h>
#include <hip/hip_bf16.h>

typedef unsigned u32x16 __attribute__((ext_vector_type(16)));
#define DI __device__ __forceinline__
constexpr int SQ = 208, DM = 512, NH = 32;
constexpr size_t WS_C = 0, WS_O = 1u << 20, WS_P = 15u << 20;

DI unsigned rfl(unsigned x) { return __builtin_amdgcn_readfirstlane(x); }
DI void dma16(__amdgpu_buffer_rsrc_t rs, unsigned voff, unsigned soff, unsigned ldsaddr) {
    asm volatile("s_mov_b32 m0, %0\n\ts_nop 0\n\tbuffer_load_dwordx4 %1, %2, %3 offen lds" :: "s"(ldsaddr), "v"(voff), "s"(rs), "s"(soff) : "memory"); }
DI void dma16_lo32(__amdgpu_buffer_rsrc_t rs, unsigned voff, unsigned soff, unsigned ldsaddr) {
    asm volatile("s_mov_b32 m0, %0\n\ts_mov_b32 exec_hi, 0\n\tbuffer_load_dwordx4 %1, %2, %3 offen lds\n\ts_mov_b32 exec_hi, -1" :: "s"(ldsaddr), "v"(voff), "s"(rs), "s"(soff) : "memory"); }
#define RSRC(p, n) __builtin_amdgcn_make_buffer_rsrc((void*)(p), (short)0, (n), 0x00020000)

__global__ __launch_bounds__(256, 1) void mha_kernel(const char* __restrict__ x, const char* __restrict__ kc, const char* __restrict__ vc, const char* __restrict__ wq,
                                                     const char* __restrict__ wo, char* __restrict__ out, char* __restrict__ ws) {
    __shared__ __attribute__((aligned(16))) char lds[163840];
    const unsigned tid = threadIdx.x, l = tid & 63, w = rfl(tid >> 6), c = l & 15, g = l >> 4, q = (l >> 2) & 3, p = l & 3;
    const unsigned xcd = blockIdx.x & 7, r = blockIdx.x >> 3, h = (xcd & 3) * 8 + (r & 7), b = (xcd >> 2) * 4 + (r >> 3), rot = ((h & 7) + 2 * (b & 3)) % 6, bh = b * NH + h;
    const unsigned hq = h >> 2, nq = h & 3;
    const unsigned L0 = (unsigned)(size_t)lds;
    constexpr unsigned WR = 32768, XR = 81920;
    {
        unsigned long long pc; unsigned lim, o = (r + 32 * tid) * 128;
        asm volatile("s_getpc_b64 %0\n.Lmha_beg:\n\ts_mov_b32 %1, .Lmha_end-.Lmha_beg-4\n\tv_min_u32 %2, %1, %2\n\ts_mov_b32 m0, %3\n\ts_nop 0\n\tglobal_load_lds_dword %2, %0"
                     : "=&s"(pc), "=&s"(lim), "+v"(o) : "s"(L0 + w * 256) : "memory"); }
    u32x16 qa; unsigned xs;
#pragma unroll
    for (int v = 0; v < 2; ++v) { qa[12 + v] = w * 131072 + g * 8192 + ((c ^ ((g << 2) | v)) * 16); qa[14 + v] = w * 49152 + (l >> 3) * 1024 + (((l & 7) ^ ((4 * v + g) & 7)) * 16); }
    xs = (192 + 4 * w) * 1024 + (l >> 3) * 1024 + (((l & 7) ^ ((2 * w + g) & 7)) * 16);
#pragma unroll
    for (int j = 0; j < 4; ++j)
#pragma unroll
        for (int a = 0; a < 2; ++a) qa[j * 2 + a] = WR + (8 * g + q) * 256 + (((4 * (w ^ j) + p) ^ ((q << 2) | (g & 1))) * 16) + ((a ^ (p & 1)) * 8);
#pragma unroll
    for (int ks = 0; ks < 2; ++ks) { const unsigned t = XR + c * 128 + (((4 * ks + g) ^ ((c >> 1) & 7)) * 16); qa[8 + ks] = t + w * 6144; qa[10 + ks] = t + 24576; }
    asm volatile("" : "+v"(qa));
    __builtin_amdgcn_sched_barrier(0);
    const __amdgpu_buffer_rsrc_t rX = RSRC(x + (size_t)b * SQ * DM * 2, SQ * DM * 2);
    const __amdgpu_buffer_rsrc_t rW = RSRC(wq + h * 256, DM * 8192 - h * 256);
    { const unsigned st = h & 1;
#pragma unroll
      for (int i = 0; i < 4; ++i) dma16(rW, qa[12 + ((i >> 1) & 1)], st * 524288 + i * 32768, L0 + WR + w * 4096 + i * 1024);
#pragma unroll
      for (int i = 0; i < 6; ++i) dma16(rX, qa[14 + (i & 1)], st * 128 + i * 8192, L0 + XR + w * 6144 + i * 1024);
      dma16_lo32(rX, xs, st * 128, L0 + XR + 24576 + w * 512); }
    __builtin_amdgcn_sched_barrier(0);
    const unsigned k3 = (hq * 11) >> 5, v1 = hq == 0 ? 3 : 0, i1 = hq == 0 ? 2 : hq - 1;
    unsigned red0 = hq * 8192, red1 = hq < 5 ? (hq + 8) * 8192 : 0x40000000u;
    unsigned out0 = (3 * w + hq - 3 * k3) * 16384 + ((1 + k3) ^ w) * 64, out1 = hq < 5 ? (i1 < 3 ? 3 * w + i1 : 12) * 16384 + (v1 ^ w) * 64 : 0x40000000u;
    const __amdgpu_buffer_rsrc_t rK = RSRC(kc + (size_t)bh * 131072, 131072);
    const __amdgpu_buffer_rsrc_t rV = RSRC(vc + (size_t)bh * 131072, 131072);
    const __amdgpu_buffer_rsrc_t rWo = RSRC(wo + (size_t)hq * 524288, 524288);
    const __amdgpu_buffer_rsrc_t rO = RSRC(ws + WS_O + (size_t)(b * NH + hq * 4) * 53248, 4 * 53248);
    const __amdgpu_buffer_rsrc_t rP = RSRC(ws + WS_P + (size_t)(b * 4 + nq) * (52 * 8192), 52 * 8192);
    const __amdgpu_buffer_rsrc_t rC = RSRC(ws + WS_C, 256 * 1024);
    const __amdgpu_buffer_rsrc_t rOut = RSRC(out + (size_t)b * SQ * 1024 + nq * 256, SQ * 1024 - nq * 256);
    asm volatile(
      "s_lshl_b32 s40, %[w], 12\n"
      "s_mul_i32 s41, %[w], 6144\n"
      "s_lshl_b32 s42, %[w], 9\n"
      "s_lshl_b32 s43, %[w], 10\n"
      "s_andn2_b32 s46, 1, %[nq]\n"
      "s_lshl_b32 s44, s46, 19\n"
      "s_lshl_b32 s45, s46, 7\n"
      "s_add_u32 m0, s40, 49152\n"
      "s_add_u32 s46, s44, 0\n"
      "buffer_load_dwordx4 v148, %[rW], s46 offen lds\n"
      "s_add_u32 m0, s40, 50176\n"
      "s_add_u32 s46, s44, 32768\n"
      "buffer_load_dwordx4 v148, %[rW], s46 offen lds\n"
      "s_add_u32 m0, s40, 51200\n"
      "s_add_u32 s46, s44, 65536\n"
      "buffer_load_dwordx4 v149, %[rW], s46 offen lds\n"
      "s_add_u32 m0, s40, 52224\n"
      "s_add_u32 s46, s44, 98304\n"
      "buffer_load_dwordx4 v149, %[rW], s46 offen lds\n"
      "s_add_u32 m0, s41, 108544\n"
      "s_add_u32 s46, s45, 0\n"
      "buffer_load_dwordx4 v150, %[rX], s46 offen lds\n"
      "s_add_u32 m0, s41, 109568\n"
      "s_add_u32 s46, s45, 8192\n"
      "buffer_load_dwordx4 v151, %[rX], s46 offen lds\n"
      "s_add_u32 m0, s41, 110592\n"
      "s_add_u32 s46, s45, 16384\n"
      "buffer_load_dwordx4 v150, %[rX], s46 offen lds\n"
      "s_add_u32 m0, s41, 111616\n"
      "s_add_u32 s46, s45, 24576\n"
      "buffer_load_dwordx4 v151, %[rX], s46 offen lds\n"
      "s_add_u32 m0, s41, 112640\n"
      "s_add_u32 s46, s45, 32768\n"
      "buffer_load_dwordx4 v150, %[rX], s46 offen lds\n"
      "s_add_u32 m0, s41, 113664\n"
      "s_add_u32 s46, s45, 40960\n"
      "buffer_load_dwordx4 v151, %[rX], s46 offen lds\n"
      "s_add_u32 m0, s42, 133120\n"
      "s_mov_b32 exec_hi, 0\n"
      "buffer_load_dwordx4 v251, %[rX], s45 offen lds\n"
      "s_mov_b32 exec_hi, -1\n"
      "s_waitcnt vmcnt(11)\n"
      "s_barrier\n"
      "ds_read_b128 v[104:107], v144 offset:0\n"
      "ds_read_b128 v[108:111], v144 offset:2048\n"
      "ds_read_b128 v[112:115], v144 offset:4096\n"
      "ds_read_b128 v[116:119], v146 offset:0\n"
      "ds_read_b64_tr_b16 a[64:65], v136 offset:0\n"
      "ds_read_b64_tr_b16 a[66:67], v136 offset:1024\n"
      "ds_read_b64_tr_b16 a[68:69], v137 offset:0\n"
      "ds_read_b64_tr_b16 a[70:71], v137 offset:1024\n"
      "ds_read_b64_tr_b16 a[72:73], v138 offset:0\n"
      "ds_read_b64_tr_b16 a[74:75], v138 offset:1024\n"
      "ds_read_b64_tr_b16 a[76:77], v139 offset:0\n"
      "ds_read_b64_tr_b16 a[78:79], v139 offset:1024\n"
      "ds_read_b64_tr_b16 a[80:81], v140 offset:0\n"
      "ds_read_b64_tr_b16 a[82:83], v140 offset:1024\n"
      "s_waitcnt lgkmcnt(8)\n"
      "v_mfma_f32_16x16x32_bf16 v[0:3], a[64:67], v[104:107], 0\n"
      "s_add_u32 s46, %[rot], 2\n"
      "s_sub_u32 s45, s46, 6\n"
      "s_cmp_gt_u32 s46, 7\n"
      "s_cselect_b32 s46, s45, s46\n"
      "ds_read_b64_tr_b16 a[84:85], v141 offset:0\n"
      "ds_read_b64_tr_b16 a[86:87], v141 offset:1024\n"
      "v_mfma_f32_16x16x32_bf16 v[32:35], a[64:67], v[108:111], 0\n"
      "s_lshl_b32 s44, s46, 19\n"
      "ds_read_b64_tr_b16 a[88:89], v142 offset:0\n"
      "ds_read_b64_tr_b16 a[90:91], v142 offset:1024\n"
      "v_mfma_f32_16x16x32_bf16 v[64:67], a[64:67], v[112:115], 0\n"
      "s_lshl_b32 s45, s46, 7\n"
      "ds_read_b64_tr_b16 a[92:93], v143 offset:0\n"
      "ds_read_b64_tr_b16 a[94:95], v143 offset:1024\n"
      "v_mfma_f32_16x16x32_bf16 v[96:99], a[64:67], v[116:119], 0\n"
      "s_waitcnt lgkmcnt(12)\n"
      "s_add_u32 m0, s40, 65536\n"
      "s_add_u32 s46, s44, 0\n"
      "buffer_load_dwordx4 v148, %[rW], s46 offen lds\n"
      "v_mfma_f32_16x16x32_bf16 v[4:7], a[68:71], v[104:107], 0\n"
      "s_add_u32 m0, s40, 66560\n"
      "ds_read_b128 v[120:123], v145 offset:0\n"
      "v_mbcnt_lo_u32_b32 v192, -1, 0\n"
      "v_mfma_f32_16x16x32_bf16 v[36:39], a[68:71], v[108:111], 0\n"
      "s_add_u32 s46, s44, 32768\n"
      "buffer_load_dwordx4 v148, %[rW], s46 offen lds\n"
      "ds_read_b128 v[124:127], v145 offset:2048\n"
      "v_mfma_f32_16x16x32_bf16 v[68:71], a[68:71], v[112:115], 0\n"
      "s_add_u32 m0, s40, 67584\n"
      "v_mbcnt_hi_u32_b32 v192, -1, v192\n"
      "v_and_b32 v193, 15, v192\n"
      "v_mfma_f32_16x16x32_bf16 v[100:103], a[68:71], v[116:119], 0\n"
      "s_waitcnt lgkmcnt(12)\n"
      "ds_read_b128 v[128:131], v145 offset:4096\n"
      "ds_read_b128 v[132:135], v147 offset:0\n"
      "v_mfma_f32_16x16x32_bf16 v[8:11], a[72:75], v[104:107], 0\n"
      "s_add_u32 s46, s44, 65536\n"
      "buffer_load_dwordx4 v149, %[rW], s46 offen lds\n"
      "v_lshrrev_b32 v194, 4, v192\n"
      "v_mfma_f32_16x16x32_bf16 v[40:43], a[72:75], v[108:111], 0\n"
      "s_add_u32 m0, s40, 68608\n"
      "v_lshlrev_b32 v195, 4, v192\n"
      "v_mfma_f32_16x16x32_bf16 v[72:75], a[72:75], v[112:115], 0\n"
      "s_waitcnt lgkmcnt(12)\n"
      "ds_read_b64_tr_b16 a[64:65], v136 offset:8192\n"
      "ds_read_b64_tr_b16 a[66:67], v136 offset:9216\n"
      "v_mfma_f32_16x16x32_bf16 v[12:15], a[76:79], v[104:107], 0\n"
      "s_add_u32 s46, s44, 98304\n"
      "buffer_load_dwordx4 v149, %[rW], s46 offen lds\n"
      "v_lshlrev_b32 v196, 4, v193\n"
      "v_mfma_f32_16x16x32_bf16 v[44:47], a[76:79], v[108:111], 0\n"
      "s_add_u32 m0, s41, 135168\n"
      "v_lshrrev_b32 v199, 2, v192\n"
      "v_mfma_f32_16x16x32_bf16 v[76:79], a[76:79], v[112:115], 0\n"
      "s_waitcnt lgkmcnt(12)\n"
      "ds_read_b64_tr_b16 a[68:69], v137 offset:8192\n"
      "ds_read_b64_tr_b16 a[70:71], v137 offset:9216\n"
      "v_mfma_f32_16x16x32_bf16 v[16:19], a[80:83], v[104:107], 0\n"
      "s_add_u32 s46, s45, 0\n"
      "buffer_load_dwordx4 v150, %[rX], s46 offen lds\n"
      "v_mfma_f32_16x16x32_bf16 v[48:51], a[80:83], v[108:111], 0\n"
      "s_add_u32 m0, s41, 136192\n"
      "v_mfma_f32_16x16x32_bf16 v[80:83], a[80:83], v[112:115], 0\n"
      "s_waitcnt lgkmcnt(12)\n"
      "ds_read_b64_tr_b16 a[72:73], v138 offset:8192\n"
      "ds_read_b64_tr_b16 a[74:75], v138 offset:9216\n"
      "v_mfma_f32_16x16x32_bf16 v[20:23], a[84:87], v[104:107], 0\n"
      "s_add_u32 s46, s45, 8192\n"
      "buffer_load_dwordx4 v151, %[rX], s46 offen lds\n"
      "v_mfma_f32_16x16x32_bf16 v[52:55], a[84:87], v[108:111], 0\n"
      "s_lshl_b32 s58, %[w], 6\n"
      "v_lshl_add_u32 v153, v194, 4, s58\n"
      "v_accvgpr_write_b32 a254, v153\n"
      "v_mfma_f32_16x16x32_bf16 v[84:87], a[84:87], v[112:115], 0\n"
      "s_waitcnt lgkmcnt(12)\n"
      "ds_read_b64_tr_b16 a[76:77], v139 offset:8192\n"
      "ds_read_b64_tr_b16 a[78:79], v139 offset:9216\n"
      "v_mfma_f32_16x16x32_bf16 v[24:27], a[88:91], v[104:107], 0\n"
      "s_add_u32 m0, s41, 137216\n"
      "s_add_u32 s46, s45, 16384\n"
      "buffer_load_dwordx4 v150, %[rX], s46 offen lds\n"
      "v_mfma_f32_16x16x32_bf16 v[56:59], a[88:91], v[108:111], 0\n"
      "s_add_u32 m0, s41, 138240\n"
      "v_xor_b32 v197, v195, v153\n"
      "v_add_u32 v248, s43, v197\n"
      "v_mfma_f32_16x16x32_bf16 v[88:91], a[88:91], v[112:115], 0\n"
      "s_waitcnt lgkmcnt(12)\n"
      "ds_read_b64_tr_b16 a[80:81], v140 offset:8192\n"
      "ds_read_b64_tr_b16 a[82:83], v140 offset:9216\n"
      "v_mfma_f32_16x16x32_bf16 v[28:31], a[92:95], v[104:107], 0\n"
      "s_add_u32 s46, s45, 24576\n"
      "buffer_load_dwordx4 v151, %[rX], s46 offen lds\n"
      "v_xor_b32 v197, v153, v196\n"
      "v_mfma_f32_16x16x32_bf16 v[60:63], a[92:95], v[108:111], 0\n"
      "s_add_u32 m0, s41, 139264\n"
      "v_lshl_add_u32 v240, v193, 8, v197\n"
      "v_accvgpr_write_b32 a255, v240\n"
      "v_mfma_f32_16x16x32_bf16 v[92:95], a[92:95], v[112:115], 0\n"
      "s_waitcnt lgkmcnt(8)\n"
      "ds_read_b64_tr_b16 a[84:85], v141 offset:8192\n"
      "ds_read_b64_tr_b16 a[86:87], v141 offset:9216\n"
      "v_mfma_f32_16x16x32_bf16 v[0:3], a[64:67], v[120:123], v[0:3]\n"
      "s_add_u32 s46, s45, 32768\n"
      "buffer_load_dwordx4 v150, %[rX], s46 offen lds\n"
      "v_xor_b32 v241, 64, v240\n"
      "v_mfma_f32_16x16x32_bf16 v[32:35], a[64:67], v[124:127], v[32:35]\n"
      "s_add_u32 m0, s41, 140288\n"
      "ds_read_b64_tr_b16 a[88:89], v142 offset:8192\n"
      "ds_read_b64_tr_b16 a[90:91], v142 offset:9216\n"
      "v_mfma_f32_16x16x32_bf16 v[64:67], a[64:67], v[128:131], v[64:67]\n"
      "s_add_u32 s46, s45, 40960\n"
      "buffer_load_dwordx4 v151, %[rX], s46 offen lds\n"
      "v_xor_b32 v242, 128, v240\n"
      "v_mfma_f32_16x16x32_bf16 v[96:99], a[64:67], v[132:135], v[96:99]\n"
      "s_waitcnt lgkmcnt(10)\n"
      "ds_read_b64_tr_b16 a[92:93], v143 offset:8192\n"
      "v_xor_b32 v243, 192, v240\n"
      "v_mfma_f32_16x16x32_bf16 v[4:7], a[68:71], v[120:123], v[4:7]\n"
      "s_add_u32 m0, s42, 159744\n"
      "s_mov_b32 exec_hi, 0\n"
      "buffer_load_dwordx4 v251, %[rX], s45 offen lds\n"
      "s_mov_b32 exec_hi, -1\n"
      "v_mfma_f32_16x16x32_bf16 v[36:39], a[68:71], v[124:127], v[36:39]\n"
      "s_lshl_b32 s58, %[w], 13\n"
      "ds_read_b64_tr_b16 a[94:95], v143 offset:9216\n"
      "v_add_u32 v244, s58, v240\n"
      "v_mfma_f32_16x16x32_bf16 v[68:71], a[68:71], v[128:131], v[68:71]\n"
      "v_add_u32 v245, s58, v241\n"
      "v_add_u32 v246, s58, v242\n"
      "v_mfma_f32_16x16x32_bf16 v[100:103], a[68:71], v[132:135], v[100:103]\n"
      "s_waitcnt lgkmcnt(10)\n"
      "v_add_u32 v247, s58, v243\n"
      "v_and_b32 v197, 7, v192\n"
      "v_mfma_f32_16x16x32_bf16 v[8:11], a[72:75], v[120:123], v[8:11]\n"
      "v_mfma_f32_16x16x32_bf16 v[40:43], a[72:75], v[124:127], v[40:43]\n"
      "v_lshlrev_b32 v198, 1, v194\n"
      "v_mfma_f32_16x16x32_bf16 v[72:75], a[72:75], v[128:131], v[72:75]\n"
      "s_waitcnt lgkmcnt(8)\n"
      "v_xor_b32 v197, v197, v198\n"
      "v_mfma_f32_16x16x32_bf16 v[12:15], a[76:79], v[120:123], v[12:15]\n"
      "v_mfma_f32_16x16x32_bf16 v[44:47], a[76:79], v[124:127], v[44:47]\n"
      "v_lshlrev_b32 v197, 4, v197\n"
      "v_mfma_f32_16x16x32_bf16 v[76:79], a[76:79], v[128:131], v[76:79]\n"
      "s_waitcnt lgkmcnt(6)\n"
      "v_lshrrev_b32 v198, 3, v192\n"
      "v_mfma_f32_16x16x32_bf16 v[16:19], a[80:83], v[120:123], v[16:19]\n"
      "s_waitcnt vmcnt(11) lgkmcnt(0)\n"
      "s_barrier\n"
      "ds_read_b128 v[104:107], v144 offset:26624\n"
      "ds_read_b128 v[108:111], v144 offset:28672\n"
      "v_mfma_f32_16x16x32_bf16 v[48:51], a[80:83], v[124:127], v[48:51]\n"
      "ds_read_b128 v[112:115], v144 offset:30720\n"
      "ds_read_b128 v[116:119], v146 offset:26624\n"
      "ds_read_b64_tr_b16 a[64:65], v136 offset:16384\n"
      "v_mfma_f32_16x16x32_bf16 v[80:83], a[80:83], v[128:131], v[80:83]\n"
      "ds_read_b64_tr_b16 a[66:67], v136 offset:17408\n"
      "ds_read_b64_tr_b16 a[68:69], v137 offset:16384\n"
      "ds_read_b64_tr_b16 a[70:71], v137 offset:17408\n"
      "v_mfma_f32_16x16x32_bf16 v[20:23], a[84:87], v[120:123], v[20:23]\n"
      "ds_read_b64_tr_b16 a[72:73], v138 offset:16384\n"
      "ds_read_b64_tr_b16 a[74:75], v138 offset:17408\n"
      "v_mfma_f32_16x16x32_bf16 v[52:55], a[84:87], v[124:127], v[52:55]\n"
      "ds_read_b64_tr_b16 a[76:77], v139 offset:16384\n"
      "ds_read_b64_tr_b16 a[78:79], v139 offset:17408\n"
      "v_mfma_f32_16x16x32_bf16 v[84:87], a[84:87], v[128:131], v[84:87]\n"
      "ds_read_b64_tr_b16 a[80:81], v140 offset:16384\n"
      "v_lshl_add_u32 v197, v198, 8, v197\n"
      "v_mfma_f32_16x16x32_bf16 v[24:27], a[88:91], v[120:123], v[24:27]\n"
      "s_lshl_b32 s58, %[w], 11\n"
      "v_add_u32 v249, s58, v197\n"
      "v_add_u32 v250, 98304, v195\n"
      "v_mfma_f32_16x16x32_bf16 v[56:59], a[88:91], v[124:127], v[56:59]\n"
      "ds_read_b64_tr_b16 a[82:83], v140 offset:17408\n"
      "v_add_u32 v156, s43, v250\n"
      "v_mfma_f32_16x16x32_bf16 v[88:91], a[88:91], v[128:131], v[88:91]\n"
      "v_accvgpr_write_b32 a246, v195\n"
      "v_and_b32 v198, 3, v198\n"
      "v_mfma_f32_16x16x32_bf16 v[28:31], a[92:95], v[120:123], v[28:31]\n"
      "v_and_b32 v197, 3, v192\n"
      "v_mfma_f32_16x16x32_bf16 v[60:63], a[92:95], v[124:127], v[60:63]\n"
      "v_mfma_f32_16x16x32_bf16 v[92:95], a[92:95], v[128:131], v[92:95]\n"
      "s_waitcnt lgkmcnt(8)\n"
      "ds_read_b64_tr_b16 a[84:85], v141 offset:16384\n"
      "ds_read_b64_tr_b16 a[86:87], v141 offset:17408\n"
      "v_mfma_f32_16x16x32_bf16 v[0:3], a[64:67], v[104:107], v[0:3]\n"
      "ds_read_b64_tr_b16 a[88:89], v142 offset:16384\n"
      "ds_read_b64_tr_b16 a[90:91], v142 offset:17408\n"
      "v_mfma_f32_16x16x32_bf16 v[32:35], a[64:67], v[108:111], v[32:35]\n"
      "v_lshlrev_b32 v200, 3, v197\n"
      "v_lshl_add_u32 v198, v198, 5, v200\n"
      "v_mfma_f32_16x16x32_bf16 v[64:67], a[64:67], v[112:115], v[64:67]\n"
      "ds_read_b64_tr_b16 a[92:93], v143 offset:16384\n"
      "ds_read_b64_tr_b16 a[94:95], v143 offset:17408\n"
      "v_mfma_f32_16x16x32_bf16 v[96:99], a[64:67], v[116:119], v[96:99]\n"
      "s_waitcnt lgkmcnt(12)\n"
      "ds_read_b128 v[120:123], v145 offset:26624\n"
      "v_lshl_add_u32 v198, v199, 7, v198\n"
      "v_mfma_f32_16x16x32_bf16 v[4:7], a[68:71], v[104:107], v[4:7]\n"
      "s_lshl_b32 s58, %[w], 5\n"
      "ds_read_b128 v[124:127], v145 offset:28672\n"
      "v_xor_b32 v201, s58, v198\n"
      "v_mfma_f32_16x16x32_bf16 v[36:39], a[68:71], v[108:111], v[36:39]\n"
      "v_accvgpr_write_b32 a240, v201\n"
      "v_mfma_f32_16x16x32_bf16 v[68:71], a[68:71], v[112:115], v[68:71]\n"
      "v_add_u32 v252, 131072, v198\n"
      "v_mfma_f32_16x16x32_bf16 v[100:103], a[68:71], v[116:119], v[100:103]\n"
      "s_waitcnt lgkmcnt(12)\n"
      "ds_read_b128 v[128:131], v145 offset:30720\n"
      "ds_read_b128 v[132:135], v147 offset:26624\n"
      "v_mfma_f32_16x16x32_bf16 v[8:11], a[72:75], v[104:107], v[8:11]\n"
      "v_xor_b32 v253, 32, v252\n"
      "v_mfma_f32_16x16x32_bf16 v[40:43], a[72:75], v[108:111], v[40:43]\n"
      "v_xor_b32 v254, 64, v252\n"
      "v_mfma_f32_16x16x32_bf16 v[72:75], a[72:75], v[112:115], v[72:75]\n"
      "s_waitcnt lgkmcnt(12)\n"
      "ds_read_b64_tr_b16 a[64:65], v136 offset:24576\n"
      "ds_read_b64_tr_b16 a[66:67], v136 offset:25600\n"
      "v_mfma_f32_16x16x32_bf16 v[12:15], a[76:79], v[104:107], v[12:15]\n"
      "s_add_u32 s46, %[rot], 3\n"
      "s_sub_u32 s45, s46, 6\n"
      "s_cmp_gt_u32 s46, 7\n"
      "s_cselect_b32 s46, s45, s46\n"
      "v_xor_b32 v255, 96, v252\n"
      "v_mfma_f32_16x16x32_bf16 v[44:47], a[76:79], v[108:111], v[44:47]\n"
      "s_lshl_b32 s44, s46, 19\n"
      "v_lshl_add_u32 v184, v197, 10, %[cq]\n"
      "v_mfma_f32_16x16x32_bf16 v[76:79], a[76:79], v[112:115], v[76:79]\n"
      "s_waitcnt lgkmcnt(12)\n"
      "ds_read_b64_tr_b16 a[68:69], v137 offset:24576\n"
      "ds_read_b64_tr_b16 a[70:71], v137 offset:25600\n"
      "v_mfma_f32_16x16x32_bf16 v[16:19], a[80:83], v[104:107], v[16:19]\n"
      "s_lshl_b32 s45, s46, 7\n"
      "s_add_u32 m0, s40, 32768\n"
      "s_add_u32 s46, s44, 0\n"
      "buffer_load_dwordx4 v148, %[rW], s46 offen lds\n"
      "v_mfma_f32_16x16x32_bf16 v[48:51], a[80:83], v[108:111], v[48:51]\n"
      "s_add_u32 m0, s40, 33792\n"
      "v_accvgpr_write_b32 a249, v184\n"
      "v_mfma_f32_16x16x32_bf16 v[80:83], a[80:83], v[112:115], v[80:83]\n"
      "s_waitcnt lgkmcnt(12)\n"
      "s_add_u32 s46, s44, 32768\n"
      "buffer_load_dwordx4 v148, %[rW], s46 offen lds\n"
      "v_mfma_f32_16x16x32_bf16 v[20:23], a[84:87], v[104:107], v[20:23]\n"
      "s_add_u32 m0, s40, 34816\n"
      "ds_read_b64_tr_b16 a[72:73], v138 offset:24576\n"
      "ds_read_b64_tr_b16 a[74:75], v138 offset:25600\n"
      "v_mfma_f32_16x16x32_bf16 v[52:55], a[84:87], v[108:111], v[52:55]\n"
      "s_add_u32 s46, s44, 65536\n"
      "buffer_load_dwordx4 v149, %[rW], s46 offen lds\n"
      "v_mfma_f32_16x16x32_bf16 v[84:87], a[84:87], v[112:115], v[84:87]\n"
      "s_waitcnt lgkmcnt(12)\n"
      "ds_read_b64_tr_b16 a[76:77], v139 offset:24576\n"
      "ds_read_b64_tr_b16 a[78:79], v139 offset:25600\n"
      "v_mfma_f32_16x16x32_bf16 v[24:27], a[88:91], v[104:107], v[24:27]\n"
      "s_add_u32 m0, s40, 35840\n"
      "s_add_u32 s46, s44, 98304\n"
      "buffer_load_dwordx4 v149, %[rW], s46 offen lds\n"
      "v_mfma_f32_16x16x32_bf16 v[56:59], a[88:91], v[108:111], v[56:59]\n"
      "s_lshr_b32 s58, %[hq1k], 10\n"
      "v_add_u32 v197, s58, v192\n"
      "v_and_b32 v197, 7, v197\n"
      "v_mfma_f32_16x16x32_bf16 v[88:91], a[88:91], v[112:115], v[88:91]\n"
      "s_waitcnt lgkmcnt(12)\n"
      "s_add_u32 m0, s41, 81920\n"
      "s_add_u32 s46, s45, 0\n"
      "buffer_load_dwordx4 v150, %[rX], s46 offen lds\n"
      "v_mfma_f32_16x16x32_bf16 v[28:31], a[92:95], v[104:107], v[28:31]\n"
      "s_add_u32 m0, s41, 82944\n"
      "ds_read_b64_tr_b16 a[80:81], v140 offset:24576\n"
      "ds_read_b64_tr_b16 a[82:83], v140 offset:25600\n"
      "v_mfma_f32_16x16x32_bf16 v[60:63], a[92:95], v[108:111], v[60:63]\n"
      "s_add_u32 s46, s45, 8192\n"
      "buffer_load_dwordx4 v151, %[rX], s46 offen lds\n"
      "v_lshl_add_u32 v185, v197, 12, %[cg]\n"
      "v_mfma_f32_16x16x32_bf16 v[92:95], a[92:95], v[112:115], v[92:95]\n"
      "s_waitcnt lgkmcnt(8)\n"
      "ds_read_b64_tr_b16 a[84:85], v141 offset:24576\n"
      "v_accvgpr_write_b32 a250, v185\n"
      "v_mfma_f32_16x16x32_bf16 v[0:3], a[64:67], v[120:123], v[0:3]\n"
      "s_add_u32 m0, s41, 83968\n"
      "s_add_u32 s46, s45, 16384\n"
      "buffer_load_dwordx4 v150, %[rX], s46 offen lds\n"
      "v_mfma_f32_16x16x32_bf16 v[32:35], a[64:67], v[124:127], v[32:35]\n"
      "s_and_b32 s58, s43, 1024\n"
      "ds_read_b64_tr_b16 a[86:87], v141 offset:25600\n"
      "ds_read_b64_tr_b16 a[88:89], v142 offset:24576\n"
      "v_mfma_f32_16x16x32_bf16 v[64:67], a[64:67], v[128:131], v[64:67]\n"
      "s_add_u32 s58, s58, 122880\n"
      "s_add_u32 m0, s41, 84992\n"
      "v_add_u32 v197, s58, v195\n"
      "v_mfma_f32_16x16x32_bf16 v[96:99], a[64:67], v[132:135], v[96:99]\n"
      "s_waitcnt lgkmcnt(9)\n"
      "s_add_u32 s46, s45, 24576\n"
      "buffer_load_dwordx4 v151, %[rX], s46 offen lds\n"
      "v_accvgpr_write_b32 a241, v197\n"
      "v_mfma_f32_16x16x32_bf16 v[4:7], a[68:71], v[120:123], v[4:7]\n"
      "s_add_u32 m0, s41, 86016\n"
      "s_add_u32 s46, s45, 32768\n"
      "buffer_load_dwordx4 v150, %[rX], s46 offen lds\n"
      "ds_read_b64_tr_b16 a[90:91], v142 offset:25600\n"
      "v_mfma_f32_16x16x32_bf16 v[36:39], a[68:71], v[124:127], v[36:39]\n"
      "s_mul_i32 s58, %[w], 3072\n"
      "v_add_u32 v197, s58, v195\n"
      "v_accvgpr_write_b32 a242, v197\n"
      "v_mfma_f32_16x16x32_bf16 v[68:71], a[68:71], v[128:131], v[68:71]\n"
      "s_add_u32 m0, s41, 87040\n"
      "s_add_u32 s46, s45, 40960\n"
      "buffer_load_dwordx4 v151, %[rX], s46 offen lds\n"
      "v_mfma_f32_16x16x32_bf16 v[100:103], a[68:71], v[132:135], v[100:103]\n"
      "s_waitcnt lgkmcnt(8)\n"
      "ds_read_b64_tr_b16 a[92:93], v143 offset:24576\n"
      "ds_read_b64_tr_b16 a[94:95], v143 offset:25600\n"
      "v_mfma_f32_16x16x32_bf16 v[8:11], a[72:75], v[120:123], v[8:11]\n"
      "s_mov_b32 s58, 98304\n"
      "s_add_u32 m0, s42, 106496\n"
      "v_lshl_add_u32 v197, v193, 2, s58\n"
      "v_mfma_f32_16x16x32_bf16 v[40:43], a[72:75], v[124:127], v[40:43]\n"
      "s_mov_b32 exec_hi, 0\n"
      "buffer_load_dwordx4 v251, %[rX], s45 offen lds\n"
      "s_mov_b32 exec_hi, -1\n"
      "v_accvgpr_write_b32 a243, v197\n"
      "v_mfma_f32_16x16x32_bf16 v[72:75], a[72:75], v[128:131], v[72:75]\n"
      "s_waitcnt lgkmcnt(8)\n"
      "s_and_b32 s58, %[w], 1\n"
      "s_lshl_b32 s58, s58, 7\n"
      "v_lshl_add_u32 v197, v194, 5, s58\n"
      "v_mfma_f32_16x16x32_bf16 v[12:15], a[76:79], v[120:123], v[12:15]\n"
      "v_xor_b32 v197, v197, v196\n"
      "v_lshl_add_u32 v197, v194, 10, v197\n"
      "v_mfma_f32_16x16x32_bf16 v[44:47], a[76:79], v[124:127], v[44:47]\n"
      "s_lshl_b32 s58, %[nq], 8\n"
      "v_mfma_f32_16x16x32_bf16 v[76:79], a[76:79], v[128:131], v[76:79]\n"
      "s_waitcnt lgkmcnt(6)\n"
      "v_mfma_f32_16x16x32_bf16 v[16:19], a[80:83], v[120:123], v[16:19]\n"
      "s_waitcnt vmcnt(11) lgkmcnt(0)\n"
      "s_barrier\n"
      "ds_read_b128 v[104:107], v144 offset:53248\n"
      "ds_read_b128 v[108:111], v144 offset:55296\n"
      "v_mfma_f32_16x16x32_bf16 v[48:51], a[80:83], v[124:127], v[48:51]\n"
      "s_add_u32 s58, s58, s40\n"
      "ds_read_b128 v[112:115], v144 offset:57344\n"
      "ds_read_b128 v[116:119], v146 offset:53248\n"
      "v_add_u32 v197, s58, v197\n"
      "v_mfma_f32_16x16x32_bf16 v[80:83], a[80:83], v[128:131], v[80:83]\n"
      "s_add_u32 s46, %[rot], 4\n"
      "s_sub_u32 s45, s46, 6\n"
      "s_cmp_gt_u32 s46, 7\n"
      "s_cselect_b32 s46, s45, s46\n"
      "ds_read_b64_tr_b16 a[64:65], v136 offset:32768\n"
      "ds_read_b64_tr_b16 a[66:67], v136 offset:33792\n"
      "ds_read_b64_tr_b16 a[68:69], v137 offset:32768\n"
      "v_mfma_f32_16x16x32_bf16 v[20:23], a[84:87], v[120:123], v[20:23]\n"
      "s_lshl_b32 s44, s46, 19\n"
      "ds_read_b64_tr_b16 a[70:71], v137 offset:33792\n"
      "v_accvgpr_write_b32 a244, v197\n"
      "v_mfma_f32_16x16x32_bf16 v[52:55], a[84:87], v[124:127], v[52:55]\n"
      "s_lshl_b32 s58, %[w], 1\n"
      "s_lshl_b32 s45, s46, 7\n"
      "ds_read_b64_tr_b16 a[72:73], v138 offset:32768\n"
      "v_and_b32 v197, 7, v199\n"
      "v_mfma_f32_16x16x32_bf16 v[84:87], a[84:87], v[128:131], v[84:87]\n"
      "s_add_u32 m0, s40, 49152\n"
      "s_add_u32 s46, s44, 0\n"
      "buffer_load_dwordx4 v148, %[rW], s46 offen lds\n"
      "ds_read_b64_tr_b16 a[74:75], v138 offset:33792\n"
      "v_mfma_f32_16x16x32_bf16 v[24:27], a[88:91], v[120:123], v[24:27]\n"
      "s_add_u32 m0, s40, 50176\n"
      "v_xor_b32 v197, s58, v197\n"
      "v_lshl_add_u32 v197, v197, 5, v200\n"
      "v_mfma_f32_16x16x32_bf16 v[56:59], a[88:91], v[124:127], v[56:59]\n"
      "s_add_u32 s46, s44, 32768\n"
      "ds_read_b64_tr_b16 a[76:77], v139 offset:32768\n"
      "ds_read_b64_tr_b16 a[78:79], v139 offset:33792\n"
      "v_mfma_f32_16x16x32_bf16 v[88:91], a[88:91], v[128:131], v[88:91]\n"
      "buffer_load_dwordx4 v148, %[rW], s46 offen lds\n"
      "v_lshl_add_u32 v197, v199, 8, v197\n"
      "v_mfma_f32_16x16x32_bf16 v[28:31], a[92:95], v[120:123], v[28:31]\n"
      "s_add_u32 m0, s40, 51200\n"
      "ds_read_b64_tr_b16 a[80:81], v140 offset:32768\n"
      "v_accvgpr_write_b32 a245, v197\n"
      "v_mfma_f32_16x16x32_bf16 v[60:63], a[92:95], v[124:127], v[60:63]\n"
      "s_mul_i32 s58, %[w], 106496\n"
      "ds_read_b64_tr_b16 a[82:83], v140 offset:33792\n"
      "v_add_u32 v197, s58, v195\n"
      "v_mfma_f32_16x16x32_bf16 v[92:95], a[92:95], v[128:131], v[92:95]\n"
      "s_waitcnt lgkmcnt(8)\n"
      "ds_read_b64_tr_b16 a[84:85], v141 offset:32768\n"
      "v_accvgpr_write_b32 a247, v197\n"
      "v_mfma_f32_16x16x32_bf16 v[0:3], a[64:67], v[104:107], v[0:3]\n"
      "s_add_u32 s46, s44, 65536\n"
      "buffer_load_dwordx4 v149, %[rW], s46 offen lds\n"
      "ds_read_b64_tr_b16 a[86:87], v141 offset:33792\n"
      "v_mfma_f32_16x16x32_bf16 v[32:35], a[64:67], v[108:111], v[32:35]\n"
      "s_add_u32 m0, s40, 52224\n"
      "v_and_b32 v197, 1, v194\n"
      "v_lshlrev_b32 v197, 5, v197\n"
      "v_mfma_f32_16x16x32_bf16 v[64:67], a[64:67], v[112:115], v[64:67]\n"
      "s_add_u32 s46, s44, 98304\n"
      "buffer_load_dwordx4 v149, %[rW], s46 offen lds\n"
      "ds_read_b64_tr_b16 a[88:89], v142 offset:32768\n"
      "v_mfma_f32_16x16x32_bf16 v[96:99], a[64:67], v[116:119], v[96:99]\n"
      "s_waitcnt lgkmcnt(9)\n"
      "ds_read_b64_tr_b16 a[90:91], v142 offset:33792\n"
      "v_lshrrev_b32 v198, 1, v194\n"
      "v_mfma_f32_16x16x32_bf16 v[4:7], a[68:71], v[104:107], v[4:7]\n"
      "s_add_u32 m0, s41, 108544\n"
      "ds_read_b64_tr_b16 a[92:93], v143 offset:32768\n"
      "v_lshl_add_u32 v197, v198, 4, v197\n"
      "v_mfma_f32_16x16x32_bf16 v[36:39], a[68:71], v[108:111], v[36:39]\n"
      "s_add_u32 s46, s45, 0\n"
      "buffer_load_dwordx4 v150, %[rX], s46 offen lds\n"
      "ds_read_b64_tr_b16 a[94:95], v143 offset:33792\n"
      "v_mfma_f32_16x16x32_bf16 v[68:71], a[68:71], v[112:115], v[68:71]\n"
      "s_add_u32 m0, s41, 109568\n"
      "v_lshl_add_u32 v197, v193, 10, v197\n"
      "v_accvgpr_write_b32 a248, v197\n"
      "v_mfma_f32_16x16x32_bf16 v[100:103], a[68:71], v[116:119], v[100:103]\n"
      "s_waitcnt lgkmcnt(10)\n"
      "ds_read_b128 v[120:123], v145 offset:53248\n"
      "ds_read_b128 v[124:127], v145 offset:55296\n"
      "v_mfma_f32_16x16x32_bf16 v[8:11], a[72:75], v[104:107], v[8:11]\n"
      "s_add_u32 s46, s45, 8192\n"
      "ds_read_b128 v[128:131], v145 offset:57344\n"
      "ds_read_b128 v[132:135], v147 offset:53248\n"
      "v_mfma_f32_16x16x32_bf16 v[40:43], a[72:75], v[108:111], v[40:43]\n"
      "buffer_load_dwordx4 v151, %[rX], s46 offen lds\n"
      "v_lshrrev_b32 v198, 3, v192\n"
      "v_mfma_f32_16x16x32_bf16 v[72:75], a[72:75], v[112:115], v[72:75]\n"
      "s_waitcnt lgkmcnt(12)\n"
      "ds_read_b64_tr_b16 a[64:65], v136 offset:40960\n"
      "ds_read_b64_tr_b16 a[66:67], v136 offset:41984\n"
      "v_mfma_f32_16x16x32_bf16 v[12:15], a[76:79], v[104:107], v[12:15]\n"
      "s_add_u32 m0, s41, 110592\n"
      "v_lshlrev_b32 v198, 27, v198\n"
      "v_lshl_add_u32 v197, v192, 7, %[tw]\n"
      "v_mfma_f32_16x16x32_bf16 v[44:47], a[76:79], v[108:111], v[44:47]\n"
      "s_add_u32 s46, s45, 16384\n"
      "buffer_load_dwordx4 v150, %[rX], s46 offen lds\n"
      "v_or_b32 v197, v197, v198\n"
      "v_mfma_f32_16x16x32_bf16 v[76:79], a[76:79], v[112:115], v[76:79]\n"
      "s_waitcnt lgkmcnt(12)\n"
      "ds_read_b64_tr_b16 a[68:69], v137 offset:40960\n"
      "ds_read_b64_tr_b16 a[70:71], v137 offset:41984\n"
      "v_mfma_f32_16x16x32_bf16 v[16:19], a[80:83], v[104:107], v[16:19]\n"
      "s_add_u32 m0, s41, 111616\n"
      "v_accvgpr_write_b32 a251, v197\n"
      "v_mfma_f32_16x16x32_bf16 v[48:51], a[80:83], v[108:111], v[48:51]\n"
      "s_lshr_b32 s58, %[w], 1\n"
      "v_mfma_f32_16x16x32_bf16 v[80:83], a[80:83], v[112:115], v[80:83]\n"
      "s_waitcnt lgkmcnt(12)\n"
      "ds_read_b64_tr_b16 a[72:73], v138 offset:40960\n"
      "ds_read_b64_tr_b16 a[74:75], v138 offset:41984\n"
      "v_mfma_f32_16x16x32_bf16 v[20:23], a[84:87], v[104:107], v[20:23]\n"
      "s_lshl_b32 s58, s58, 10\n"
      "v_mfma_f32_16x16x32_bf16 v[52:55], a[84:87], v[108:111], v[52:55]\n"
      "s_add_u32 s58, s58, 122880\n"
      "v_add_u32 v197, s58, v195\n"
      "v_mfma_f32_16x16x32_bf16 v[84:87], a[84:87], v[112:115], v[84:87]\n"
      "s_waitcnt lgkmcnt(12)\n"
      "s_add_u32 s46, s45, 24576\n"
      "buffer_load_dwordx4 v151, %[rX], s46 offen lds\n"
      "v_mfma_f32_16x16x32_bf16 v[24:27], a[88:91], v[104:107], v[24:27]\n"
      "s_and_b32 s58, %[w], 1\n"
      "v_lshl_add_u32 v197, s58, 3, v197\n"
      "v_accvgpr_write_b32 a253, v197\n"
      "v_mfma_f32_16x16x32_bf16 v[56:59], a[88:91], v[108:111], v[56:59]\n"
      "s_add_u32 m0, s41, 112640\n"
      "ds_read_b64_tr_b16 a[76:77], v139 offset:40960\n"
      "ds_read_b64_tr_b16 a[78:79], v139 offset:41984\n"
      "v_mfma_f32_16x16x32_bf16 v[88:91], a[88:91], v[112:115], v[88:91]\n"
      "s_waitcnt lgkmcnt(12)\n"
      "s_add_u32 s46, s45, 32768\n"
      "buffer_load_dwordx4 v150, %[rX], s46 offen lds\n"
      "v_mfma_f32_16x16x32_bf16 v[28:31], a[92:95], v[104:107], v[28:31]\n"
      "s_add_u32 m0, s41, 113664\n"
      "ds_read_b64_tr_b16 a[80:81], v140 offset:40960\n"
      "ds_read_b64_tr_b16 a[82:83], v140 offset:41984\n"
      "v_mfma_f32_16x16x32_bf16 v[60:63], a[92:95], v[108:111], v[60:63]\n"
      "s_add_u32 s46, s45, 40960\n"
      "buffer_load_dwordx4 v151, %[rX], s46 offen lds\n"
      "v_mfma_f32_16x16x32_bf16 v[92:95], a[92:95], v[112:115], v[92:95]\n"
      "s_waitcnt lgkmcnt(8)\n"
      "ds_read_b64_tr_b16 a[84:85], v141 offset:40960\n"
      "ds_read_b64_tr_b16 a[86:87], v141 offset:41984\n"
      "v_mfma_f32_16x16x32_bf16 v[0:3], a[64:67], v[120:123], v[0:3]\n"
      "s_add_u32 m0, s42, 133120\n"
      "s_mov_b32 exec_hi, 0\n"
      "buffer_load_dwordx4 v251, %[rX], s45 offen lds\n"
      "s_mov_b32 exec_hi, -1\n"
      "v_mfma_f32_16x16x32_bf16 v[32:35], a[64:67], v[124:127], v[32:35]\n"
      "ds_read_b64_tr_b16 a[88:89], v142 offset:40960\n"
      "ds_read_b64_tr_b16 a[90:91], v142 offset:41984\n"
      "v_mfma_f32_16x16x32_bf16 v[64:67], a[64:67], v[128:131], v[64:67]\n"
      "ds_read_b64_tr_b16 a[92:93], v143 offset:40960\n"
      "ds_read_b64_tr_b16 a[94:95], v143 offset:41984\n"
      "v_mfma_f32_16x16x32_bf16 v[96:99], a[64:67], v[132:135], v[96:99]\n"
      "s_waitcnt lgkmcnt(12)\n"
      "v_mfma_f32_16x16x32_bf16 v[4:7], a[68:71], v[120:123], v[4:7]\n"
      "v_mov_b32 v202, 0x3f803f80\n"
      "v_mfma_f32_16x16x32_bf16 v[36:39], a[68:71], v[124:127], v[36:39]\n"
      "v_mfma_f32_16x16x32_bf16 v[68:71], a[68:71], v[128:131], v[68:71]\n"
      "v_mfma_f32_16x16x32_bf16 v[100:103], a[68:71], v[132:135], v[100:103]\n"
      "s_waitcnt lgkmcnt(10)\n"
      "v_mfma_f32_16x16x32_bf16 v[8:11], a[72:75], v[120:123], v[8:11]\n"
      "v_mfma_f32_16x16x32_bf16 v[40:43], a[72:75], v[124:127], v[40:43]\n"
      "v_mfma_f32_16x16x32_bf16 v[72:75], a[72:75], v[128:131], v[72:75]\n"
      "s_waitcnt lgkmcnt(8)\n"
      "v_mfma_f32_16x16x32_bf16 v[12:15], a[76:79], v[120:123], v[12:15]\n"
      "v_mfma_f32_16x16x32_bf16 v[44:47], a[76:79], v[124:127], v[44:47]\n"
      "v_mfma_f32_16x16x32_bf16 v[76:79], a[76:79], v[128:131], v[76:79]\n"
      "s_waitcnt lgkmcnt(6)\n"
      "v_mfma_f32_16x16x32_bf16 v[16:19], a[80:83], v[120:123], v[16:19]\n"
      "s_waitcnt vmcnt(11) lgkmcnt(0)\n"
      "s_barrier\n"
      "ds_read_b128 v[104:107], v144 offset:0\n"
      "ds_read_b128 v[108:111], v144 offset:2048\n"
      "v_mfma_f32_16x16x32_bf16 v[48:51], a[80:83], v[124:127], v[48:51]\n"
      "s_add_u32 s46, %[rot], 5\n"
      "s_sub_u32 s45, s46, 6\n"
      "s_cmp_gt_u32 s46, 7\n"
      "s_cselect_b32 s46, s45, s46\n"
      "ds_read_b128 v[112:115], v144 offset:4096\n"
      "ds_read_b128 v[116:119], v146 offset:0\n"
      "ds_read_b64_tr_b16 a[64:65], v136 offset:0\n"
      "v_mfma_f32_16x16x32_bf16 v[80:83], a[80:83], v[128:131], v[80:83]\n"
      "s_lshl_b32 s44, s46, 19\n"
      "ds_read_b64_tr_b16 a[66:67], v136 offset:1024\n"
      "ds_read_b64_tr_b16 a[68:69], v137 offset:0\n"
      "ds_read_b64_tr_b16 a[70:71], v137 offset:1024\n"
      "v_mfma_f32_16x16x32_bf16 v[20:23], a[84:87], v[120:123], v[20:23]\n"
      "s_lshl_b32 s45, s46, 7\n"
      "ds_read_b64_tr_b16 a[72:73], v138 offset:0\n"
      "ds_read_b64_tr_b16 a[74:75], v138 offset:1024\n"
      "v_mfma_f32_16x16x32_bf16 v[52:55], a[84:87], v[124:127], v[52:55]\n"
      "s_add_u32 m0, s40, 65536\n"
      "s_add_u32 s46, s44, 0\n"
      "buffer_load_dwordx4 v148, %[rW], s46 offen lds\n"
      "v_mfma_f32_16x16x32_bf16 v[84:87], a[84:87], v[128:131], v[84:87]\n"
      "s_add_u32 m0, s40, 66560\n"
      "ds_read_b64_tr_b16 a[76:77], v139 offset:0\n"
      "ds_read_b64_tr_b16 a[78:79], v139 offset:1024\n"
      "v_mfma_f32_16x16x32_bf16 v[24:27], a[88:91], v[120:123], v[24:27]\n"
      "s_add_u32 s46, s44, 32768\n"
      "buffer_load_dwordx4 v148, %[rW], s46 offen lds\n"
      "ds_read_b64_tr_b16 a[80:81], v140 offset:0\n"
      "v_mfma_f32_16x16x32_bf16 v[56:59], a[88:91], v[124:127], v[56:59]\n"
      "s_add_u32 m0, s40, 67584\n"
      "ds_read_b64_tr_b16 a[82:83], v140 offset:1024\n"
      "v_mfma_f32_16x16x32_bf16 v[88:91], a[88:91], v[128:131], v[88:91]\n"
      "s_add_u32 s46, s44, 65536\n"
      "buffer_load_dwordx4 v149, %[rW], s46 offen lds\n"
      "v_mfma_f32_16x16x32_bf16 v[28:31], a[92:95], v[120:123], v[28:31]\n"
      "s_add_u32 m0, s40, 68608\n"
      "v_mfma_f32_16x16x32_bf16 v[60:63], a[92:95], v[124:127], v[60:63]\n"
      "s_add_u32 s46, s44, 98304\n"
      "buffer_load_dwordx4 v149, %[rW], s46 offen lds\n"
      "v_mfma_f32_16x16x32_bf16 v[92:95], a[92:95], v[128:131], v[92:95]\n"
      "s_waitcnt lgkmcnt(8)\n"
      "ds_read_b64_tr_b16 a[84:85], v141 offset:0\n"
      "ds_read_b64_tr_b16 a[86:87], v141 offset:1024\n"
      "v_mfma_f32_16x16x32_bf16 v[0:3], a[64:67], v[104:107], v[0:3]\n"
      "s_add_u32 m0, s41, 135168\n"
      "ds_read_b64_tr_b16 a[88:89], v142 offset:0\n"
      "ds_read_b64_tr_b16 a[90:91], v142 offset:1024\n"
      "v_mfma_f32_16x16x32_bf16 v[32:35], a[64:67], v[108:111], v[32:35]\n"
      "s_add_u32 s46, s45, 0\n"
      "buffer_load_dwordx4 v150, %[rX], s46 offen lds\n"
      "ds_read_b64_tr_b16 a[92:93], v143 offset:0\n"
      "v_mfma_f32_16x16x32_bf16 v[64:67], a[64:67], v[112:115], v[64:67]\n"
      "s_add_u32 m0, s41, 136192\n"
      "ds_read_b64_tr_b16 a[94:95], v143 offset:1024\n"
      "v_accvgpr_write_b32 a112, v202\n"
      "v_mfma_f32_16x16x32_bf16 v[96:99], a[64:67], v[116:119], v[96:99]\n"
      "s_waitcnt lgkmcnt(12)\n"
      "ds_read_b128 v[120:123], v145 offset:0\n"
      "ds_read_b128 v[124:127], v145 offset:2048\n"
      "v_mfma_f32_16x16x32_bf16 v[4:7], a[68:71], v[104:107], v[4:7]\n"
      "s_add_u32 s46, s45, 8192\n"
      "buffer_load_dwordx4 v151, %[rX], s46 offen lds\n"
      "v_mfma_f32_16x16x32_bf16 v[36:39], a[68:71], v[108:111], v[36:39]\n"
      "s_add_u32 m0, s41, 137216\n"
      "v_mfma_f32_16x16x32_bf16 v[68:71], a[68:71], v[112:115], v[68:71]\n"
      "s_add_u32 s46, s45, 16384\n"
      "buffer_load_dwordx4 v150, %[rX], s46 offen lds\n"
      "v_mfma_f32_16x16x32_bf16 v[100:103], a[68:71], v[116:119], v[100:103]\n"
      "s_waitcnt lgkmcnt(12)\n"
      "ds_read_b128 v[128:131], v145 offset:4096\n"
      "ds_read_b128 v[132:135], v147 offset:0\n"
      "v_mfma_f32_16x16x32_bf16 v[8:11], a[72:75], v[104:107], v[8:11]\n"
      "s_add_u32 m0, s41, 138240\n"
      "v_mfma_f32_16x16x32_bf16 v[40:43], a[72:75], v[108:111], v[40:43]\n"
      "s_add_u32 s46, s45, 24576\n"
      "buffer_load_dwordx4 v151, %[rX], s46 offen lds\n"
      "v_mfma_f32_16x16x32_bf16 v[72:75], a[72:75], v[112:115], v[72:75]\n"
      "s_waitcnt lgkmcnt(12)\n"
      "ds_read_b64_tr_b16 a[64:65], v136 offset:8192\n"
      "ds_read_b64_tr_b16 a[66:67], v136 offset:9216\n"
      "v_mfma_f32_16x16x32_bf16 v[12:15], a[76:79], v[104:107], v[12:15]\n"
      "s_add_u32 m0, s41, 139264\n"
      "v_mfma_f32_16x16x32_bf16 v[44:47], a[76:79], v[108:111], v[44:47]\n"
      "s_add_u32 s46, s45, 32768\n"
      "buffer_load_dwordx4 v150, %[rX], s46 offen lds\n"
      "v_mfma_f32_16x16x32_bf16 v[76:79], a[76:79], v[112:115], v[76:79]\n"
      "s_waitcnt lgkmcnt(12)\n"
      "ds_read_b64_tr_b16 a[68:69], v137 offset:8192\n"
      "ds_read_b64_tr_b16 a[70:71], v137 offset:9216\n"
      "v_mfma_f32_16x16x32_bf16 v[16:19], a[80:83], v[104:107], v[16:19]\n"
      "s_add_u32 m0, s41, 140288\n"
      "v_mfma_f32_16x16x32_bf16 v[48:51], a[80:83], v[108:111], v[48:51]\n"
      "s_add_u32 s46, s45, 40960\n"
      "buffer_load_dwordx4 v151, %[rX], s46 offen lds\n"
      "v_mfma_f32_16x16x32_bf16 v[80:83], a[80:83], v[112:115], v[80:83]\n"
      "s_waitcnt lgkmcnt(12)\n"
      "ds_read_b64_tr_b16 a[72:73], v138 offset:8192\n"
      "ds_read_b64_tr_b16 a[74:75], v138 offset:9216\n"
      "v_mfma_f32_16x16x32_bf16 v[20:23], a[84:87], v[104:107], v[20:23]\n"
      "v_mfma_f32_16x16x32_bf16 v[52:55], a[84:87], v[108:111], v[52:55]\n"
      "s_add_u32 m0, s42, 159744\n"
      "s_mov_b32 exec_hi, 0\n"
      "buffer_load_dwordx4 v251, %[rX], s45 offen lds\n"
      "s_mov_b32 exec_hi, -1\n"
      "v_mfma_f32_16x16x32_bf16 v[84:87], a[84:87], v[112:115], v[84:87]\n"
      "s_waitcnt lgkmcnt(12)\n"
      "ds_read_b64_tr_b16 a[76:77], v139 offset:8192\n"
      "ds_read_b64_tr_b16 a[78:79], v139 offset:9216\n"
      "v_mfma_f32_16x16x32_bf16 v[24:27], a[88:91], v[104:107], v[24:27]\n"
      "v_mfma_f32_16x16x32_bf16 v[56:59], a[88:91], v[108:111], v[56:59]\n"
      "v_mfma_f32_16x16x32_bf16 v[88:91], a[88:91], v[112:115], v[88:91]\n"
      "s_waitcnt lgkmcnt(12)\n"
      "ds_read_b64_tr_b16 a[80:81], v140 offset:8192\n"
      "ds_read_b64_tr_b16 a[82:83], v140 offset:9216\n"
      "v_mfma_f32_16x16x32_bf16 v[28:31], a[92:95], v[104:107], v[28:31]\n"
      "v_mfma_f32_16x16x32_bf16 v[60:63], a[92:95], v[108:111], v[60:63]\n"
      "s_add_u32 m0, s43, 0\n"
      "v_accvgpr_write_b32 a113, v202\n"
      "v_mfma_f32_16x16x32_bf16 v[92:95], a[92:95], v[112:115], v[92:95]\n"
      "s_waitcnt lgkmcnt(8)\n"
      "ds_read_b64_tr_b16 a[84:85], v141 offset:8192\n"
      "ds_read_b64_tr_b16 a[86:87], v141 offset:9216\n"
      "v_mfma_f32_16x16x32_bf16 v[0:3], a[64:67], v[120:123], v[0:3]\n"
      "s_mov_b32 s46, 0\n"
      "ds_read_b64_tr_b16 a[88:89], v142 offset:8192\n"
      "ds_read_b64_tr_b16 a[90:91], v142 offset:9216\n"
      "v_mfma_f32_16x16x32_bf16 v[32:35], a[64:67], v[124:127], v[32:35]\n"
      "buffer_load_dwordx4 v248, %[rK], s46 offen nt lds\n"
      "ds_read_b64_tr_b16 a[92:93], v143 offset:8192\n"
      "v_mfma_f32_16x16x32_bf16 v[64:67], a[64:67], v[128:131], v[64:67]\n"
      "s_add_u32 m0, s43, 4096\n"
      "ds_read_b64_tr_b16 a[94:95], v143 offset:9216\n"
      "v_mfma_f32_16x16x32_bf16 v[96:99], a[64:67], v[132:135], v[96:99]\n"
      "s_waitcnt lgkmcnt(12)\n"
      "v_mfma_f32_16x16x32_bf16 v[4:7], a[68:71], v[120:123], v[4:7]\n"
      "s_mov_b32 s46, 4096\n"
      "buffer_load_dwordx4 v248, %[rK], s46 offen nt lds\n"
      "v_mfma_f32_16x16x32_bf16 v[36:39], a[68:71], v[124:127], v[36:39]\n"
      "v_mfma_f32_16x16x32_bf16 v[68:71], a[68:71], v[128:131], v[68:71]\n"
      "v_mfma_f32_16x16x32_bf16 v[100:103], a[68:71], v[132:135], v[100:103]\n"
      "s_waitcnt lgkmcnt(10)\n"
      "v_mfma_f32_16x16x32_bf16 v[8:11], a[72:75], v[120:123], v[8:11]\n"
      "v_mfma_f32_16x16x32_bf16 v[40:43], a[72:75], v[124:127], v[40:43]\n"
      "v_mfma_f32_16x16x32_bf16 v[72:75], a[72:75], v[128:131], v[72:75]\n"
      "s_waitcnt lgkmcnt(8)\n"
      "v_mfma_f32_16x16x32_bf16 v[12:15], a[76:79], v[120:123], v[12:15]\n"
      "v_mfma_f32_16x16x32_bf16 v[44:47], a[76:79], v[124:127], v[44:47]\n"
      "v_mfma_f32_16x16x32_bf16 v[76:79], a[76:79], v[128:131], v[76:79]\n"
      "s_waitcnt lgkmcnt(6)\n"
      "v_mfma_f32_16x16x32_bf16 v[16:19], a[80:83], v[120:123], v[16:19]\n"
      "s_waitcnt vmcnt(13) lgkmcnt(0)\n"
      "s_barrier\n"
      "ds_read_b128 v[104:107], v144 offset:26624\n"
      "ds_read_b128 v[108:111], v144 offset:28672\n"
      "v_mfma_f32_16x16x32_bf16 v[48:51], a[80:83], v[124:127], v[48:51]\n"
      "s_add_u32 s46, %[rot], 6\n"
      "s_sub_u32 s45, s46, 6\n"
      "s_cmp_gt_u32 s46, 7\n"
      "s_cselect_b32 s46, s45, s46\n"
      "ds_read_b128 v[112:115], v144 offset:30720\n"
      "ds_read_b128 v[116:119], v146 offset:26624\n"
      "ds_read_b64_tr_b16 a[64:65], v136 offset:16384\n"
      "v_mfma_f32_16x16x32_bf16 v[80:83], a[80:83], v[128:131], v[80:83]\n"
      "s_lshl_b32 s44, s46, 19\n"
      "ds_read_b64_tr_b16 a[66:67], v136 offset:17408\n"
      "ds_read_b64_tr_b16 a[68:69], v137 offset:16384\n"
      "ds_read_b64_tr_b16 a[70:71], v137 offset:17408\n"
      "v_mfma_f32_16x16x32_bf16 v[20:23], a[84:87], v[120:123], v[20:23]\n"
      "s_lshl_b32 s45, s46, 7\n"
      "ds_read_b64_tr_b16 a[72:73], v138 offset:16384\n"
      "ds_read_b64_tr_b16 a[74:75], v138 offset:17408\n"
      "v_mfma_f32_16x16x32_bf16 v[52:55], a[84:87], v[124:127], v[52:55]\n"
      "s_add_u32 m0, s40, 32768\n"
      "s_add_u32 s46, s44, 0\n"
      "buffer_load_dwordx4 v148, %[rW], s46 offen lds\n"
      "v_mfma_f32_16x16x32_bf16 v[84:87], a[84:87], v[128:131], v[84:87]\n"
      "s_add_u32 m0, s40, 33792\n"
      "ds_read_b64_tr_b16 a[76:77], v139 offset:16384\n"
      "ds_read_b64_tr_b16 a[78:79], v139 offset:17408\n"
      "v_mfma_f32_16x16x32_bf16 v[24:27], a[88:91], v[120:123], v[24:27]\n"
      "s_add_u32 s46, s44, 32768\n"
      "buffer_load_dwordx4 v148, %[rW], s46 offen lds\n"
      "ds_read_b64_tr_b16 a[80:81], v140 offset:16384\n"
      "v_mfma_f32_16x16x32_bf16 v[56:59], a[88:91], v[124:127], v[56:59]\n"
      "s_add_u32 m0, s40, 34816\n"
      "ds_read_b64_tr_b16 a[82:83], v140 offset:17408\n"
      "v_accvgpr_write_b32 a114, v202\n"
      "v_mfma_f32_16x16x32_bf16 v[88:91], a[88:91], v[128:131], v[88:91]\n"
      "s_add_u32 s46, s44, 65536\n"
      "buffer_load_dwordx4 v149, %[rW], s46 offen lds\n"
      "v_mfma_f32_16x16x32_bf16 v[28:31], a[92:95], v[120:123], v[28:31]\n"
      "s_add_u32 m0, s40, 35840\n"
      "v_mfma_f32_16x16x32_bf16 v[60:63], a[92:95], v[124:127], v[60:63]\n"
      "s_add_u32 s46, s44, 98304\n"
      "buffer_load_dwordx4 v149, %[rW], s46 offen lds\n"
      "v_mfma_f32_16x16x32_bf16 v[92:95], a[92:95], v[128:131], v[92:95]\n"
      "s_waitcnt lgkmcnt(8)\n"
      "ds_read_b64_tr_b16 a[84:85], v141 offset:16384\n"
      "ds_read_b64_tr_b16 a[86:87], v141 offset:17408\n"
      "v_mfma_f32_16x16x32_bf16 v[0:3], a[64:67], v[104:107], v[0:3]\n"
      "s_add_u32 m0, s41, 81920\n"
      "ds_read_b64_tr_b16 a[88:89], v142 offset:16384\n"
      "ds_read_b64_tr_b16 a[90:91], v142 offset:17408\n"
      "v_mfma_f32_16x16x32_bf16 v[32:35], a[64:67], v[108:111], v[32:35]\n"
      "s_add_u32 s46, s45, 0\n"
      "buffer_load_dwordx4 v150, %[rX], s46 offen lds\n"
      "ds_read_b64_tr_b16 a[92:93], v143 offset:16384\n"
      "v_mfma_f32_16x16x32_bf16 v[64:67], a[64:67], v[112:115], v[64:67]\n"
      "s_add_u32 m0, s41, 82944\n"
      "ds_read_b64_tr_b16 a[94:95], v143 offset:17408\n"
      "v_mfma_f32_16x16x32_bf16 v[96:99], a[64:67], v[116:119], v[96:99]\n"
      "s_waitcnt lgkmcnt(12)\n"
      "ds_read_b128 v[120:123], v145 offset:26624\n"
      "ds_read_b128 v[124:127], v145 offset:28672\n"
      "v_mfma_f32_16x16x32_bf16 v[4:7], a[68:71], v[104:107], v[4:7]\n"
      "s_add_u32 s46, s45, 8192\n"
      "buffer_load_dwordx4 v151, %[rX], s46 offen lds\n"
      "v_mfma_f32_16x16x32_bf16 v[36:39], a[68:71], v[108:111], v[36:39]\n"
      "s_add_u32 m0, s41, 83968\n"
      "v_mfma_f32_16x16x32_bf16 v[68:71], a[68:71], v[112:115], v[68:71]\n"
      "s_add_u32 s46, s45, 16384\n"
      "buffer_load_dwordx4 v150, %[rX], s46 offen lds\n"
      "v_mfma_f32_16x16x32_bf16 v[100:103], a[68:71], v[116:119], v[100:103]\n"
      "s_waitcnt lgkmcnt(12)\n"
      "ds_read_b128 v[128:131], v145 offset:30720\n"
      "ds_read_b128 v[132:135], v147 offset:26624\n"
      "v_mfma_f32_16x16x32_bf16 v[8:11], a[72:75], v[104:107], v[8:11]\n"
      "s_add_u32 m0, s41, 84992\n"
      "v_mfma_f32_16x16x32_bf16 v[40:43], a[72:75], v[108:111], v[40:43]\n"
      "s_add_u32 s46, s45, 24576\n"
      "buffer_load_dwordx4 v151, %[rX], s46 offen lds\n"
      "v_mfma_f32_16x16x32_bf16 v[72:75], a[72:75], v[112:115], v[72:75]\n"
      "s_waitcnt lgkmcnt(12)\n"
      "ds_read_b64_tr_b16 a[64:65], v136 offset:24576\n"
      "ds_read_b64_tr_b16 a[66:67], v136 offset:25600\n"
      "v_mfma_f32_16x16x32_bf16 v[12:15], a[76:79], v[104:107], v[12:15]\n"
      "s_add_u32 m0, s41, 86016\n"
      "v_mfma_f32_16x16x32_bf16 v[44:47], a[76:79], v[108:111], v[44:47]\n"
      "s_add_u32 s46, s45, 32768\n"
      "buffer_load_dwordx4 v150, %[rX], s46 offen lds\n"
      "v_mfma_f32_16x16x32_bf16 v[76:79], a[76:79], v[112:115], v[76:79]\n"
      "s_waitcnt lgkmcnt(12)\n"
      "ds_read_b64_tr_b16 a[68:69], v137 offset:24576\n"
      "ds_read_b64_tr_b16 a[70:71], v137 offset:25600\n"
      "v_mfma_f32_16x16x32_bf16 v[16:19], a[80:83], v[104:107], v[16:19]\n"
      "s_add_u32 m0, s41, 87040\n"
      "v_mfma_f32_16x16x32_bf16 v[48:51], a[80:83], v[108:111], v[48:51]\n"
      "s_add_u32 s46, s45, 40960\n"
      "buffer_load_dwordx4 v151, %[rX], s46 offen lds\n"
      "v_mfma_f32_16x16x32_bf16 v[80:83], a[80:83], v[112:115], v[80:83]\n"
      "s_waitcnt lgkmcnt(12)\n"
      "ds_read_b64_tr_b16 a[72:73], v138 offset:24576\n"
      "ds_read_b64_tr_b16 a[74:75], v138 offset:25600\n"
      "v_mfma_f32_16x16x32_bf16 v[20:23], a[84:87], v[104:107], v[20:23]\n"
      "v_mfma_f32_16x16x32_bf16 v[52:55], a[84:87], v[108:111], v[52:55]\n"
      "s_add_u32 m0, s42, 106496\n"
      "s_mov_b32 exec_hi, 0\n"
      "buffer_load_dwordx4 v251, %[rX], s45 offen lds\n"
      "s_mov_b32 exec_hi, -1\n"
      "v_mfma_f32_16x16x32_bf16 v[84:87], a[84:87], v[112:115], v[84:87]\n"
      "s_waitcnt lgkmcnt(12)\n"
      "ds_read_b64_tr_b16 a[76:77], v139 offset:24576\n"
      "ds_read_b64_tr_b16 a[78:79], v139 offset:25600\n"
      "v_mfma_f32_16x16x32_bf16 v[24:27], a[88:91], v[104:107], v[24:27]\n"
      "v_accvgpr_write_b32 a115, v202\n"
      "v_mfma_f32_16x16x32_bf16 v[56:59], a[88:91], v[108:111], v[56:59]\n"
      "v_mfma_f32_16x16x32_bf16 v[88:91], a[88:91], v[112:115], v[88:91]\n"
      "s_waitcnt lgkmcnt(12)\n"
      "ds_read_b64_tr_b16 a[80:81], v140 offset:24576\n"
      "ds_read_b64_tr_b16 a[82:83], v140 offset:25600\n"
      "v_mfma_f32_16x16x32_bf16 v[28:31], a[92:95], v[104:107], v[28:31]\n"
      "v_mfma_f32_16x16x32_bf16 v[60:63], a[92:95], v[108:111], v[60:63]\n"
      "s_add_u32 m0, s43, 8192\n"
      "v_mfma_f32_16x16x32_bf16 v[92:95], a[92:95], v[112:115], v[92:95]\n"
      "s_waitcnt lgkmcnt(8)\n"
      "ds_read_b64_tr_b16 a[84:85], v141 offset:24576\n"
      "ds_read_b64_tr_b16 a[86:87], v141 offset:25600\n"
      "v_mfma_f32_16x16x32_bf16 v[0:3], a[64:67], v[120:123], v[0:3]\n"
      "s_mov_b32 s46, 8192\n"
      "ds_read_b64_tr_b16 a[88:89], v142 offset:24576\n"
      "ds_read_b64_tr_b16 a[90:91], v142 offset:25600\n"
      "v_mfma_f32_16x16x32_bf16 v[32:35], a[64:67], v[124:127], v[32:35]\n"
      "buffer_load_dwordx4 v248, %[rK], s46 offen nt lds\n"
      "ds_read_b64_tr_b16 a[92:93], v143 offset:24576\n"
      "v_mfma_f32_16x16x32_bf16 v[64:67], a[64:67], v[128:131], v[64:67]\n"
      "s_add_u32 m0, s43, 12288\n"
      "ds_read_b64_tr_b16 a[94:95], v143 offset:25600\n"
      "v_mfma_f32_16x16x32_bf16 v[96:99], a[64:67], v[132:135], v[96:99]\n"
      "s_waitcnt lgkmcnt(12)\n"
      "v_mfma_f32_16x16x32_bf16 v[4:7], a[68:71], v[120:123], v[4:7]\n"
      "s_mov_b32 s46, 12288\n"
      "buffer_load_dwordx4 v248, %[rK], s46 offen nt lds\n"
      "v_mfma_f32_16x16x32_bf16 v[36:39], a[68:71], v[124:127], v[36:39]\n"
      "v_mfma_f32_16x16x32_bf16 v[68:71], a[68:71], v[128:131], v[68:71]\n"
      "v_mfma_f32_16x16x32_bf16 v[100:103], a[68:71], v[132:135], v[100:103]\n"
      "s_waitcnt lgkmcnt(10)\n"
      "v_mfma_f32_16x16x32_bf16 v[8:11], a[72:75], v[120:123], v[8:11]\n"
      "v_mfma_f32_16x16x32_bf16 v[40:43], a[72:75], v[124:127], v[40:43]\n"
      "v_mfma_f32_16x16x32_bf16 v[72:75], a[72:75], v[128:131], v[72:75]\n"
      "s_waitcnt lgkmcnt(8)\n"
      "v_mfma_f32_16x16x32_bf16 v[12:15], a[76:79], v[120:123], v[12:15]\n"
      "v_mfma_f32_16x16x32_bf16 v[44:47], a[76:79], v[124:127], v[44:47]\n"
      "v_mfma_f32_16x16x32_bf16 v[76:79], a[76:79], v[128:131], v[76:79]\n"
      "s_waitcnt lgkmcnt(6)\n"
      "v_mfma_f32_16x16x32_bf16 v[16:19], a[80:83], v[120:123], v[16:19]\n"
      "s_waitcnt vmcnt(15) lgkmcnt(0)\n"
      "s_barrier\n"
      "ds_read_b128 v[104:107], v144 offset:53248\n"
      "ds_read_b128 v[108:111], v144 offset:55296\n"
      "v_mfma_f32_16x16x32_bf16 v[48:51], a[80:83], v[124:127], v[48:51]\n"
      "s_add_u32 s46, %[rot], 7\n"
      "s_sub_u32 s45, s46, 6\n"
      "s_cmp_gt_u32 s46, 7\n"
      "s_cselect_b32 s46, s45, s46\n"
      "ds_read_b128 v[112:115], v144 offset:57344\n"
      "ds_read_b128 v[116:119], v146 offset:53248\n"
      "ds_read_b64_tr_b16 a[64:65], v136 offset:32768\n"
      "v_mfma_f32_16x16x32_bf16 v[80:83], a[80:83], v[128:131], v[80:83]\n"
      "s_lshl_b32 s44, s46, 19\n"
      "ds_read_b64_tr_b16 a[66:67], v136 offset:33792\n"
      "ds_read_b64_tr_b16 a[68:69], v137 offset:32768\n"
      "ds_read_b64_tr_b16 a[70:71], v137 offset:33792\n"
      "v_mfma_f32_16x16x32_bf16 v[20:23], a[84:87], v[120:123], v[20:23]\n"
      "s_lshl_b32 s45, s46, 7\n"
      "ds_read_b64_tr_b16 a[72:73], v138 offset:32768\n"
      "ds_read_b64_tr_b16 a[74:75], v138 offset:33792\n"
      "v_mfma_f32_16x16x32_bf16 v[52:55], a[84:87], v[124:127], v[52:55]\n"
      "s_add_u32 m0, s40, 49152\n"
      "s_add_u32 s46, s44, 0\n"
      "buffer_load_dwordx4 v148, %[rW], s46 offen lds\n"
      "v_mfma_f32_16x16x32_bf16 v[84:87], a[84:87], v[128:131], v[84:87]\n"
      "s_add_u32 m0, s40, 50176\n"
      "ds_read_b64_tr_b16 a[76:77], v139 offset:32768\n"
      "ds_read_b64_tr_b16 a[78:79], v139 offset:33792\n"
      "v_mfma_f32_16x16x32_bf16 v[24:27], a[88:91], v[120:123], v[24:27]\n"
      "s_add_u32 s46, s44, 32768\n"
      "buffer_load_dwordx4 v148, %[rW], s46 offen lds\n"
      "ds_read_b64_tr_b16 a[80:81], v140 offset:32768\n"
      "v_mfma_f32_16x16x32_bf16 v[56:59], a[88:91], v[124:127], v[56:59]\n"
      "s_add_u32 s59, %[nq], 0\n"
      "ds_read_b64_tr_b16 a[82:83], v140 offset:33792\n"
      "v_mfma_f32_16x16x32_bf16 v[88:91], a[88:91], v[128:131], v[88:91]\n"
      "s_and_b32 s59, s59, 3\n"
      "s_lshl_b32 s48, s59, 17\n"
      "s_mul_i32 s52, s59, 53248\n"
      "v_mfma_f32_16x16x32_bf16 v[28:31], a[92:95], v[120:123], v[28:31]\n"
      "s_add_u32 s59, %[nq], 1\n"
      "s_and_b32 s59, s59, 3\n"
      "s_lshl_b32 s49, s59, 17\n"
      "v_mfma_f32_16x16x32_bf16 v[60:63], a[92:95], v[124:127], v[60:63]\n"
      "s_mul_i32 s53, s59, 53248\n"
      "s_add_u32 s59, %[nq], 2\n"
      "s_and_b32 s59, s59, 3\n"
      "v_mfma_f32_16x16x32_bf16 v[92:95], a[92:95], v[128:131], v[92:95]\n"
      "s_waitcnt lgkmcnt(8)\n"
      "s_lshl_b32 s50, s59, 17\n"
      "s_mul_i32 s54, s59, 53248\n"
      "v_mfma_f32_16x16x32_bf16 v[0:3], a[64:67], v[104:107], v[0:3]\n"
      "s_add_u32 s59, %[nq], 3\n"
      "s_and_b32 s59, s59, 3\n"
      "s_lshl_b32 s51, s59, 17\n"
      "v_mfma_f32_16x16x32_bf16 v[32:35], a[64:67], v[108:111], v[32:35]\n"
      "s_mul_i32 s55, s59, 53248\n"
      "s_add_u32 s59, %[nq], %[w]\n"
      "s_add_u32 s59, s59, 1\n"
      "ds_read_b64_tr_b16 a[84:85], v141 offset:32768\n"
      "v_mfma_f32_16x16x32_bf16 v[64:67], a[64:67], v[112:115], v[64:67]\n"
      "s_and_b32 s59, s59, 3\n"
      "s_mul_i32 s59, s59, 53248\n"
      "s_cmp_eq_u32 %[w], 3\n"
      "s_cselect_b32 s36, 2.0, s59\n"
      "ds_read_b64_tr_b16 a[86:87], v141 offset:33792\n"
      "v_mfma_f32_16x16x32_bf16 v[96:99], a[64:67], v[116:119], v[96:99]\n"
      "s_waitcnt lgkmcnt(8)\n"
      "s_add_u32 s59, s52, s43\n"
      "s_cmp_lt_u32 %[w], 2\n"
      "s_cselect_b32 s37, s59, 2.0\n"
      "v_mfma_f32_16x16x32_bf16 v[4:7], a[68:71], v[104:107], v[4:7]\n"
      "s_lshl_b32 s59, %[w], 2\n"
      "s_add_u32 s59, s59, %[nq]\n"
      "s_lshl_b64 s[56:57], 1, s59\n"
      "ds_read_b64_tr_b16 a[88:89], v142 offset:32768\n"
      "v_mfma_f32_16x16x32_bf16 v[36:39], a[68:71], v[108:111], v[36:39]\n"
      "s_mov_b32 s47, 0x3E0293EE\n"
      "s_mov_b32 vcc_lo, 0xffff0000\n"
      "s_mov_b32 vcc_hi, 0xffff0000\n"
      "v_mov_b32 v154, s47\n"
      "v_mfma_f32_16x16x32_bf16 v[68:71], a[68:71], v[112:115], v[68:71]\n"
      "s_add_u32 m0, s40, 51200\n"
      "s_add_u32 s46, s44, 65536\n"
      "buffer_load_dwordx4 v149, %[rW], s46 offen lds\n"
      "ds_read_b64_tr_b16 a[90:91], v142 offset:33792\n"
      "v_mfma_f32_16x16x32_bf16 v[100:103], a[68:71], v[116:119], v[100:103]\n"
      "s_waitcnt lgkmcnt(8)\n"
      "s_add_u32 m0, s40, 52224\n"
      "s_add_u32 s46, s44, 98304\n"
      "buffer_load_dwordx4 v149, %[rW], s46 offen lds\n"
      "v_mfma_f32_16x16x32_bf16 v[8:11], a[72:75], v[104:107], v[8:11]\n"
      "s_add_u32 m0, s41, 108544\n"
      "s_add_u32 s46, s45, 0\n"
      "buffer_load_dwordx4 v150, %[rX], s46 offen lds\n"
      "ds_read_b64_tr_b16 a[92:93], v143 offset:32768\n"
      "v_mfma_f32_16x16x32_bf16 v[40:43], a[72:75], v[108:111], v[40:43]\n"
      "s_add_u32 m0, s41, 109568\n"
      "ds_read_b64_tr_b16 a[94:95], v143 offset:33792\n"
      "s_add_u32 s46, s45, 8192\n"
      "buffer_load_dwordx4 v151, %[rX], s46 offen lds\n"
      "v_mfma_f32_16x16x32_bf16 v[72:75], a[72:75], v[112:115], v[72:75]\n"
      "s_waitcnt lgkmcnt(8)\n"
      "ds_read_b128 v[120:123], v145 offset:53248\n"
      "ds_read_b128 v[124:127], v145 offset:55296\n"
      "ds_read_b128 v[128:131], v145 offset:57344\n"
      "v_mfma_f32_16x16x32_bf16 v[12:15], a[76:79], v[104:107], v[12:15]\n"
      "s_add_u32 m0, s41, 110592\n"
      "ds_read_b128 v[132:135], v147 offset:53248\n"
      "ds_read_b64_tr_b16 a[64:65], v136 offset:40960\n"
      "ds_read_b64_tr_b16 a[66:67], v136 offset:41984\n"
      "v_mfma_f32_16x16x32_bf16 v[44:47], a[76:79], v[108:111], v[44:47]\n"
      "s_add_u32 s46, s45, 16384\n"
      "buffer_load_dwordx4 v150, %[rX], s46 offen lds\n"
      "v_mov_b32 v155, s47\n"
      "v_mfma_f32_16x16x32_bf16 v[76:79], a[76:79], v[112:115], v[76:79]\n"
      "s_waitcnt lgkmcnt(12)\n"
      "ds_read_b64_tr_b16 a[68:69], v137 offset:40960\n"
      "ds_read_b64_tr_b16 a[70:71], v137 offset:41984\n"
      "v_mfma_f32_16x16x32_bf16 v[16:19], a[80:83], v[104:107], v[16:19]\n"
      "s_add_u32 m0, s41, 111616\n"
      "v_mfma_f32_16x16x32_bf16 v[48:51], a[80:83], v[108:111], v[48:51]\n"
      "s_add_u32 s46, s45, 24576\n"
      "buffer_load_dwordx4 v151, %[rX], s46 offen lds\n"
      "v_mfma_f32_16x16x32_bf16 v[80:83], a[80:83], v[112:115], v[80:83]\n"
      "s_waitcnt lgkmcnt(12)\n"
      "ds_read_b64_tr_b16 a[72:73], v138 offset:40960\n"
      "ds_read_b64_tr_b16 a[74:75], v138 offset:41984\n"
      "v_mfma_f32_16x16x32_bf16 v[20:23], a[84:87], v[104:107], v[20:23]\n"
      "s_add_u32 m0, s41, 112640\n"
      "v_mfma_f32_16x16x32_bf16 v[52:55], a[84:87], v[108:111], v[52:55]\n"
      "s_add_u32 s46, s45, 32768\n"
      "buffer_load_dwordx4 v150, %[rX], s46 offen lds\n"
      "v_mfma_f32_16x16x32_bf16 v[84:87], a[84:87], v[112:115], v[84:87]\n"
      "s_waitcnt lgkmcnt(12)\n"
      "ds_read_b64_tr_b16 a[76:77], v139 offset:40960\n"
      "ds_read_b64_tr_b16 a[78:79], v139 offset:41984\n"
      "v_mfma_f32_16x16x32_bf16 v[24:27], a[88:91], v[104:107], v[24:27]\n"
      "s_add_u32 m0, s41, 113664\n"
      "v_mfma_f32_16x16x32_bf16 v[56:59], a[88:91], v[108:111], v[56:59]\n"
      "s_add_u32 s46, s45, 40960\n"
      "buffer_load_dwordx4 v151, %[rX], s46 offen lds\n"
      "v_mfma_f32_16x16x32_bf16 v[88:91], a[88:91], v[112:115], v[88:91]\n"
      "s_waitcnt lgkmcnt(12)\n"
      "ds_read_b64_tr_b16 a[80:81], v140 offset:40960\n"
      "ds_read_b64_tr_b16 a[82:83], v140 offset:41984\n"
      "v_mfma_f32_16x16x32_bf16 v[28:31], a[92:95], v[104:107], v[28:31]\n"
      "s_add_u32 m0, s42, 133120\n"
      "s_mov_b32 exec_hi, 0\n"
      "buffer_load_dwordx4 v251, %[rX], s45 offen lds\n"
      "s_mov_b32 exec_hi, -1\n"
      "v_mfma_f32_16x16x32_bf16 v[60:63], a[92:95], v[108:111], v[60:63]\n"
      "s_add_u32 m0, s43, 16384\n"
      "v_mfma_f32_16x16x32_bf16 v[92:95], a[92:95], v[112:115], v[92:95]\n"
      "s_waitcnt lgkmcnt(8)\n"
      "ds_read_b64_tr_b16 a[84:85], v141 offset:40960\n"
      "ds_read_b64_tr_b16 a[86:87], v141 offset:41984\n"
      "v_mfma_f32_16x16x32_bf16 v[0:3], a[64:67], v[120:123], v[0:3]\n"
      "s_mov_b32 s46, 16384\n"
      "ds_read_b64_tr_b16 a[88:89], v142 offset:40960\n"
      "ds_read_b64_tr_b16 a[90:91], v142 offset:41984\n"
      "v_mfma_f32_16x16x32_bf16 v[32:35], a[64:67], v[124:127], v[32:35]\n"
      "buffer_load_dwordx4 v248, %[rK], s46 offen nt lds\n"
      "ds_read_b64_tr_b16 a[92:93], v143 offset:40960\n"
      "v_mfma_f32_16x16x32_bf16 v[64:67], a[64:67], v[128:131], v[64:67]\n"
      "s_add_u32 m0, s43, 20480\n"
      "ds_read_b64_tr_b16 a[94:95], v143 offset:41984\n"
      "v_mfma_f32_16x16x32_bf16 v[96:99], a[64:67], v[132:135], v[96:99]\n"
      "s_waitcnt lgkmcnt(12)\n"
      "v_mfma_f32_16x16x32_bf16 v[4:7], a[68:71], v[120:123], v[4:7]\n"
      "s_mov_b32 s46, 20480\n"
      "buffer_load_dwordx4 v248, %[rK], s46 offen nt lds\n"
      "v_mfma_f32_16x16x32_bf16 v[36:39], a[68:71], v[124:127], v[36:39]\n"
      "v_mfma_f32_16x16x32_bf16 v[68:71], a[68:71], v[128:131], v[68:71]\n"
      "v_mfma_f32_16x16x32_bf16 v[100:103], a[68:71], v[132:135], v[100:103]\n"
      "s_waitcnt lgkmcnt(10)\n"
      "v_mfma_f32_16x16x32_bf16 v[8:11], a[72:75], v[120:123], v[8:11]\n"
      "v_mfma_f32_16x16x32_bf16 v[40:43], a[72:75], v[124:127], v[40:43]\n"
      "v_mfma_f32_16x16x32_bf16 v[72:75], a[72:75], v[128:131], v[72:75]\n"
      "s_waitcnt lgkmcnt(8)\n"
      "v_mfma_f32_16x16x32_bf16 v[12:15], a[76:79], v[120:123], v[12:15]\n"
      "v_mfma_f32_16x16x32_bf16 v[44:47], a[76:79], v[124:127], v[44:47]\n"
      "v_mfma_f32_16x16x32_bf16 v[76:79], a[76:79], v[128:131], v[76:79]\n"
      "s_waitcnt lgkmcnt(6)\n"
      "v_mfma_f32_16x16x32_bf16 v[16:19], a[80:83], v[120:123], v[16:19]\n"
      "s_waitcnt vmcnt(15) lgkmcnt(0)\n"
      "s_barrier\n"
      "ds_read_b128 v[104:107], v144 offset:0\n"
      "ds_read_b128 v[108:111], v144 offset:2048\n"
      "v_mfma_f32_16x16x32_bf16 v[48:51], a[80:83], v[124:127], v[48:51]\n"
      "ds_read_b128 v[112:115], v144 offset:4096\n"
      "ds_read_b128 v[116:119], v146 offset:0\n"
      "ds_read_b64_tr_b16 a[64:65], v136 offset:0\n"
      "v_mfma_f32_16x16x32_bf16 v[80:83], a[80:83], v[128:131], v[80:83]\n"
      "ds_read_b64_tr_b16 a[66:67], v136 offset:1024\n"
      "ds_read_b64_tr_b16 a[68:69], v137 offset:0\n"
      "ds_read_b64_tr_b16 a[70:71], v137 offset:1024\n"
      "v_mfma_f32_16x16x32_bf16 v[20:23], a[84:87], v[120:123], v[20:23]\n"
      "buffer_load_dword a201, v184, %[rC], 0 offen sc1\n"
      "ds_read_b64_tr_b16 a[72:73], v138 offset:0\n"
      "v_mfma_f32_16x16x32_bf16 v[52:55], a[84:87], v[124:127], v[52:55]\n"
      "ds_read_b64_tr_b16 a[74:75], v138 offset:1024\n"
      "ds_read_b64_tr_b16 a[76:77], v139 offset:0\n"
      "v_mfma_f32_16x16x32_bf16 v[84:87], a[84:87], v[128:131], v[84:87]\n"
      "ds_read_b64_tr_b16 a[78:79], v139 offset:1024\n"
      "ds_read_b64_tr_b16 a[80:81], v140 offset:0\n"
      "v_mfma_f32_16x16x32_bf16 v[24:27], a[88:91], v[120:123], v[24:27]\n"
      "ds_read_b64_tr_b16 a[82:83], v140 offset:1024\n"
      "buffer_load_dword a252, v185, %[rC], 0 offen sc1\n"
      "v_mfma_f32_16x16x32_bf16 v[56:59], a[88:91], v[124:127], v[56:59]\n"
      "v_mfma_f32_16x16x32_bf16 v[88:91], a[88:91], v[128:131], v[88:91]\n"
      "v_mfma_f32_16x16x32_bf16 v[28:31], a[92:95], v[120:123], v[28:31]\n"
      "v_mfma_f32_16x16x32_bf16 v[60:63], a[92:95], v[124:127], v[60:63]\n"
      "v_mfma_f32_16x16x32_bf16 v[92:95], a[92:95], v[128:131], v[92:95]\n"
      "s_waitcnt lgkmcnt(8)\n"
      "ds_read_b64_tr_b16 a[84:85], v141 offset:0\n"
      "ds_read_b64_tr_b16 a[86:87], v141 offset:1024\n"
      "v_mfma_f32_16x16x32_bf16 v[0:3], a[64:67], v[104:107], v[0:3]\n"
      "s_add_u32 m0, s43, 24576\n"
      "ds_read_b64_tr_b16 a[88:89], v142 offset:0\n"
      "ds_read_b64_tr_b16 a[90:91], v142 offset:1024\n"
      "v_mfma_f32_16x16x32_bf16 v[32:35], a[64:67], v[108:111], v[32:35]\n"
      "s_mov_b32 s46, 24576\n"
      "buffer_load_dwordx4 v248, %[rK], s46 offen nt lds\n"
      "ds_read_b64_tr_b16 a[92:93], v143 offset:0\n"
      "v_mfma_f32_16x16x32_bf16 v[64:67], a[64:67], v[112:115], v[64:67]\n"
      "ds_read_b64_tr_b16 a[94:95], v143 offset:1024\n"
      "v_mfma_f32_16x16x32_bf16 v[96:99], a[64:67], v[116:119], v[96:99]\n"
      "s_waitcnt lgkmcnt(12)\n"
      "ds_read_b128 v[120:123], v145 offset:0\n"
      "ds_read_b128 v[124:127], v145 offset:2048\n"
      "v_mfma_f32_16x16x32_bf16 v[4:7], a[68:71], v[104:107], v[4:7]\n"
      "v_mfma_f32_16x16x32_bf16 v[36:39], a[68:71], v[108:111], v[36:39]\n"
      "v_mfma_f32_16x16x32_bf16 v[68:71], a[68:71], v[112:115], v[68:71]\n"
      "v_mfma_f32_16x16x32_bf16 v[100:103], a[68:71], v[116:119], v[100:103]\n"
      "s_waitcnt lgkmcnt(12)\n"
      "ds_read_b128 v[128:131], v145 offset:4096\n"
      "ds_read_b128 v[132:135], v147 offset:0\n"
      "v_mfma_f32_16x16x32_bf16 v[8:11], a[72:75], v[104:107], v[8:11]\n"
      "s_add_u32 m0, s43, 28672\n"
      "v_mfma_f32_16x16x32_bf16 v[40:43], a[72:75], v[108:111], v[40:43]\n"
      "s_mov_b32 s46, 28672\n"
      "buffer_load_dwordx4 v248, %[rK], s46 offen nt lds\n"
      "v_mfma_f32_16x16x32_bf16 v[72:75], a[72:75], v[112:115], v[72:75]\n"
      "s_waitcnt lgkmcnt(12)\n"
      "ds_read_b64_tr_b16 a[64:65], v136 offset:8192\n"
      "ds_read_b64_tr_b16 a[66:67], v136 offset:9216\n"
      "v_mfma_f32_16x16x32_bf16 v[12:15], a[76:79], v[104:107], v[12:15]\n"
      "v_mfma_f32_16x16x32_bf16 v[44:47], a[76:79], v[108:111], v[44:47]\n"
      "v_mfma_f32_16x16x32_bf16 v[76:79], a[76:79], v[112:115], v[76:79]\n"
      "s_waitcnt lgkmcnt(12)\n"
      "ds_read_b64_tr_b16 a[68:69], v137 offset:8192\n"
      "ds_read_b64_tr_b16 a[70:71], v137 offset:9216\n"
      "v_mfma_f32_16x16x32_bf16 v[16:19], a[80:83], v[104:107], v[16:19]\n"
      "v_mfma_f32_16x16x32_bf16 v[48:51], a[80:83], v[108:111], v[48:51]\n"
      "s_add_u32 m0, s43, 155648\n"
      "v_mfma_f32_16x16x32_bf16 v[80:83], a[80:83], v[112:115], v[80:83]\n"
      "s_waitcnt lgkmcnt(12)\n"
      "ds_read_b64_tr_b16 a[72:73], v138 offset:8192\n"
      "ds_read_b64_tr_b16 a[74:75], v138 offset:9216\n"
      "v_mfma_f32_16x16x32_bf16 v[20:23], a[84:87], v[104:107], v[20:23]\n"
      "s_mov_b32 s46, 49152\n"
      "buffer_load_dwordx4 v249, %[rV], s46 offen nt lds\n"
      "v_mfma_f32_16x16x32_bf16 v[52:55], a[84:87], v[108:111], v[52:55]\n"
      "v_mfma_f32_16x16x32_bf16 v[84:87], a[84:87], v[112:115], v[84:87]\n"
      "s_waitcnt lgkmcnt(12)\n"
      "ds_read_b64_tr_b16 a[76:77], v139 offset:8192\n"
      "ds_read_b64_tr_b16 a[78:79], v139 offset:9216\n"
      "v_mfma_f32_16x16x32_bf16 v[24:27], a[88:91], v[104:107], v[24:27]\n"
      "v_mfma_f32_16x16x32_bf16 v[56:59], a[88:91], v[108:111], v[56:59]\n"
      "v_mfma_f32_16x16x32_bf16 v[88:91], a[88:91], v[112:115], v[88:91]\n"
      "s_waitcnt lgkmcnt(12)\n"
      "ds_read_b64_tr_b16 a[80:81], v140 offset:8192\n"
      "ds_read_b64_tr_b16 a[82:83], v140 offset:9216\n"
      "v_mfma_f32_16x16x32_bf16 v[28:31], a[92:95], v[104:107], v[28:31]\n"
      "s_add_u32 m0, s43, 159744\n"
      "v_mfma_f32_16x16x32_bf16 v[60:63], a[92:95], v[108:111], v[60:63]\n"
      "s_mov_b32 s46, 57344\n"
      "buffer_load_dwordx4 v249, %[rV], s46 offen nt lds\n"
      "v_mfma_f32_16x16x32_bf16 v[92:95], a[92:95], v[112:115], v[92:95]\n"
      "s_waitcnt lgkmcnt(8)\n"
      "ds_read_b64_tr_b16 a[84:85], v141 offset:8192\n"
      "ds_read_b64_tr_b16 a[86:87], v141 offset:9216\n"
      "v_mfma_f32_16x16x32_bf16 v[0:3], a[64:67], v[120:123], v[0:3]\n"
      "ds_read_b64_tr_b16 a[88:89], v142 offset:8192\n"
      "ds_read_b64_tr_b16 a[90:91], v142 offset:9216\n"
      "v_mfma_f32_16x16x32_bf16 v[32:35], a[64:67], v[124:127], v[32:35]\n"
      "ds_read_b64_tr_b16 a[92:93], v143 offset:8192\n"
      "ds_read_b64_tr_b16 a[94:95], v143 offset:9216\n"
      "v_mfma_f32_16x16x32_bf16 v[64:67], a[64:67], v[128:131], v[64:67]\n"
      "v_mfma_f32_16x16x32_bf16 v[96:99], a[64:67], v[132:135], v[96:99]\n"
      "s_waitcnt lgkmcnt(12)\n"
      "v_mfma_f32_16x16x32_bf16 v[4:7], a[68:71], v[120:123], v[4:7]\n"
      "v_mfma_f32_16x16x32_bf16 v[36:39], a[68:71], v[124:127], v[36:39]\n"
      "v_mfma_f32_16x16x32_bf16 v[68:71], a[68:71], v[128:131], v[68:71]\n"
      "v_mfma_f32_16x16x32_bf16 v[100:103], a[68:71], v[132:135], v[100:103]\n"
      "s_waitcnt lgkmcnt(10)\n"
      "v_mfma_f32_16x16x32_bf16 v[8:11], a[72:75], v[120:123], v[8:11]\n"
      "v_mfma_f32_16x16x32_bf16 v[40:43], a[72:75], v[124:127], v[40:43]\n"
      "v_mfma_f32_16x16x32_bf16 v[72:75], a[72:75], v[128:131], v[72:75]\n"
      "s_waitcnt lgkmcnt(8)\n"
      "v_mfma_f32_16x16x32_bf16 v[12:15], a[76:79], v[120:123], v[12:15]\n"
      "v_mfma_f32_16x16x32_bf16 v[44:47], a[76:79], v[124:127], v[44:47]\n"
      "v_mfma_f32_16x16x32_bf16 v[76:79], a[76:79], v[128:131], v[76:79]\n"
      "s_waitcnt lgkmcnt(6)\n"
      "v_mfma_f32_16x16x32_bf16 v[16:19], a[80:83], v[120:123], v[16:19]\n"
      "s_waitcnt vmcnt(8) lgkmcnt(0)\n"
      "s_barrier\n"
      "ds_read_b128 v[104:107], v144 offset:26624\n"
      "ds_read_b128 v[108:111], v144 offset:28672\n"
      "v_mfma_f32_16x16x32_bf16 v[48:51], a[80:83], v[124:127], v[48:51]\n"
      "s_add_u32 m0, s43, 65536\n"
      "ds_read_b128 v[112:115], v144 offset:30720\n"
      "ds_read_b128 v[116:119], v146 offset:26624\n"
      "ds_read_b64_tr_b16 a[64:65], v136 offset:16384\n"
      "v_mfma_f32_16x16x32_bf16 v[80:83], a[80:83], v[128:131], v[80:83]\n"
      "s_mov_b32 s46, 32768\n"
      "ds_read_b64_tr_b16 a[66:67], v136 offset:17408\n"
      "ds_read_b128 v[168:171], v145 offset:26624\n"
      "ds_read_b128 v[172:175], v145 offset:28672\n"
      "v_mfma_f32_16x16x32_bf16 v[20:23], a[84:87], v[120:123], v[20:23]\n"
      "ds_read_b128 v[176:179], v145 offset:30720\n"
      "ds_read_b128 v[180:183], v147 offset:26624\n"
      "ds_read_b64_tr_b16 a[68:69], v136 offset:24576\n"
      "ds_read_b64_tr_b16 a[70:71], v136 offset:25600\n"
      "v_mfma_f32_16x16x32_bf16 v[52:55], a[84:87], v[124:127], v[52:55]\n"
      "buffer_load_dwordx4 v248, %[rK], s46 offen nt lds\n"
      "ds_read_b64_tr_b16 a[72:73], v137 offset:16384\n"
      "ds_read_b64_tr_b16 a[74:75], v137 offset:17408\n"
      "v_mfma_f32_16x16x32_bf16 v[84:87], a[84:87], v[128:131], v[84:87]\n"
      "s_add_u32 m0, s43, 69632\n"
      "v_mfma_f32_16x16x32_bf16 v[24:27], a[88:91], v[120:123], v[24:27]\n"
      "s_mov_b32 s46, 36864\n"
      "buffer_load_dwordx4 v248, %[rK], s46 offen nt lds\n"
      "v_mfma_f32_16x16x32_bf16 v[56:59], a[88:91], v[124:127], v[56:59]\n"
      "v_mfma_f32_16x16x32_bf16 v[88:91], a[88:91], v[128:131], v[88:91]\n"
      "v_mfma_f32_16x16x32_bf16 v[28:31], a[92:95], v[120:123], v[28:31]\n"
      "s_add_u32 m0, s43, 73728\n"
      "v_mfma_f32_16x16x32_bf16 v[60:63], a[92:95], v[124:127], v[60:63]\n"
      "s_mov_b32 s46, 40960\n"
      "buffer_load_dwordx4 v248, %[rK], s46 offen nt lds\n"
      "v_mfma_f32_16x16x32_bf16 v[92:95], a[92:95], v[128:131], v[92:95]\n"
      "s_waitcnt lgkmcnt(8)\n"
      "ds_read_b64_tr_b16 a[76:77], v137 offset:24576\n"
      "ds_read_b64_tr_b16 a[78:79], v137 offset:25600\n"
      "v_mfma_f32_16x16x32_bf16 v[0:3], a[64:67], v[104:107], v[0:3]\n"
      "ds_read_b64_tr_b16 a[80:81], v138 offset:16384\n"
      "ds_read_b64_tr_b16 a[82:83], v138 offset:17408\n"
      "v_mfma_f32_16x16x32_bf16 v[32:35], a[64:67], v[108:111], v[32:35]\n"
      "ds_read_b64_tr_b16 a[84:85], v138 offset:24576\n"
      "ds_read_b64_tr_b16 a[86:87], v138 offset:25600\n"
      "v_mfma_f32_16x16x32_bf16 v[64:67], a[64:67], v[112:115], v[64:67]\n"
      "s_add_u32 m0, s43, 77824\n"
      "v_mfma_f32_16x16x32_bf16 v[96:99], a[64:67], v[116:119], v[96:99]\n"
      "s_waitcnt lgkmcnt(8)\n"
      "ds_read_b64_tr_b16 a[88:89], v139 offset:16384\n"
      "ds_read_b64_tr_b16 a[90:91], v139 offset:17408\n"
      "v_mfma_f32_16x16x32_bf16 v[0:3], a[68:71], v[168:171], v[0:3]\n"
      "s_mov_b32 s46, 45056\n"
      "buffer_load_dwordx4 v248, %[rK], s46 offen nt lds\n"
      "ds_read_b64_tr_b16 a[92:93], v139 offset:24576\n"
      "v_mfma_f32_16x16x32_bf16 v[32:35], a[68:71], v[172:175], v[32:35]\n"
      "ds_read_b64_tr_b16 a[94:95], v139 offset:25600\n"
      "ds_read_b64_tr_b16 a[64:65], v140 offset:16384\n"
      "v_mfma_f32_16x16x32_bf16 v[64:67], a[68:71], v[176:179], v[64:67]\n"
      "ds_read_b64_tr_b16 a[66:67], v140 offset:17408\n"
      "v_mfma_f32_16x16x32_bf16 v[96:99], a[68:71], v[180:183], v[96:99]\n"
      "s_waitcnt lgkmcnt(12)\n"
      "v_mfma_f32_16x16x32_bf16 v[4:7], a[72:75], v[104:107], v[4:7]\n"
      "s_add_u32 m0, s43, 81920\n"
      "ds_read_b64_tr_b16 a[68:69], v140 offset:24576\n"
      "ds_read_b64_tr_b16 a[70:71], v140 offset:25600\n"
      "v_mfma_f32_16x16x32_bf16 v[36:39], a[72:75], v[108:111], v[36:39]\n"
      "s_mov_b32 s46, 49152\n"
      "buffer_load_dwordx4 v248, %[rK], s46 offen nt lds\n"
      "v_mfma_f32_16x16x32_bf16 v[68:71], a[72:75], v[112:115], v[68:71]\n"
      "v_mfma_f32_16x16x32_bf16 v[100:103], a[72:75], v[116:119], v[100:103]\n"
      "s_waitcnt lgkmcnt(12)\n"
      "ds_read_b64_tr_b16 a[72:73], v141 offset:16384\n"
      "ds_read_b64_tr_b16 a[74:75], v141 offset:17408\n"
      "v_mfma_f32_16x16x32_bf16 v[4:7], a[76:79], v[168:171], v[4:7]\n"
      "v_mfma_f32_16x16x32_bf16 v[36:39], a[76:79], v[172:175], v[36:39]\n"
      "v_mfma_f32_16x16x32_bf16 v[68:71], a[76:79], v[176:179], v[68:71]\n"
      "s_add_u32 m0, s43, 86016\n"
      "v_mfma_f32_16x16x32_bf16 v[100:103], a[76:79], v[180:183], v[100:103]\n"
      "s_waitcnt lgkmcnt(12)\n"
      "ds_read_b64_tr_b16 a[76:77], v141 offset:24576\n"
      "ds_read_b64_tr_b16 a[78:79], v141 offset:25600\n"
      "v_mfma_f32_16x16x32_bf16 v[8:11], a[80:83], v[104:107], v[8:11]\n"
      "s_mov_b32 s46, 53248\n"
      "buffer_load_dwordx4 v248, %[rK], s46 offen nt lds\n"
      "v_mfma_f32_16x16x32_bf16 v[40:43], a[80:83], v[108:111], v[40:43]\n"
      "v_mfma_f32_16x16x32_bf16 v[72:75], a[80:83], v[112:115], v[72:75]\n"
      "s_waitcnt lgkmcnt(12)\n"
      "ds_read_b64_tr_b16 a[80:81], v142 offset:16384\n"
      "v_mul_f32 v96, s47, v96\n"
      "v_mfma_f32_16x16x32_bf16 v[8:11], a[84:87], v[168:171], v[8:11]\n"
      "s_add_u32 m0, s43, 90112\n"
      "ds_read_b64_tr_b16 a[82:83], v142 offset:17408\n"
      "v_mul_f32 v97, s47, v97\n"
      "v_mfma_f32_16x16x32_bf16 v[40:43], a[84:87], v[172:175], v[40:43]\n"
      "s_mov_b32 s46, 57344\n"
      "buffer_load_dwordx4 v248, %[rK], s46 offen nt lds\n"
      "v_mul_f32 v98, s47, v98\n"
      "v_mfma_f32_16x16x32_bf16 v[72:75], a[84:87], v[176:179], v[72:75]\n"
      "s_waitcnt lgkmcnt(12)\n"
      "ds_read_b64_tr_b16 a[84:85], v142 offset:24576\n"
      "v_mul_f32 v99, s47, v99\n"
      "v_mfma_f32_16x16x32_bf16 v[12:15], a[88:91], v[104:107], v[12:15]\n"
      "ds_read_b64_tr_b16 a[86:87], v142 offset:25600\n"
      "v_mul_f32 v100, s47, v100\n"
      "v_mfma_f32_16x16x32_bf16 v[44:47], a[88:91], v[108:111], v[44:47]\n"
      "v_mul_f32 v101, s47, v101\n"
      "v_mul_f32 v102, s47, v102\n"
      "v_mfma_f32_16x16x32_bf16 v[76:79], a[88:91], v[112:115], v[76:79]\n"
      "s_waitcnt lgkmcnt(12)\n"
      "ds_read_b64_tr_b16 a[88:89], v143 offset:16384\n"
      "ds_read_b64_tr_b16 a[90:91], v143 offset:17408\n"
      "v_mfma_f32_16x16x32_bf16 v[12:15], a[92:95], v[168:171], v[12:15]\n"
      "s_add_u32 m0, s43, 94208\n"
      "v_mul_f32 v103, s47, v103\n"
      "v_cvt_pk_bf16_f32 v96, v96, v97\n"
      "v_mfma_f32_16x16x32_bf16 v[44:47], a[92:95], v[172:175], v[44:47]\n"
      "s_mov_b32 s46, 61440\n"
      "buffer_load_dwordx4 v248, %[rK], s46 offen nt lds\n"
      "v_cvt_pk_bf16_f32 v97, v98, v99\n"
      "v_mfma_f32_16x16x32_bf16 v[76:79], a[92:95], v[176:179], v[76:79]\n"
      "s_waitcnt lgkmcnt(12)\n"
      "ds_read_b64_tr_b16 a[92:93], v143 offset:24576\n"
      "ds_read_b64_tr_b16 a[94:95], v143 offset:25600\n"
      "v_mfma_f32_16x16x32_bf16 v[16:19], a[64:67], v[104:107], v[16:19]\n"
      "v_cvt_pk_bf16_f32 v98, v100, v101\n"
      "v_cvt_pk_bf16_f32 v99, v102, v103\n"
      "v_mfma_f32_16x16x32_bf16 v[48:51], a[64:67], v[108:111], v[48:51]\n"
      "v_cndmask_b32 v100, v96, v98, vcc\n"
      "v_mfma_f32_16x16x32_bf16 v[80:83], a[64:67], v[112:115], v[80:83]\n"
      "s_waitcnt lgkmcnt(12)\n"
      "v_cndmask_b32 v101, v97, v99, vcc\n"
      "v_mfma_f32_16x16x32_bf16 v[16:19], a[68:71], v[168:171], v[16:19]\n"
      "v_cndmask_b32 v102, v98, v96, vcc\n"
      "v_mfma_f32_16x16x32_bf16 v[48:51], a[68:71], v[172:175], v[48:51]\n"
      "v_cndmask_b32 v103, v99, v97, vcc\n"
      "v_mfma_f32_16x16x32_bf16 v[80:83], a[68:71], v[176:179], v[80:83]\n"
      "s_waitcnt lgkmcnt(10)\n"
      "v_mul_f32 v0, s47, v0\n"
      "v_mfma_f32_16x16x32_bf16 v[20:23], a[72:75], v[104:107], v[20:23]\n"
      "v_mul_f32 v1, s47, v1\n"
      "v_mfma_f32_16x16x32_bf16 v[52:55], a[72:75], v[108:111], v[52:55]\n"
      "v_mul_f32 v2, s47, v2\n"
      "v_mfma_f32_16x16x32_bf16 v[84:87], a[72:75], v[112:115], v[84:87]\n"
      "s_waitcnt lgkmcnt(8)\n"
      "v_mul_f32 v3, s47, v3\n"
      "v_mfma_f32_16x16x32_bf16 v[20:23], a[76:79], v[168:171], v[20:23]\n"
      "v_mul_f32 v4, s47, v4\n"
      "v_mfma_f32_16x16x32_bf16 v[52:55], a[76:79], v[172:175], v[52:55]\n"
      "v_mul_f32 v5, s47, v5\n"
      "v_mfma_f32_16x16x32_bf16 v[84:87], a[76:79], v[176:179], v[84:87]\n"
      "s_waitcnt lgkmcnt(6)\n"
      "v_mul_f32 v6, s47, v6\n"
      "v_mfma_f32_16x16x32_bf16 v[24:27], a[80:83], v[104:107], v[24:27]\n"
      "v_mul_f32 v7, s47, v7\n"
      "v_mfma_f32_16x16x32_bf16 v[56:59], a[80:83], v[108:111], v[56:59]\n"
      "v_cvt_pk_bf16_f32 v0, v0, v1\n"
      "v_mfma_f32_16x16x32_bf16 v[88:91], a[80:83], v[112:115], v[88:91]\n"
      "s_waitcnt lgkmcnt(4)\n"
      "v_cvt_pk_bf16_f32 v1, v2, v3\n"
      "v_mfma_f32_16x16x32_bf16 v[24:27], a[84:87], v[168:171], v[24:27]\n"
      "v_cvt_pk_bf16_f32 v2, v4, v5\n"
      "v_mfma_f32_16x16x32_bf16 v[56:59], a[84:87], v[172:175], v[56:59]\n"
      "v_cvt_pk_bf16_f32 v3, v6, v7\n"
      "v_mfma_f32_16x16x32_bf16 v[88:91], a[84:87], v[176:179], v[88:91]\n"
      "s_waitcnt lgkmcnt(2)\n"
      "v_cndmask_b32 v4, v0, v2, vcc\n"
      "v_mfma_f32_16x16x32_bf16 v[28:31], a[88:91], v[104:107], v[28:31]\n"
      "v_cndmask_b32 v5, v1, v3, vcc\n"
      "v_mfma_f32_16x16x32_bf16 v[60:63], a[88:91], v[108:111], v[60:63]\n"
      "v_cndmask_b32 v6, v2, v0, vcc\n"
      "v_mfma_f32_16x16x32_bf16 v[92:95], a[88:91], v[112:115], v[92:95]\n"
      "s_waitcnt lgkmcnt(0)\n"
      "v_cndmask_b32 v7, v3, v1, vcc\n"
      "v_mfma_f32_16x16x32_bf16 v[28:31], a[92:95], v[168:171], v[28:31]\n"
      "v_accvgpr_write_b32 a0, v4\n"
      "v_mfma_f32_16x16x32_bf16 v[60:63], a[92:95], v[172:175], v[60:63]\n"
      "v_accvgpr_write_b32 a1, v5\n"
      "v_mfma_f32_16x16x32_bf16 v[92:95], a[92:95], v[176:179], v[92:95]\n"
      "v_accvgpr_write_b32 a2, v6\n"
      "v_accvgpr_write_b32 a3, v7\n"
      "v_mul_f32 v32, s47, v32\n"
      "v_mul_f32 v33, s47, v33\n"
      "v_mul_f32 v34, s47, v34\n"
      "v_mul_f32 v35, s47, v35\n"
      "v_mul_f32 v36, s47, v36\n"
      "v_mul_f32 v37, s47, v37\n"
      "v_mul_f32 v38, s47, v38\n"
      "v_mul_f32 v39, s47, v39\n"
      "v_cvt_pk_bf16_f32 v32, v32, v33\n"
      "v_cvt_pk_bf16_f32 v33, v34, v35\n"
      "v_cvt_pk_bf16_f32 v34, v36, v37\n"
      "v_cvt_pk_bf16_f32 v35, v38, v39\n"
      "v_cndmask_b32 v36, v32, v34, vcc\n"
      "v_cndmask_b32 v37, v33, v35, vcc\n"
      "v_cndmask_b32 v38, v34, v32, vcc\n"
      "v_cndmask_b32 v39, v35, v33, vcc\n"
      "v_accvgpr_write_b32 a16, v36\n"
      "v_accvgpr_write_b32 a17, v37\n"
      "v_accvgpr_write_b32 a18, v38\n"
      "v_accvgpr_write_b32 a19, v39\n"
      "s_barrier\n"
      ".p2align 3\n"
      "ds_write_b128 v156, v[100:103] offset:16384\n"
      "v_pk_mul_f32 v[64:65], v[64:65], v[154:155]\n"
      "v_pk_mul_f32 v[66:67], v[66:67], v[154:155]\n"
      "v_pk_mul_f32 v[68:69], v[68:69], v[154:155]\n"
      "v_pk_mul_f32 v[70:71], v[70:71], v[154:155]\n"
      "v_pk_mul_f32 v[8:9], v[8:9], v[154:155]\n"
      "v_cvt_pk_bf16_f32 v64, v64, v65\n"
      "v_pk_mul_f32 v[10:11], v[10:11], v[154:155]\n"
      "v_cvt_pk_bf16_f32 v65, v66, v67\n"
      "v_pk_mul_f32 v[12:13], v[12:13], v[154:155]\n"
      "v_cvt_pk_bf16_f32 v66, v68, v69\n"
      "v_pk_mul_f32 v[14:15], v[14:15], v[154:155]\n"
      "v_cvt_pk_bf16_f32 v67, v70, v71\n"
      "v_pk_mul_f32 v[40:41], v[40:41], v[154:155]\n"
      "v_cvt_pk_bf16_f32 v8, v8, v9\n"
      "v_cndmask_b32 v68, v64, v66, vcc\n"
      "v_pk_mul_f32 v[42:43], v[42:43], v[154:155]\n"
      "v_cvt_pk_bf16_f32 v9, v10, v11\n"
      "v_cndmask_b32 v69, v65, v67, vcc\n"
      "v_pk_mul_f32 v[44:45], v[44:45], v[154:155]\n"
      "v_cvt_pk_bf16_f32 v10, v12, v13\n"
      "v_cndmask_b32 v70, v66, v64, vcc\n"
      "v_pk_mul_f32 v[46:47], v[46:47], v[154:155]\n"
      "v_cvt_pk_bf16_f32 v11, v14, v15\n"
      "v_cndmask_b32 v71, v67, v65, vcc\n"
      "v_pk_mul_f32 v[72:73], v[72:73], v[154:155]\n"
      "v_cvt_pk_bf16_f32 v40, v40, v41\n"
      "v_cndmask_b32 v12, v8, v10, vcc\n"
      "v_accvgpr_write_b32 a32, v68\n"
      "v_pk_mul_f32 v[74:75], v[74:75], v[154:155]\n"
      "v_cvt_pk_bf16_f32 v41, v42, v43\n"
      "v_cndmask_b32 v13, v9, v11, vcc\n"
      "v_accvgpr_write_b32 a33, v69\n"
      "v_pk_mul_f32 v[76:77], v[76:77], v[154:155]\n"
      "v_cvt_pk_bf16_f32 v42, v44, v45\n"
      "v_cndmask_b32 v14, v10, v8, vcc\n"
      "v_accvgpr_write_b32 a34, v70\n"
      "v_pk_mul_f32 v[78:79], v[78:79], v[154:155]\n"
      "v_cvt_pk_bf16_f32 v43, v46, v47\n"
      "v_cndmask_b32 v15, v11, v9, vcc\n"
      "v_accvgpr_write_b32 a35, v71\n"
      "v_pk_mul_f32 v[16:17], v[16:17], v[154:155]\n"
      "v_cvt_pk_bf16_f32 v72, v72, v73\n"
      "v_cndmask_b32 v44, v40, v42, vcc\n"
      "v_accvgpr_write_b32 a4, v12\n"
      "v_pk_mul_f32 v[18:19], v[18:19], v[154:155]\n"
      "v_cvt_pk_bf16_f32 v73, v74, v75\n"
      "v_cndmask_b32 v45, v41, v43, vcc\n"
      "v_accvgpr_write_b32 a5, v13\n"
      "v_pk_mul_f32 v[20:21], v[20:21], v[154:155]\n"
      "v_cvt_pk_bf16_f32 v74, v76, v77\n"
      "v_cndmask_b32 v46, v42, v40, vcc\n"
      "v_accvgpr_write_b32 a6, v14\n"
      "v_pk_mul_f32 v[22:23], v[22:23], v[154:155]\n"
      "v_cvt_pk_bf16_f32 v75, v78, v79\n"
      "v_cndmask_b32 v47, v43, v41, vcc\n"
      "v_accvgpr_write_b32 a7, v15\n"
      "v_cvt_pk_bf16_f32 v16, v16, v17\n"
      "v_cndmask_b32 v76, v72, v74, vcc\n"
      "v_accvgpr_write_b32 a20, v44\n"
      "v_cvt_pk_bf16_f32 v17, v18, v19\n"
      "v_cndmask_b32 v77, v73, v75, vcc\n"
      "v_accvgpr_write_b32 a21, v45\n"
      "v_cvt_pk_bf16_f32 v18, v20, v21\n"
      "v_cndmask_b32 v78, v74, v72, vcc\n"
      "v_accvgpr_write_b32 a22, v46\n"
      "v_cvt_pk_bf16_f32 v19, v22, v23\n"
      "v_cndmask_b32 v79, v75, v73, vcc\n"
      "v_accvgpr_write_b32 a23, v47\n"
      "v_cndmask_b32 v20, v16, v18, vcc\n"
      "v_accvgpr_write_b32 a36, v76\n"
      "v_cndmask_b32 v21, v17, v19, vcc\n"
      "v_accvgpr_write_b32 a37, v77\n"
      "v_cndmask_b32 v22, v18, v16, vcc\n"
      "v_accvgpr_write_b32 a38, v78\n"
      "v_cndmask_b32 v23, v19, v17, vcc\n"
      "v_accvgpr_write_b32 a39, v79\n"
      "v_accvgpr_write_b32 a8, v20\n"
      "v_accvgpr_write_b32 a9, v21\n"
      "v_accvgpr_write_b32 a10, v22\n"
      "v_accvgpr_write_b32 a11, v23\n"
      "v_xor_b32 v157, 0, v156\n"
      "v_xor_b32 v158, 1024, v156\n"
      "v_xor_b32 v159, 2048, v156\n"
      "v_xor_b32 v160, 3072, v156\n"
      "v_xor_b32 v161, 0, v153\n"
      "v_xor_b32 v162, 64, v153\n"
      "v_xor_b32 v163, 128, v153\n"
      "v_xor_b32 v164, 192, v153\n"
      "s_waitcnt vmcnt(10) lgkmcnt(0)\n"
      "s_barrier\n"
      "ds_read_b128 a[168:171], v161 offset:0\n"
      "ds_read_b128 a[172:175], v162 offset:0\n"
      "ds_read_b128 a[176:179], v163 offset:0\n"
      "ds_read_b128 a[180:183], v164 offset:0\n"
      "ds_read_b128 a[48:51], v157 offset:16384\n"
      "ds_read_b128 a[52:55], v158 offset:16384\n"
      "ds_read_b128 a[56:59], v159 offset:16384\n"
      "ds_read_b128 a[60:63], v160 offset:16384\n"
      "v_pk_mul_f32 v[48:49], v[48:49], v[154:155]\n"
      "v_pk_mul_f32 v[50:51], v[50:51], v[154:155]\n"
      "v_pk_mul_f32 v[52:53], v[52:53], v[154:155]\n"
      "v_pk_mul_f32 v[54:55], v[54:55], v[154:155]\n"
      "v_pk_mul_f32 v[80:81], v[80:81], v[154:155]\n"
      "v_cvt_pk_bf16_f32 v48, v48, v49\n"
      "v_pk_mul_f32 v[82:83], v[82:83], v[154:155]\n"
      "v_cvt_pk_bf16_f32 v49, v50, v51\n"
      "v_pk_mul_f32 v[84:85], v[84:85], v[154:155]\n"
      "v_cvt_pk_bf16_f32 v50, v52, v53\n"
      "v_pk_mul_f32 v[86:87], v[86:87], v[154:155]\n"
      "v_cvt_pk_bf16_f32 v51, v54, v55\n"
      "v_pk_mul_f32 v[24:25], v[24:25], v[154:155]\n"
      "v_cvt_pk_bf16_f32 v80, v80, v81\n"
      "v_cndmask_b32 v52, v48, v50, vcc\n"
      "v_pk_mul_f32 v[26:27], v[26:27], v[154:155]\n"
      "v_cvt_pk_bf16_f32 v81, v82, v83\n"
      "v_cndmask_b32 v53, v49, v51, vcc\n"
      "v_pk_mul_f32 v[28:29], v[28:29], v[154:155]\n"
      "v_cvt_pk_bf16_f32 v82, v84, v85\n"
      "v_cndmask_b32 v54, v50, v48, vcc\n"
      "v_pk_mul_f32 v[30:31], v[30:31], v[154:155]\n"
      "v_cvt_pk_bf16_f32 v83, v86, v87\n"
      "v_cndmask_b32 v55, v51, v49, vcc\n"
      "v_pk_mul_f32 v[56:57], v[56:57], v[154:155]\n"
      "v_cvt_pk_bf16_f32 v24, v24, v25\n"
      "v_cndmask_b32 v84, v80, v82, vcc\n"
      "v_accvgpr_write_b32 a24, v52\n"
      "v_pk_mul_f32 v[58:59], v[58:59], v[154:155]\n"
      "v_cvt_pk_bf16_f32 v25, v26, v27\n"
      "v_cndmask_b32 v85, v81, v83, vcc\n"
      "v_accvgpr_write_b32 a25, v53\n"
      "v_pk_mul_f32 v[60:61], v[60:61], v[154:155]\n"
      "v_cvt_pk_bf16_f32 v26, v28, v29\n"
      "v_cndmask_b32 v86, v82, v80, vcc\n"
      "v_accvgpr_write_b32 a26, v54\n"
      "v_pk_mul_f32 v[62:63], v[62:63], v[154:155]\n"
      "v_cvt_pk_bf16_f32 v27, v30, v31\n"
      "v_cndmask_b32 v87, v83, v81, vcc\n"
      "v_accvgpr_write_b32 a27, v55\n"
      "v_pk_mul_f32 v[88:89], v[88:89], v[154:155]\n"
      "v_cvt_pk_bf16_f32 v56, v56, v57\n"
      "v_cndmask_b32 v28, v24, v26, vcc\n"
      "v_accvgpr_write_b32 a40, v84\n"
      "v_pk_mul_f32 v[90:91], v[90:91], v[154:155]\n"
      "v_cvt_pk_bf16_f32 v57, v58, v59\n"
      "v_cndmask_b32 v29, v25, v27, vcc\n"
      "v_accvgpr_write_b32 a41, v85\n"
      "v_pk_mul_f32 v[92:93], v[92:93], v[154:155]\n"
      "v_cvt_pk_bf16_f32 v58, v60, v61\n"
      "v_cndmask_b32 v30, v26, v24, vcc\n"
      "v_accvgpr_write_b32 a42, v86\n"
      "v_pk_mul_f32 v[94:95], v[94:95], v[154:155]\n"
      "v_cvt_pk_bf16_f32 v59, v62, v63\n"
      "v_cndmask_b32 v31, v27, v25, vcc\n"
      "v_accvgpr_write_b32 a43, v87\n"
      "v_cvt_pk_bf16_f32 v88, v88, v89\n"
      "v_cndmask_b32 v60, v56, v58, vcc\n"
      "v_accvgpr_write_b32 a12, v28\n"
      "v_cvt_pk_bf16_f32 v89, v90, v91\n"
      "v_cndmask_b32 v61, v57, v59, vcc\n"
      "v_accvgpr_write_b32 a13, v29\n"
      "v_cvt_pk_bf16_f32 v90, v92, v93\n"
      "v_cndmask_b32 v62, v58, v56, vcc\n"
      "v_accvgpr_write_b32 a14, v30\n"
      "v_cvt_pk_bf16_f32 v91, v94, v95\n"
      "v_cndmask_b32 v63, v59, v57, vcc\n"
      "v_accvgpr_write_b32 a15, v31\n"
      "v_cndmask_b32 v92, v88, v90, vcc\n"
      "v_accvgpr_write_b32 a28, v60\n"
      "v_cndmask_b32 v93, v89, v91, vcc\n"
      "v_accvgpr_write_b32 a29, v61\n"
      "v_cndmask_b32 v94, v90, v88, vcc\n"
      "v_accvgpr_write_b32 a30, v62\n"
      "v_cndmask_b32 v95, v91, v89, vcc\n"
      "v_accvgpr_write_b32 a31, v63\n"
      "v_accvgpr_write_b32 a44, v92\n"
      "v_accvgpr_write_b32 a45, v93\n"
      "v_accvgpr_write_b32 a46, v94\n"
      "v_accvgpr_write_b32 a47, v95\n"
      "v_mov_b32 v251, v156\n"
      "s_mov_b32 s42, 0\n"
      "v_mov_b32 v32, 0x7f800000\n"
      "v_mov_b32 v33, 0x7f800000\n"
      "v_mov_b32 v34, 0x7f800000\n"
      "v_mov_b32 v35, 0x7f800000\n"
      "s_nop 1\n"
      ".Lmha_S:\n"
      "s_waitcnt lgkmcnt(3)\n"
      "v_mfma_f32_16x16x32_bf16 v[16:19], a[168:171], a[48:51], 0\n"
      "s_waitcnt lgkmcnt(2)\n"
      "v_mfma_f32_16x16x32_bf16 v[20:23], a[172:175], a[52:55], 0\n"
      "s_waitcnt lgkmcnt(1)\n"
      "v_mfma_f32_16x16x32_bf16 v[24:27], a[176:179], a[56:59], 0\n"
      "s_waitcnt lgkmcnt(0)\n"
      "v_mfma_f32_16x16x32_bf16 v[28:31], a[180:183], a[60:63], 0\n"
      "v_mfma_f32_16x16x32_bf16 v[0:3], a[168:171], a[0:3], 0\n"
      "s_add_u32 m0, s43, 131072\n"
      "ds_read_b128 a[64:67], v240 offset:0\n"
      "ds_read_b128 a[68:71], v241 offset:0\n"
      "v_mfma_f32_16x16x32_bf16 v[0:3], a[172:175], a[4:7], v[0:3]\n"
      "s_mov_b32 s46, 0\n"
      "ds_read_b128 a[72:75], v242 offset:0\n"
      "buffer_load_dwordx4 v249, %[rV], s46 offen nt lds\n"
      "v_mfma_f32_16x16x32_bf16 v[0:3], a[176:179], a[8:11], v[0:3]\n"
      "v_add_f32 v16, v16, v20\n"
      "v_add_f32 v24, v24, v28\n"
      "v_mfma_f32_16x16x32_bf16 v[0:3], a[180:183], a[12:15], v[0:3]\n"
      "v_add_f32 v12, v16, v24\n"
      "v_min_f32 v236, -v12, v35\n"
      "v_min_f32 v237, -v12, v35\n"
      "v_mfma_f32_16x16x32_bf16 v[4:7], a[168:171], a[16:19], 0\n"
      "v_min_f32 v238, -v12, v35\n"
      "v_min_f32 v239, -v12, v35\n"
      "v_mfma_f32_16x16x32_bf16 v[4:7], a[172:175], a[20:23], v[4:7]\n"
      "ds_read_b128 a[76:79], v243 offset:0\n"
      "ds_read_b128 a[80:83], v240 offset:4096\n"
      "v_mfma_f32_16x16x32_bf16 v[4:7], a[176:179], a[24:27], v[4:7]\n"
      "v_min_f32 v224, -v0, v32\n"
      "v_min_f32 v225, -v0, v32\n"
      "v_mfma_f32_16x16x32_bf16 v[4:7], a[180:183], a[28:31], v[4:7]\n"
      "v_min_f32 v226, -v0, v32\n"
      "v_min_f32 v227, -v0, v32\n"
      "v_mfma_f32_16x16x32_bf16 v[8:11], a[168:171], a[32:35], 0\n"
      "s_add_u32 m0, s43, 135168\n"
      "ds_read_b128 a[84:87], v241 offset:4096\n"
      "ds_read_b128 a[88:91], v242 offset:4096\n"
      "v_mfma_f32_16x16x32_bf16 v[8:11], a[172:175], a[36:39], v[8:11]\n"
      "s_mov_b32 s46, 8192\n"
      "buffer_load_dwordx4 v249, %[rV], s46 offen nt lds\n"
      "v_mfma_f32_16x16x32_bf16 v[8:11], a[176:179], a[40:43], v[8:11]\n"
      "v_min_f32 v228, -v4, v33\n"
      "v_min_f32 v229, -v4, v33\n"
      "v_mfma_f32_16x16x32_bf16 v[8:11], a[180:183], a[44:47], v[8:11]\n"
      "s_waitcnt lgkmcnt(6)\n"
      "v_min_f32 v230, -v4, v33\n"
      "v_min_f32 v231, -v4, v33\n"
      "s_nop 4\n"
      "v_min_f32 v232, -v8, v34\n"
      "v_min_f32 v233, -v8, v34\n"
      "v_min_f32 v234, -v8, v34\n"
      "v_min_f32 v235, -v8, v34\n"
      "v_mfma_f32_16x16x32_bf16 v[168:171], a[64:67], a[0:3], v[224:227]\n"
      "ds_read_b128 a[92:95], v243 offset:4096\n"
      "v_mfma_f32_16x16x32_bf16 v[176:179], a[64:67], a[16:19], v[228:231]\n"
      "v_mfma_f32_16x16x32_bf16 v[184:187], a[64:67], a[32:35], v[232:235]\n"
      "s_waitcnt lgkmcnt(6)\n"
      "ds_read_b128 a[64:67], v240 offset:8192\n"
      "ds_read_b128 a[168:171], v244 offset:0\n"
      "v_mfma_f32_16x16x32_bf16 v[168:171], a[68:71], a[4:7], v[168:171]\n"
      "v_mfma_f32_16x16x32_bf16 v[176:179], a[68:71], a[20:23], v[176:179]\n"
      "v_mfma_f32_16x16x32_bf16 v[184:187], a[68:71], a[36:39], v[184:187]\n"
      "s_waitcnt lgkmcnt(7)\n"
      "ds_read_b128 a[68:71], v241 offset:8192\n"
      "ds_read_b128 a[172:175], v245 offset:0\n"
      "v_mfma_f32_16x16x32_bf16 v[168:171], a[72:75], a[8:11], v[168:171]\n"
      "s_add_u32 m0, s43, 139264\n"
      "v_mfma_f32_16x16x32_bf16 v[176:179], a[72:75], a[24:27], v[176:179]\n"
      "s_mov_b32 s46, 16384\n"
      "buffer_load_dwordx4 v249, %[rV], s46 offen nt lds\n"
      "v_mfma_f32_16x16x32_bf16 v[184:187], a[72:75], a[40:43], v[184:187]\n"
      "s_waitcnt lgkmcnt(8)\n"
      "v_accvgpr_read_b32 v152, a251\n"
      "v_mfma_f32_16x16x32_bf16 v[168:171], a[76:79], a[12:15], v[168:171]\n"
      "ds_read_b128 a[72:75], v242 offset:8192\n"
      "ds_read_b128 a[176:179], v246 offset:0\n"
      "v_mfma_f32_16x16x32_bf16 v[176:179], a[76:79], a[28:31], v[176:179]\n"
      "v_mfma_f32_16x16x32_bf16 v[184:187], a[76:79], a[44:47], v[184:187]\n"
      "s_waitcnt lgkmcnt(9)\n"
      "v_mfma_f32_16x16x32_bf16 v[172:175], a[80:83], a[0:3], v[224:227]\n"
      "v_mfma_f32_16x16x32_bf16 v[180:183], a[80:83], a[16:19], v[228:231]\n"
      "s_nop 0\n"
      "v_exp_f32 v168, v168\n"
      "v_mfma_f32_16x16x32_bf16 v[188:191], a[80:83], a[32:35], v[232:235]\n"
      "s_waitcnt lgkmcnt(8)\n"
      "v_exp_f32 v169, v169\n"
      "v_mfma_f32_16x16x32_bf16 v[172:175], a[84:87], a[4:7], v[172:175]\n"
      "s_add_u32 m0, s43, 143360\n"
      "v_exp_f32 v170, v170\n"
      "v_mfma_f32_16x16x32_bf16 v[180:183], a[84:87], a[20:23], v[180:183]\n"
      "s_mov_b32 s46, 24576\n"
      "ds_read_b128 a[76:79], v243 offset:8192\n"
      "v_cvt_pk_bf16_f32 v0, v168, v169\n"
      "v_mfma_f32_16x16x32_bf16 v[188:191], a[84:87], a[36:39], v[188:191]\n"
      "s_waitcnt lgkmcnt(8)\n"
      "v_exp_f32 v171, v171\n"
      "v_mfma_f32_16x16x32_bf16 v[172:175], a[88:91], a[8:11], v[172:175]\n"
      "v_exp_f32 v176, v176\n"
      "v_mfma_f32_16x16x32_bf16 v[180:183], a[88:91], a[24:27], v[180:183]\n"
      "ds_read_b128 a[180:183], v247 offset:0\n"
      "v_cvt_pk_bf16_f32 v1, v170, v171\n"
      "v_mfma_f32_16x16x32_bf16 v[188:191], a[88:91], a[40:43], v[188:191]\n"
      "s_waitcnt lgkmcnt(8)\n"
      "v_exp_f32 v177, v177\n"
      "v_mfma_f32_16x16x32_bf16 v[172:175], a[92:95], a[12:15], v[172:175]\n"
      "v_exp_f32 v178, v178\n"
      "v_cvt_pk_bf16_f32 v4, v176, v177\n"
      "v_mfma_f32_16x16x32_bf16 v[180:183], a[92:95], a[28:31], v[180:183]\n"
      "buffer_load_dwordx4 v249, %[rV], s46 offen nt lds\n"
      "v_exp_f32 v179, v179\n"
      "v_mfma_f32_16x16x32_bf16 v[188:191], a[92:95], a[44:47], v[188:191]\n"
      "s_waitcnt lgkmcnt(7)\n"
      "v_exp_f32 v184, v184\n"
      "v_cvt_pk_bf16_f32 v5, v178, v179\n"
      "v_mfma_f32_16x16x32_bf16 v[192:195], a[64:67], a[0:3], v[224:227]\n"
      "ds_read_b128 a[80:83], v240 offset:12288\n"
      "v_exp_f32 v185, v185\n"
      "v_mfma_f32_16x16x32_bf16 v[200:203], a[64:67], a[16:19], v[228:231]\n"
      "s_add_u32 m0, s43, 147456\n"
      "ds_read_b128 a[184:187], v244 offset:4096\n"
      "v_exp_f32 v186, v186\n"
      "v_mfma_f32_16x16x32_bf16 v[208:211], a[64:67], a[32:35], v[232:235]\n"
      "s_waitcnt lgkmcnt(8)\n"
      "ds_read_b128 a[84:87], v241 offset:12288\n"
      "v_cvt_pk_bf16_f32 v8, v184, v185\n"
      "v_mfma_f32_16x16x32_bf16 v[216:219], a[168:171], a[48:51], v[236:239]\n"
      "s_waitcnt lgkmcnt(8)\n"
      "v_exp_f32 v187, v187\n"
      "s_nop 0\n"
      "v_cvt_pk_bf16_f32 v9, v186, v187\n"
      "v_mfma_f32_16x16x32_bf16 v[192:195], a[68:71], a[4:7], v[192:195]\n"
      "s_mov_b32 s46, 32768\n"
      "ds_read_b128 a[188:191], v245 offset:4096\n"
      "v_exp_f32 v172, v172\n"
      "v_mfma_f32_16x16x32_bf16 v[200:203], a[68:71], a[20:23], v[200:203]\n"
      "v_exp_f32 v173, v173\n"
      "v_mfma_f32_16x16x32_bf16 v[208:211], a[68:71], a[36:39], v[208:211]\n"
      "s_waitcnt lgkmcnt(8)\n"
      "v_exp_f32 v174, v174\n"
      "v_cvt_pk_bf16_f32 v2, v172, v173\n"
      "v_mfma_f32_16x16x32_bf16 v[216:219], a[172:175], a[52:55], v[216:219]\n"
      "s_waitcnt lgkmcnt(7)\n"
      "v_exp_f32 v175, v175\n"
      "v_mfma_f32_16x16x32_bf16 v[192:195], a[72:75], a[8:11], v[192:195]\n"
      "buffer_load_dwordx4 v249, %[rV], s46 offen nt lds\n"
      "v_exp_f32 v180, v180\n"
      "v_mfma_f32_16x16x32_bf16 v[200:203], a[72:75], a[24:27], v[200:203]\n"
      "ds_read_b128 a[88:91], v242 offset:12288\n"
      "ds_read_b128 a[192:195], v246 offset:4096\n"
      "v_cvt_pk_bf16_f32 v3, v174, v175\n"
      "v_mfma_f32_16x16x32_bf16 v[208:211], a[72:75], a[40:43], v[208:211]\n"
      "s_waitcnt lgkmcnt(8)\n"
      "v_exp_f32 v181, v181\n"
      "v_mfma_f32_16x16x32_bf16 v[216:219], a[176:179], a[56:59], v[216:219]\n"
      "s_waitcnt lgkmcnt(7)\n"
      "v_exp_f32 v182, v182\n"
      "v_cvt_pk_bf16_f32 v6, v180, v181\n"
      "v_mfma_f32_16x16x32_bf16 v[192:195], a[76:79], a[12:15], v[192:195]\n"
      "s_add_u32 m0, s43, 151552\n"
      "ds_read_b128 a[92:95], v243 offset:12288\n"
      "v_exp_f32 v183, v183\n"
      "v_mfma_f32_16x16x32_bf16 v[200:203], a[76:79], a[28:31], v[200:203]\n"
      "s_mov_b32 s46, 40960\n"
      "ds_read_b128 a[196:199], v247 offset:4096\n"
      "ds_read_b128 a[64:67], v240 offset:16384\n"
      "v_mfma_f32_16x16x32_bf16 v[208:211], a[76:79], a[44:47], v[208:211]\n"
      "s_waitcnt lgkmcnt(9)\n"
      "v_exp_f32 v188, v188\n"
      "v_cvt_pk_bf16_f32 v7, v182, v183\n"
      "v_mfma_f32_16x16x32_bf16 v[216:219], a[180:183], a[60:63], v[216:219]\n"
      "s_waitcnt lgkmcnt(8)\n"
      "v_exp_f32 v189, v189\n"
      "v_mfma_f32_16x16x32_bf16 v[196:199], a[80:83], a[0:3], v[224:227]\n"
      "v_exp_f32 v190, v190\n"
      "v_cvt_pk_bf16_f32 v10, v188, v189\n"
      "v_mfma_f32_16x16x32_bf16 v[204:207], a[80:83], a[16:19], v[228:231]\n"
      "buffer_load_dwordx4 v249, %[rV], s46 offen nt lds\n"
      "v_exp_f32 v191, v191\n"
      "v_mfma_f32_16x16x32_bf16 v[212:215], a[80:83], a[32:35], v[232:235]\n"
      "s_waitcnt lgkmcnt(7)\n"
      "ds_read_b128 a[68:71], v241 offset:16384\n"
      "v_cvt_pk_bf16_f32 v11, v190, v191\n"
      "v_mfma_f32_16x16x32_bf16 v[220:223], a[184:187], a[48:51], v[236:239]\n"
      "s_waitcnt lgkmcnt(7)\n"
      "v_exp_f32 v192, v192\n"
      "v_mfma_f32_16x16x32_bf16 v[196:199], a[84:87], a[4:7], v[196:199]\n"
      "s_add_u32 m0, s43, 32768\n"
      "v_exp_f32 v193, v193\n"
      "v_mfma_f32_16x16x32_bf16 v[204:207], a[84:87], a[20:23], v[204:207]\n"
      "s_mov_b32 s46, 65536\n"
      "v_exp_f32 v194, v194\n"
      "v_cvt_pk_bf16_f32 v12, v192, v193\n"
      "v_mfma_f32_16x16x32_bf16 v[212:215], a[84:87], a[36:39], v[212:215]\n"
      "s_waitcnt lgkmcnt(6)\n"
      "v_exp_f32 v195, v195\n"
      "v_mfma_f32_16x16x32_bf16 v[220:223], a[188:191], a[52:55], v[220:223]\n"
      "s_waitcnt lgkmcnt(5)\n"
      "v_exp_f32 v200, v200\n"
      "v_cvt_pk_bf16_f32 v13, v194, v195\n"
      "v_mfma_f32_16x16x32_bf16 v[196:199], a[88:91], a[8:11], v[196:199]\n"
      "s_mov_b32 s59, 0\n"
      "ds_read_b128 a[72:75], v242 offset:16384\n"
      "v_exp_f32 v201, v201\n"
      "v_mfma_f32_16x16x32_bf16 v[204:207], a[88:91], a[24:27], v[204:207]\n"
      "v_exp_f32 v202, v202\n"
      "v_cvt_pk_bf16_f32 v16, v200, v201\n"
      "v_mfma_f32_16x16x32_bf16 v[212:215], a[88:91], a[40:43], v[212:215]\n"
      "s_waitcnt lgkmcnt(5)\n"
      "ds_read_b128 a[76:79], v243 offset:16384\n"
      "v_exp_f32 v203, v203\n"
      "v_mfma_f32_16x16x32_bf16 v[220:223], a[192:195], a[56:59], v[220:223]\n"
      "s_waitcnt lgkmcnt(5)\n"
      "v_exp_f32 v208, v208\n"
      "v_cvt_pk_bf16_f32 v17, v202, v203\n"
      "v_mfma_f32_16x16x32_bf16 v[196:199], a[92:95], a[12:15], v[196:199]\n"
      "buffer_load_dwordx4 v248, %[rK], s46 offen nt lds\n"
      "buffer_load_dword a200, v152, %[rWo], s59 offen\n"
      "v_mfma_f32_16x16x32_bf16 v[204:207], a[92:95], a[28:31], v[204:207]\n"
      "v_exp_f32 v209, v209\n"
      "v_mfma_f32_16x16x32_bf16 v[212:215], a[92:95], a[44:47], v[212:215]\n"
      "s_waitcnt lgkmcnt(4)\n"
      "v_exp_f32 v210, v210\n"
      "v_cvt_pk_bf16_f32 v20, v208, v209\n"
      "v_mfma_f32_16x16x32_bf16 v[220:223], a[196:199], a[60:63], v[220:223]\n"
      "s_waitcnt lgkmcnt(3)\n"
      "ds_read_b128 a[80:83], v240 offset:20480\n"
      "v_exp_f32 v211, v211\n"
      "v_mfma_f32_16x16x32_bf16 v[168:171], a[64:67], a[0:3], v[224:227]\n"
      "s_add_u32 m0, s43, 36864\n"
      "v_exp_f32 v216, v216\n"
      "v_cvt_pk_bf16_f32 v21, v210, v211\n"
      "v_mfma_f32_16x16x32_bf16 v[176:179], a[64:67], a[16:19], v[228:231]\n"
      "s_mov_b32 s46, 69632\n"
      "ds_read_b128 a[84:87], v241 offset:20480\n"
      "v_exp_f32 v217, v217\n"
      "v_mfma_f32_16x16x32_bf16 v[184:187], a[64:67], a[32:35], v[232:235]\n"
      "s_waitcnt lgkmcnt(4)\n"
      "v_exp_f32 v218, v218\n"
      "v_cvt_pk_bf16_f32 v216, v216, v217\n"
      "v_mfma_f32_16x16x32_bf16 v[168:171], a[68:71], a[4:7], v[168:171]\n"
      "v_exp_f32 v219, v219\n"
      "s_nop 0\n"
      "v_cvt_pk_bf16_f32 v217, v218, v219\n"
      "v_mfma_f32_16x16x32_bf16 v[176:179], a[68:71], a[20:23], v[176:179]\n"
      "v_exp_f32 v196, v196\n"
      "v_mfma_f32_16x16x32_bf16 v[184:187], a[68:71], a[36:39], v[184:187]\n"
      "buffer_load_dwordx4 v248, %[rK], s46 offen nt lds\n"
      "v_exp_f32 v197, v197\n"
      "v_mfma_f32_16x16x32_bf16 a[96:99], a[112:115], v[0:3], 0\n"
      "s_waitcnt lgkmcnt(3)\n"
      "v_exp_f32 v198, v198\n"
      "v_cvt_pk_bf16_f32 v14, v196, v197\n"
      "v_mfma_f32_16x16x32_bf16 v[168:171], a[72:75], a[8:11], v[168:171]\n"
      "s_add_u32 m0, s43, 40960\n"
      "ds_read_b128 a[88:91], v242 offset:20480\n"
      "v_exp_f32 v199, v199\n"
      "v_mfma_f32_16x16x32_bf16 v[176:179], a[72:75], a[24:27], v[176:179]\n"
      "s_mov_b32 s46, 73728\n"
      "v_exp_f32 v204, v204\n"
      "v_cvt_pk_bf16_f32 v15, v198, v199\n"
      "v_mfma_f32_16x16x32_bf16 v[184:187], a[72:75], a[40:43], v[184:187]\n"
      "s_waitcnt lgkmcnt(3)\n"
      "v_exp_f32 v205, v205\n"
      "v_exp_f32 v206, v206\n"
      "v_mfma_f32_16x16x32_bf16 v[168:171], a[76:79], a[12:15], v[168:171]\n"
      "ds_read_b128 a[92:95], v243 offset:20480\n"
      "v_cvt_pk_bf16_f32 v18, v204, v205\n"
      "v_exp_f32 v207, v207\n"
      "v_mfma_f32_16x16x32_bf16 v[176:179], a[76:79], a[28:31], v[176:179]\n"
      "v_exp_f32 v212, v212\n"
      "v_cvt_pk_bf16_f32 v19, v206, v207\n"
      "v_mfma_f32_16x16x32_bf16 v[184:187], a[76:79], a[44:47], v[184:187]\n"
      "ds_read_b128 a[64:67], v240 offset:24576\n"
      "v_exp_f32 v213, v213\n"
      "v_mfma_f32_16x16x32_bf16 a[100:103], a[112:115], v[4:7], 0\n"
      "s_waitcnt lgkmcnt(4)\n"
      "v_exp_f32 v214, v214\n"
      "v_cvt_pk_bf16_f32 v22, v212, v213\n"
      "v_mfma_f32_16x16x32_bf16 v[172:175], a[80:83], a[0:3], v[224:227]\n"
      "v_exp_f32 v215, v215\n"
      "v_exp_f32 v220, v220\n"
      "v_mfma_f32_16x16x32_bf16 v[180:183], a[80:83], a[16:19], v[228:231]\n"
      "buffer_load_dwordx4 v248, %[rK], s46 offen nt lds\n"
      "v_cvt_pk_bf16_f32 v23, v214, v215\n"
      "v_exp_f32 v221, v221\n"
      "v_mfma_f32_16x16x32_bf16 v[188:191], a[80:83], a[32:35], v[232:235]\n"
      "s_waitcnt lgkmcnt(3)\n"
      "ds_read_b128 a[68:71], v241 offset:24576\n"
      "v_exp_f32 v222, v222\n"
      "v_mfma_f32_16x16x32_bf16 v[172:175], a[84:87], a[4:7], v[172:175]\n"
      "s_add_u32 m0, s43, 45056\n"
      "v_cvt_pk_bf16_f32 v218, v220, v221\n"
      "v_exp_f32 v223, v223\n"
      "s_nop 0\n"
      "v_cvt_pk_bf16_f32 v219, v222, v223\n"
      "v_mfma_f32_16x16x32_bf16 v[180:183], a[84:87], a[20:23], v[180:183]\n"
      "s_mov_b32 s46, 77824\n"
      "ds_write_b128 v251, v[216:219] offset:0\n"
      "ds_read_b128 a[72:75], v242 offset:24576\n"
      "v_exp_f32 v168, v168\n"
      "v_mfma_f32_16x16x32_bf16 v[188:191], a[84:87], a[36:39], v[188:191]\n"
      "v_exp_f32 v169, v169\n"
      "v_mfma_f32_16x16x32_bf16 a[104:107], a[112:115], v[8:11], 0\n"
      "s_waitcnt lgkmcnt(5)\n"
      "v_exp_f32 v170, v170\n"
      "v_cvt_pk_bf16_f32 v24, v168, v169\n"
      "v_mfma_f32_16x16x32_bf16 v[172:175], a[88:91], a[8:11], v[172:175]\n"
      "ds_read_b128 a[76:79], v243 offset:24576\n"
      "v_exp_f32 v171, v171\n"
      "v_mfma_f32_16x16x32_bf16 v[180:183], a[88:91], a[24:27], v[180:183]\n"
      "v_exp_f32 v176, v176\n"
      "v_cvt_pk_bf16_f32 v25, v170, v171\n"
      "v_mfma_f32_16x16x32_bf16 v[188:191], a[88:91], a[40:43], v[188:191]\n"
      "s_waitcnt lgkmcnt(5)\n"
      "v_exp_f32 v177, v177\n"
      "v_mfma_f32_16x16x32_bf16 v[172:175], a[92:95], a[12:15], v[172:175]\n"
      "v_exp_f32 v178, v178\n"
      "v_cvt_pk_bf16_f32 v28, v176, v177\n"
      "v_mfma_f32_16x16x32_bf16 v[180:183], a[92:95], a[28:31], v[180:183]\n"
      "buffer_load_dwordx4 v248, %[rK], s46 offen nt lds\n"
      "v_exp_f32 v179, v179\n"
      "v_mfma_f32_16x16x32_bf16 v[188:191], a[92:95], a[44:47], v[188:191]\n"
      "s_waitcnt lgkmcnt(4)\n"
      "ds_read_b128 a[80:83], v240 offset:28672\n"
      "v_exp_f32 v184, v184\n"
      "v_mfma_f32_16x16x32_bf16 v[192:195], a[64:67], a[0:3], v[224:227]\n"
      "ds_read_b128 a[84:87], v241 offset:28672\n"
      "v_cvt_pk_bf16_f32 v29, v178, v179\n"
      "v_mfma_f32_16x16x32_bf16 v[200:203], a[64:67], a[16:19], v[228:231]\n"
      "v_exp_f32 v185, v185\n"
      "v_mfma_f32_16x16x32_bf16 v[208:211], a[64:67], a[32:35], v[232:235]\n"
      "s_waitcnt lgkmcnt(5)\n"
      "v_exp_f32 v186, v186\n"
      "v_cvt_pk_bf16_f32 v32, v184, v185\n"
      "v_mfma_f32_16x16x32_bf16 v[192:195], a[68:71], a[4:7], v[192:195]\n"
      "s_mov_b32 s59, 1024\n"
      "v_exp_f32 v187, v187\n"
      "s_nop 0\n"
      "v_cvt_pk_bf16_f32 v33, v186, v187\n"
      "v_mfma_f32_16x16x32_bf16 v[200:203], a[68:71], a[20:23], v[200:203]\n"
      "v_exp_f32 v172, v172\n"
      "v_mfma_f32_16x16x32_bf16 v[208:211], a[68:71], a[36:39], v[208:211]\n"
      "ds_read_b128 a[88:91], v242 offset:28672\n"
      "v_exp_f32 v173, v173\n"
      "v_mfma_f32_16x16x32_bf16 a[96:99], a[112:115], v[12:15], a[96:99]\n"
      "s_waitcnt lgkmcnt(4)\n"
      "v_exp_f32 v174, v174\n"
      "v_cvt_pk_bf16_f32 v26, v172, v173\n"
      "v_mfma_f32_16x16x32_bf16 v[192:195], a[72:75], a[8:11], v[192:195]\n"
      "v_exp_f32 v175, v175\n"
      "v_mfma_f32_16x16x32_bf16 v[200:203], a[72:75], a[24:27], v[200:203]\n"
      "v_exp_f32 v180, v180\n"
      "v_cvt_pk_bf16_f32 v27, v174, v175\n"
      "v_mfma_f32_16x16x32_bf16 v[208:211], a[72:75], a[40:43], v[208:211]\n"
      "s_waitcnt lgkmcnt(3)\n"
      "ds_read_b128 a[92:95], v243 offset:28672\n"
      "v_exp_f32 v181, v181\n"
      "v_mfma_f32_16x16x32_bf16 v[192:195], a[76:79], a[12:15], v[192:195]\n"
      "v_exp_f32 v182, v182\n"
      "v_cvt_pk_bf16_f32 v30, v180, v181\n"
      "v_mfma_f32_16x16x32_bf16 v[200:203], a[76:79], a[28:31], v[200:203]\n"
      "buffer_load_dword a200, v152, %[rWo], s59 offen\n"
      "v_add_u32 v240, 65536, v240\n"
      "v_add_u32 v241, 65536, v241\n"
      "v_mfma_f32_16x16x32_bf16 v[208:211], a[76:79], a[44:47], v[208:211]\n"
      "v_exp_f32 v183, v183\n"
      "v_add_u32 v242, 65536, v242\n"
      "v_add_u32 v243, 65536, v243\n"
      "v_mfma_f32_16x16x32_bf16 a[100:103], a[112:115], v[16:19], a[100:103]\n"
      "s_waitcnt lgkmcnt(3)\n"
      "v_add_u32 v244, 65536, v244\n"
      "v_add_u32 v245, 65536, v245\n"
      "v_add_u32 v246, 65536, v246\n"
      "v_add_u32 v247, 65536, v247\n"
      "v_mfma_f32_16x16x32_bf16 v[196:199], a[80:83], a[0:3], v[224:227]\n"
      "v_exp_f32 v188, v188\n"
      "v_cvt_pk_bf16_f32 v31, v182, v183\n"
      "v_mfma_f32_16x16x32_bf16 v[204:207], a[80:83], a[16:19], v[228:231]\n"
      "v_exp_f32 v189, v189\n"
      "v_mfma_f32_16x16x32_bf16 v[212:215], a[80:83], a[32:35], v[232:235]\n"
      "s_waitcnt vmcnt(6) lgkmcnt(0)\n"
      "s_barrier\n"
      "v_exp_f32 v190, v190\n"
      "v_cvt_pk_bf16_f32 v34, v188, v189\n"
      "v_mfma_f32_16x16x32_bf16 v[196:199], a[84:87], a[4:7], v[196:199]\n"
      "s_add_u32 m0, s43, 49152\n"
      "v_exp_f32 v191, v191\n"
      "s_nop 0\n"
      "v_cvt_pk_bf16_f32 v35, v190, v191\n"
      "v_mfma_f32_16x16x32_bf16 v[204:207], a[84:87], a[20:23], v[204:207]\n"
      "s_mov_b32 s46, 81920\n"
      "ds_read_b128 a[64:67], v240 offset:0\n"
      "v_exp_f32 v192, v192\n"
      "v_mfma_f32_16x16x32_bf16 v[212:215], a[84:87], a[36:39], v[212:215]\n"
      "ds_read_b64_tr_b16 a[68:69], v252 offset:0\n"
      "ds_read_b64_tr_b16 a[70:71], v252 offset:2048\n"
      "v_exp_f32 v193, v193\n"
      "v_mfma_f32_16x16x32_bf16 a[104:107], a[112:115], v[20:23], a[104:107]\n"
      "ds_read_b128 a[72:75], v241 offset:0\n"
      "v_exp_f32 v194, v194\n"
      "v_mfma_f32_16x16x32_bf16 v[196:199], a[88:91], a[8:11], v[196:199]\n"
      "buffer_load_dwordx4 v248, %[rK], s46 offen nt lds\n"
      "v_cvt_pk_bf16_f32 v36, v192, v193\n"
      "v_mfma_f32_16x16x32_bf16 v[204:207], a[88:91], a[24:27], v[204:207]\n"
      "ds_read_b128 a[76:79], v242 offset:0\n"
      "v_exp_f32 v195, v195\n"
      "v_mfma_f32_16x16x32_bf16 v[212:215], a[88:91], a[40:43], v[212:215]\n"
      "v_exp_f32 v200, v200\n"
      "v_cvt_pk_bf16_f32 v37, v194, v195\n"
      "v_mfma_f32_16x16x32_bf16 v[196:199], a[92:95], a[12:15], v[196:199]\n"
      "v_exp_f32 v201, v201\n"
      "v_mfma_f32_16x16x32_bf16 v[204:207], a[92:95], a[28:31], v[204:207]\n"
      "s_add_u32 m0, s43, 53248\n"
      "v_exp_f32 v202, v202\n"
      "v_cvt_pk_bf16_f32 v40, v200, v201\n"
      "v_mfma_f32_16x16x32_bf16 v[212:215], a[92:95], a[44:47], v[212:215]\n"
      "s_mov_b32 s46, 86016\n"
      "ds_read_b64_tr_b16 a[80:81], v253 offset:0\n"
      "v_exp_f32 v203, v203\n"
      "v_mfma_f32_16x16x32_bf16 a[108:111], a[112:115], v[216:219], 0\n"
      "s_waitcnt lgkmcnt(5)\n"
      "ds_read_b64_tr_b16 a[82:83], v253 offset:2048\n"
      "v_exp_f32 v208, v208\n"
      "v_mfma_f32_16x16x32_bf16 v[168:171], a[64:67], a[0:3], v[224:227]\n"
      "ds_read_b128 a[84:87], v243 offset:0\n"
      "v_cvt_pk_bf16_f32 v41, v202, v203\n"
      "v_mfma_f32_16x16x32_bf16 v[176:179], a[64:67], a[16:19], v[228:231]\n"
      "v_exp_f32 v209, v209\n"
      "v_mfma_f32_16x16x32_bf16 v[184:187], a[64:67], a[32:35], v[232:235]\n"
      "s_waitcnt lgkmcnt(5)\n"
      "v_exp_f32 v210, v210\n"
      "v_cvt_pk_bf16_f32 v44, v208, v209\n"
      "v_mfma_f32_16x16x32_bf16 a[116:119], a[68:71], v[0:3], 0\n"
      "v_exp_f32 v211, v211\n"
      "s_nop 0\n"
      "v_cvt_pk_bf16_f32 v45, v210, v211\n"
      "v_mfma_f32_16x16x32_bf16 a[124:127], a[68:71], v[4:7], 0\n"
      "buffer_load_dwordx4 v248, %[rK], s46 offen nt lds\n"
      "v_exp_f32 v196, v196\n"
      "v_mfma_f32_16x16x32_bf16 a[132:135], a[68:71], v[8:11], 0\n"
      "s_waitcnt lgkmcnt(4)\n"
      "ds_read_b128 a[88:91], v240 offset:4096\n"
      "v_exp_f32 v197, v197\n"
      "v_mfma_f32_16x16x32_bf16 v[168:171], a[72:75], a[4:7], v[168:171]\n"
      "v_exp_f32 v198, v198\n"
      "v_cvt_pk_bf16_f32 v38, v196, v197\n"
      "v_mfma_f32_16x16x32_bf16 v[176:179], a[72:75], a[20:23], v[176:179]\n"
      "ds_read_b128 a[92:95], v241 offset:4096\n"
      "v_exp_f32 v199, v199\n"
      "v_mfma_f32_16x16x32_bf16 v[184:187], a[72:75], a[36:39], v[184:187]\n"
      "s_add_u32 m0, s43, 57344\n"
      "v_exp_f32 v204, v204\n"
      "v_mfma_f32_16x16x32_bf16 a[96:99], a[112:115], v[24:27], a[96:99]\n"
      "s_waitcnt lgkmcnt(5)\n"
      "ds_read_b128 a[64:67], v242 offset:4096\n"
      "v_cvt_pk_bf16_f32 v39, v198, v199\n"
      "v_mfma_f32_16x16x32_bf16 v[168:171], a[76:79], a[8:11], v[168:171]\n"
      "s_mov_b32 s46, 90112\n"
      "v_exp_f32 v205, v205\n"
      "v_mfma_f32_16x16x32_bf16 v[176:179], a[76:79], a[24:27], v[176:179]\n"
      "v_exp_f32 v206, v206\n"
      "v_mfma_f32_16x16x32_bf16 v[184:187], a[76:79], a[40:43], v[184:187]\n"
      "s_waitcnt lgkmcnt(4)\n"
      "ds_read_b64_tr_b16 a[68:69], v254 offset:0\n"
      "v_cvt_pk_bf16_f32 v42, v204, v205\n"
      "v_mfma_f32_16x16x32_bf16 a[120:123], a[80:83], v[0:3], 0\n"
      "v_exp_f32 v207, v207\n"
      "v_mfma_f32_16x16x32_bf16 a[128:131], a[80:83], v[4:7], 0\n"
      "s_mov_b32 s59, 2048\n"
      "ds_read_b64_tr_b16 a[70:71], v254 offset:2048\n"
      "buffer_load_dwordx4 v248, %[rK], s46 offen nt lds\n"
      "v_mfma_f32_16x16x32_bf16 a[136:139], a[80:83], v[8:11], 0\n"
      "s_waitcnt lgkmcnt(5)\n"
      "v_exp_f32 v212, v212\n"
      "v_mfma_f32_16x16x32_bf16 v[168:171], a[84:87], a[12:15], v[168:171]\n"
      "ds_read_b128 a[72:75], v243 offset:4096\n"
      "v_cvt_pk_bf16_f32 v43, v206, v207\n"
      "v_mfma_f32_16x16x32_bf16 v[176:179], a[84:87], a[28:31], v[176:179]\n"
      "v_exp_f32 v213, v213\n"
      "v_mfma_f32_16x16x32_bf16 v[184:187], a[84:87], a[44:47], v[184:187]\n"
      "v_exp_f32 v214, v214\n"
      "v_mfma_f32_16x16x32_bf16 a[100:103], a[112:115], v[28:31], a[100:103]\n"
      "s_waitcnt lgkmcnt(5)\n"
      "ds_read_b128 a[76:79], v240 offset:8192\n"
      "v_cvt_pk_bf16_f32 v46, v212, v213\n"
      "v_mfma_f32_16x16x32_bf16 v[172:175], a[88:91], a[0:3], v[224:227]\n"
      "s_add_u32 m0, s43, 61440\n"
      "ds_read_b128 a[168:171], v244 offset:0\n"
      "buffer_load_dword a200, v152, %[rWo], s59 offen\n"
      "v_mfma_f32_16x16x32_bf16 v[180:183], a[88:91], a[16:19], v[228:231]\n"
      "s_mov_b32 s46, 94208\n"
      "v_exp_f32 v215, v215\n"
      "v_mfma_f32_16x16x32_bf16 v[188:191], a[88:91], a[32:35], v[232:235]\n"
      "s_waitcnt lgkmcnt(6)\n"
      "ds_read_b64_tr_b16 a[80:81], v255 offset:0\n"
      "v_cvt_pk_bf16_f32 v47, v214, v215\n"
      "v_mfma_f32_16x16x32_bf16 v[172:175], a[92:95], a[4:7], v[172:175]\n"
      "v_exp_f32 v168, v168\n"
      "v_mfma_f32_16x16x32_bf16 v[180:183], a[92:95], a[20:23], v[180:183]\n"
      "ds_read_b64_tr_b16 a[82:83], v255 offset:2048\n"
      "v_exp_f32 v169, v169\n"
      "v_mfma_f32_16x16x32_bf16 v[188:191], a[92:95], a[36:39], v[188:191]\n"
      "v_exp_f32 v170, v170\n"
      "v_mfma_f32_16x16x32_bf16 a[104:107], a[112:115], v[32:35], a[104:107]\n"
      "s_waitcnt lgkmcnt(7)\n"
      "buffer_load_dwordx4 v248, %[rK], s46 offen nt lds\n"
      "v_cvt_pk_bf16_f32 v48, v168, v169\n"
      "v_mfma_f32_16x16x32_bf16 v[172:175], a[64:67], a[8:11], v[172:175]\n"
      "ds_read_b128 a[84:87], v241 offset:8192\n"
      "v_exp_f32 v171, v171\n"
      "v_mfma_f32_16x16x32_bf16 v[180:183], a[64:67], a[24:27], v[180:183]\n"
      "ds_read_b128 a[172:175], v245 offset:0\n"
      "v_exp_f32 v176, v176\n"
      "v_mfma_f32_16x16x32_bf16 v[188:191], a[64:67], a[40:43], v[188:191]\n"
      "s_waitcnt lgkmcnt(7)\n"
      "ds_read_b128 a[88:91], v242 offset:8192\n"
      "v_cvt_pk_bf16_f32 v49, v170, v171\n"
      "v_mfma_f32_16x16x32_bf16 a[140:143], a[68:71], v[0:3], 0\n"
      "s_add_u32 m0, s43, 0\n"
      "v_exp_f32 v177, v177\n"
      "v_mfma_f32_16x16x32_bf16 a[148:151], a[68:71], v[4:7], 0\n"
      "s_mov_b32 s46, 98304\n"
      "v_exp_f32 v178, v178\n"
      "v_cvt_pk_bf16_f32 v52, v176, v177\n"
      "v_mfma_f32_16x16x32_bf16 a[156:159], a[68:71], v[8:11], 0\n"
      "s_waitcnt lgkmcnt(7)\n"
      "ds_read_b128 a[176:179], v246 offset:0\n"
      "v_exp_f32 v179, v179\n"
      "v_mfma_f32_16x16x32_bf16 v[172:175], a[72:75], a[12:15], v[172:175]\n"
      "v_exp_f32 v184, v184\n"
      "v_mfma_f32_16x16x32_bf16 v[180:183], a[72:75], a[28:31], v[180:183]\n"
      "ds_read_b64_tr_b16 a[92:93], v252 offset:4096\n"
      "v_cvt_pk_bf16_f32 v53, v178, v179\n"
      "v_mfma_f32_16x16x32_bf16 v[188:191], a[72:75], a[44:47], v[188:191]\n"
      "s_waitcnt lgkmcnt(8)\n"
      "v_exp_f32 v185, v185\n"
      "v_mfma_f32_16x16x32_bf16 v[192:195], a[76:79], a[0:3], v[224:227]\n"
      "v_exp_f32 v186, v186\n"
      "v_cvt_pk_bf16_f32 v56, v184, v185\n"
      "v_mfma_f32_16x16x32_bf16 v[200:203], a[76:79], a[16:19], v[228:231]\n"
      "ds_read_b64_tr_b16 a[94:95], v252 offset:6144\n"
      "buffer_load_dwordx4 v248, %[rK], s46 offen nt lds\n"
      "v_mfma_f32_16x16x32_bf16 v[208:211], a[76:79], a[32:35], v[232:235]\n"
      "s_waitcnt lgkmcnt(8)\n"
      "v_exp_f32 v187, v187\n"
      "s_nop 0\n"
      "v_cvt_pk_bf16_f32 v57, v186, v187\n"
      "v_mfma_f32_16x16x32_bf16 v[216:219], a[168:171], a[48:51], v[236:239]\n"
      "s_waitcnt lgkmcnt(6)\n"
      "ds_read_b128 a[64:67], v243 offset:8192\n"
      "v_exp_f32 v172, v172\n"
      "v_mfma_f32_16x16x32_bf16 a[144:147], a[80:83], v[0:3], 0\n"
      "ds_read_b128 a[180:183], v247 offset:0\n"
      "ds_read_b128 a[68:71], v240 offset:12288\n"
      "v_mfma_f32_16x16x32_bf16 a[152:155], a[80:83], v[4:7], 0\n"
      "s_add_u32 m0, s43, 4096\n"
      "v_exp_f32 v173, v173\n"
      "v_mfma_f32_16x16x32_bf16 a[160:163], a[80:83], v[8:11], 0\n"
      "s_waitcnt lgkmcnt(8)\n"
      "v_exp_f32 v174, v174\n"
      "v_mfma_f32_16x16x32_bf16 v[192:195], a[84:87], a[4:7], v[192:195]\n"
      "s_mov_b32 s46, 102400\n"
      "ds_read_b128 a[184:187], v244 offset:4096\n"
      "v_cvt_pk_bf16_f32 v50, v172, v173\n"
      "v_mfma_f32_16x16x32_bf16 v[200:203], a[84:87], a[20:23], v[200:203]\n"
      "v_exp_f32 v175, v175\n"
      "v_mfma_f32_16x16x32_bf16 v[208:211], a[84:87], a[36:39], v[208:211]\n"
      "s_waitcnt lgkmcnt(8)\n"
      "v_exp_f32 v180, v180\n"
      "v_mfma_f32_16x16x32_bf16 v[216:219], a[172:175], a[52:55], v[216:219]\n"
      "buffer_load_dwordx4 v248, %[rK], s46 offen nt lds\n"
      "v_cvt_pk_bf16_f32 v51, v174, v175\n"
      "v_mfma_f32_16x16x32_bf16 a[96:99], a[112:115], v[36:39], a[96:99]\n"
      "s_waitcnt lgkmcnt(7)\n"
      "v_exp_f32 v181, v181\n"
      "v_mfma_f32_16x16x32_bf16 v[192:195], a[88:91], a[8:11], v[192:195]\n"
      "ds_read_b128 a[72:75], v241 offset:12288\n"
      "ds_read_b128 a[188:191], v245 offset:4096\n"
      "v_mfma_f32_16x16x32_bf16 v[200:203], a[88:91], a[24:27], v[200:203]\n"
      "v_exp_f32 v182, v182\n"
      "v_mfma_f32_16x16x32_bf16 v[208:211], a[88:91], a[40:43], v[208:211]\n"
      "s_waitcnt lgkmcnt(8)\n"
      "ds_read_b128 a[76:79], v242 offset:12288\n"
      "v_cvt_pk_bf16_f32 v54, v180, v181\n"
      "v_mfma_f32_16x16x32_bf16 v[216:219], a[176:179], a[56:59], v[216:219]\n"
      "s_waitcnt lgkmcnt(7)\n"
      "v_exp_f32 v183, v183\n"
      "v_mfma_f32_16x16x32_bf16 a[116:119], a[92:95], v[12:15], a[116:119]\n"
      "s_add_u32 m0, s43, 8192\n"
      "ds_read_b128 a[192:195], v246 offset:4096\n"
      "ds_read_b64_tr_b16 a[80:81], v253 offset:4096\n"
      "v_mfma_f32_16x16x32_bf16 a[124:127], a[92:95], v[16:19], a[124:127]\n"
      "s_mov_b32 s46, 106496\n"
      "v_exp_f32 v188, v188\n"
      "v_mfma_f32_16x16x32_bf16 a[132:135], a[92:95], v[20:23], a[132:135]\n"
      "s_waitcnt lgkmcnt(8)\n"
      "ds_read_b64_tr_b16 a[82:83], v253 offset:6144\n"
      "v_cvt_pk_bf16_f32 v55, v182, v183\n"
      "v_mfma_f32_16x16x32_bf16 v[192:195], a[64:67], a[12:15], v[192:195]\n"
      "s_mov_b32 s59, 3072\n"
      "v_exp_f32 v189, v189\n"
      "v_mfma_f32_16x16x32_bf16 v[200:203], a[64:67], a[28:31], v[200:203]\n"
      "v_exp_f32 v190, v190\n"
      "v_mfma_f32_16x16x32_bf16 v[208:211], a[64:67], a[44:47], v[208:211]\n"
      "s_waitcnt lgkmcnt(8)\n"
      "buffer_load_dwordx4 v248, %[rK], s46 offen nt lds\n"
      "v_cvt_pk_bf16_f32 v58, v188, v189\n"
      "v_mfma_f32_16x16x32_bf16 v[216:219], a[180:183], a[60:63], v[216:219]\n"
      "buffer_load_dword a200, v152, %[rWo], s59 offen\n"
      "ds_read_b128 a[84:87], v243 offset:12288\n"
      "v_mfma_f32_16x16x32_bf16 a[100:103], a[112:115], v[40:43], a[100:103]\n"
      "s_waitcnt lgkmcnt(8)\n"
      "v_exp_f32 v191, v191\n"
      "v_mfma_f32_16x16x32_bf16 v[196:199], a[68:71], a[0:3], v[224:227]\n"
      "ds_read_b128 a[196:199], v247 offset:4096\n"
      "v_cvt_pk_bf16_f32 v59, v190, v191\n"
      "v_mfma_f32_16x16x32_bf16 v[204:207], a[68:71], a[16:19], v[228:231]\n"
      "v_exp_f32 v192, v192\n"
      "v_mfma_f32_16x16x32_bf16 v[212:215], a[68:71], a[32:35], v[232:235]\n"
      "s_waitcnt lgkmcnt(8)\n"
      "v_exp_f32 v193, v193\n"
      "v_mfma_f32_16x16x32_bf16 v[220:223], a[184:187], a[48:51], v[236:239]\n"
      "s_waitcnt lgkmcnt(7)\n"
      "ds_read_b128 a[88:91], v240 offset:16384\n"
      "ds_read_b64_tr_b16 a[92:93], v254 offset:4096\n"
      "v_mfma_f32_16x16x32_bf16 v[196:199], a[72:75], a[4:7], v[196:199]\n"
      "s_add_u32 m0, s43, 12288\n"
      "v_exp_f32 v194, v194\n"
      "v_mfma_f32_16x16x32_bf16 v[204:207], a[72:75], a[20:23], v[204:207]\n"
      "s_mov_b32 s46, 110592\n"
      "ds_read_b64_tr_b16 a[94:95], v254 offset:6144\n"
      "v_cvt_pk_bf16_f32 v60, v192, v193\n"
      "v_mfma_f32_16x16x32_bf16 v[212:215], a[72:75], a[36:39], v[212:215]\n"
      "s_waitcnt lgkmcnt(9)\n"
      "v_exp_f32 v195, v195\n"
      "v_mfma_f32_16x16x32_bf16 v[220:223], a[188:191], a[52:55], v[220:223]\n"
      "v_exp_f32 v200, v200\n"
      "v_mfma_f32_16x16x32_bf16 a[104:107], a[112:115], v[44:47], a[104:107]\n"
      "s_waitcnt lgkmcnt(8)\n"
      "buffer_load_dwordx4 v248, %[rK], s46 offen nt lds\n"
      "v_cvt_pk_bf16_f32 v61, v194, v195\n"
      "v_mfma_f32_16x16x32_bf16 v[196:199], a[76:79], a[8:11], v[196:199]\n"
      "v_exp_f32 v201, v201\n"
      "v_mfma_f32_16x16x32_bf16 v[204:207], a[76:79], a[24:27], v[204:207]\n"
      "ds_read_b128 a[64:67], v241 offset:16384\n"
      "v_exp_f32 v202, v202\n"
      "v_mfma_f32_16x16x32_bf16 v[212:215], a[76:79], a[40:43], v[212:215]\n"
      "s_waitcnt lgkmcnt(8)\n"
      "ds_read_b128 a[68:71], v242 offset:16384\n"
      "v_cvt_pk_bf16_f32 v64, v200, v201\n"
      "v_mfma_f32_16x16x32_bf16 v[220:223], a[192:195], a[56:59], v[220:223]\n"
      "s_waitcnt lgkmcnt(7)\n"
      "v_exp_f32 v203, v203\n"
      "v_mfma_f32_16x16x32_bf16 a[120:123], a[80:83], v[12:15], a[120:123]\n"
      "v_exp_f32 v208, v208\n"
      "v_cvt_pk_bf16_f32 v65, v202, v203\n"
      "v_mfma_f32_16x16x32_bf16 a[128:131], a[80:83], v[16:19], a[128:131]\n"
      "s_add_u32 m0, s43, 16384\n"
      "v_exp_f32 v209, v209\n"
      "v_mfma_f32_16x16x32_bf16 a[136:139], a[80:83], v[20:23], a[136:139]\n"
      "s_waitcnt lgkmcnt(6)\n"
      "v_exp_f32 v210, v210\n"
      "v_cvt_pk_bf16_f32 v68, v208, v209\n"
      "v_mfma_f32_16x16x32_bf16 v[196:199], a[84:87], a[12:15], v[196:199]\n"
      "s_mov_b32 s46, 114688\n"
      "v_exp_f32 v211, v211\n"
      "v_mfma_f32_16x16x32_bf16 v[204:207], a[84:87], a[28:31], v[204:207]\n"
      "ds_read_b64_tr_b16 a[72:73], v255 offset:4096\n"
      "v_exp_f32 v216, v216\n"
      "v_mfma_f32_16x16x32_bf16 v[212:215], a[84:87], a[44:47], v[212:215]\n"
      "s_waitcnt lgkmcnt(6)\n"
      "ds_read_b64_tr_b16 a[74:75], v255 offset:6144\n"
      "v_cvt_pk_bf16_f32 v69, v210, v211\n"
      "v_mfma_f32_16x16x32_bf16 v[220:223], a[196:199], a[60:63], v[220:223]\n"
      "s_waitcnt lgkmcnt(6)\n"
      "v_exp_f32 v217, v217\n"
      "v_mfma_f32_16x16x32_bf16 v[168:171], a[88:91], a[0:3], v[224:227]\n"
      "ds_read_b128 a[76:79], v243 offset:16384\n"
      "v_exp_f32 v218, v218\n"
      "v_mfma_f32_16x16x32_bf16 v[176:179], a[88:91], a[16:19], v[228:231]\n"
      "buffer_load_dwordx4 v248, %[rK], s46 offen nt lds\n"
      "v_cvt_pk_bf16_f32 v216, v216, v217\n"
      "v_mfma_f32_16x16x32_bf16 v[184:187], a[88:91], a[32:35], v[232:235]\n"
      "s_waitcnt lgkmcnt(5)\n"
      "v_exp_f32 v219, v219\n"
      "s_nop 0\n"
      "v_cvt_pk_bf16_f32 v217, v218, v219\n"
      "v_mfma_f32_16x16x32_bf16 a[140:143], a[92:95], v[12:15], a[140:143]\n"
      "v_exp_f32 v196, v196\n"
      "v_mfma_f32_16x16x32_bf16 a[148:151], a[92:95], v[16:19], a[148:151]\n"
      "ds_read_b128 a[80:83], v240 offset:20480\n"
      "v_exp_f32 v197, v197\n"
      "v_mfma_f32_16x16x32_bf16 a[156:159], a[92:95], v[20:23], a[156:159]\n"
      "s_waitcnt lgkmcnt(5)\n"
      "v_exp_f32 v198, v198\n"
      "v_cvt_pk_bf16_f32 v62, v196, v197\n"
      "v_mfma_f32_16x16x32_bf16 v[168:171], a[64:67], a[4:7], v[168:171]\n"
      "s_add_u32 m0, s43, 20480\n"
      "v_exp_f32 v199, v199\n"
      "v_mfma_f32_16x16x32_bf16 v[176:179], a[64:67], a[20:23], v[176:179]\n"
      "s_mov_b32 s46, 118784\n"
      "ds_read_b128 a[84:87], v241 offset:20480\n"
      "v_exp_f32 v204, v204\n"
      "v_mfma_f32_16x16x32_bf16 v[184:187], a[64:67], a[36:39], v[184:187]\n"
      "ds_read_b128 a[88:91], v242 offset:20480\n"
      "v_cvt_pk_bf16_f32 v63, v198, v199\n"
      "v_mfma_f32_16x16x32_bf16 a[96:99], a[112:115], v[48:51], a[96:99]\n"
      "s_waitcnt lgkmcnt(6)\n"
      "v_exp_f32 v205, v205\n"
      "v_mfma_f32_16x16x32_bf16 v[168:171], a[68:71], a[8:11], v[168:171]\n"
      "v_exp_f32 v206, v206\n"
      "v_cvt_pk_bf16_f32 v66, v204, v205\n"
      "v_mfma_f32_16x16x32_bf16 v[176:179], a[68:71], a[24:27], v[176:179]\n"
      "v_exp_f32 v207, v207\n"
      "v_mfma_f32_16x16x32_bf16 v[184:187], a[68:71], a[40:43], v[184:187]\n"
      "s_waitcnt lgkmcnt(4)\n"
      "buffer_load_dwordx4 v248, %[rK], s46 offen nt lds\n"
      "ds_read_b64_tr_b16 a[92:93], v252 offset:8192\n"
      "v_mfma_f32_16x16x32_bf16 a[144:147], a[72:75], v[12:15], a[144:147]\n"
      "s_mov_b32 s59, 4096\n"
      "v_exp_f32 v212, v212\n"
      "v_cvt_pk_bf16_f32 v67, v206, v207\n"
      "v_mfma_f32_16x16x32_bf16 a[152:155], a[72:75], v[16:19], a[152:155]\n"
      "ds_read_b64_tr_b16 a[94:95], v252 offset:10240\n"
      "v_exp_f32 v213, v213\n"
      "v_mfma_f32_16x16x32_bf16 a[160:163], a[72:75], v[20:23], a[160:163]\n"
      "s_waitcnt lgkmcnt(5)\n"
      "v_exp_f32 v214, v214\n"
      "v_cvt_pk_bf16_f32 v70, v212, v213\n"
      "v_mfma_f32_16x16x32_bf16 v[168:171], a[76:79], a[12:15], v[168:171]\n"
      "s_add_u32 m0, s43, 24576\n"
      "v_exp_f32 v215, v215\n"
      "v_mfma_f32_16x16x32_bf16 v[176:179], a[76:79], a[28:31], v[176:179]\n"
      "s_mov_b32 s46, 122880\n"
      "ds_read_b128 a[64:67], v243 offset:20480\n"
      "v_exp_f32 v220, v220\n"
      "v_mfma_f32_16x16x32_bf16 v[184:187], a[76:79], a[44:47], v[184:187]\n"
      "buffer_load_dword a200, v152, %[rWo], s59 offen\n"
      "v_cvt_pk_bf16_f32 v71, v214, v215\n"
      "v_mfma_f32_16x16x32_bf16 a[100:103], a[112:115], v[52:55], a[100:103]\n"
      "s_waitcnt lgkmcnt(5)\n"
      "v_exp_f32 v221, v221\n"
      "v_mfma_f32_16x16x32_bf16 v[172:175], a[80:83], a[0:3], v[224:227]\n"
      "v_exp_f32 v222, v222\n"
      "v_cvt_pk_bf16_f32 v218, v220, v221\n"
      "v_mfma_f32_16x16x32_bf16 v[180:183], a[80:83], a[16:19], v[228:231]\n"
      "ds_read_b128 a[68:71], v240 offset:24576\n"
      "buffer_load_dwordx4 v248, %[rK], s46 offen nt lds\n"
      "v_mfma_f32_16x16x32_bf16 v[188:191], a[80:83], a[32:35], v[232:235]\n"
      "s_waitcnt lgkmcnt(5)\n"
      "v_exp_f32 v223, v223\n"
      "s_nop 0\n"
      "v_cvt_pk_bf16_f32 v219, v222, v223\n"
      "v_mfma_f32_16x16x32_bf16 v[172:175], a[84:87], a[4:7], v[172:175]\n"
      "ds_write_b128 v251, v[216:219] offset:4096\n"
      "ds_read_b64_tr_b16 a[72:73], v253 offset:8192\n"
      "ds_read_b64_tr_b16 a[74:75], v253 offset:10240\n"
      "v_mfma_f32_16x16x32_bf16 v[180:183], a[84:87], a[20:23], v[180:183]\n"
      "v_exp_f32 v168, v168\n"
      "v_mfma_f32_16x16x32_bf16 v[188:191], a[84:87], a[36:39], v[188:191]\n"
      "v_exp_f32 v169, v169\n"
      "v_mfma_f32_16x16x32_bf16 a[104:107], a[112:115], v[56:59], a[104:107]\n"
      "s_waitcnt lgkmcnt(7)\n"
      "v_exp_f32 v170, v170\n"
      "v_cvt_pk_bf16_f32 v72, v168, v169\n"
      "v_mfma_f32_16x16x32_bf16 v[172:175], a[88:91], a[8:11], v[172:175]\n"
      "s_add_u32 m0, s43, 28672\n"
      "ds_read_b128 a[76:79], v241 offset:24576\n"
      "v_exp_f32 v171, v171\n"
      "v_mfma_f32_16x16x32_bf16 v[180:183], a[88:91], a[24:27], v[180:183]\n"
      "s_mov_b32 s46, 126976\n"
      "v_exp_f32 v176, v176\n"
      "v_cvt_pk_bf16_f32 v73, v170, v171\n"
      "v_mfma_f32_16x16x32_bf16 v[188:191], a[88:91], a[40:43], v[188:191]\n"
      "s_waitcnt lgkmcnt(6)\n"
      "v_exp_f32 v177, v177\n"
      "v_mfma_f32_16x16x32_bf16 a[116:119], a[92:95], v[24:27], a[116:119]\n"
      "ds_read_b128 a[80:83], v242 offset:24576\n"
      "v_exp_f32 v178, v178\n"
      "v_mfma_f32_16x16x32_bf16 a[124:127], a[92:95], v[28:31], a[124:127]\n"
      "ds_read_b64_tr_b16 a[84:85], v254 offset:8192\n"
      "v_cvt_pk_bf16_f32 v76, v176, v177\n"
      "v_mfma_f32_16x16x32_bf16 a[132:135], a[92:95], v[32:35], a[132:135]\n"
      "s_waitcnt lgkmcnt(7)\n"
      "v_exp_f32 v179, v179\n"
      "v_mfma_f32_16x16x32_bf16 v[172:175], a[64:67], a[12:15], v[172:175]\n"
      "ds_read_b64_tr_b16 a[86:87], v254 offset:10240\n"
      "v_exp_f32 v184, v184\n"
      "v_mfma_f32_16x16x32_bf16 v[180:183], a[64:67], a[28:31], v[180:183]\n"
      "buffer_load_dwordx4 v248, %[rK], s46 offen nt lds\n"
      "v_cvt_pk_bf16_f32 v77, v178, v179\n"
      "v_mfma_f32_16x16x32_bf16 v[188:191], a[64:67], a[44:47], v[188:191]\n"
      "s_waitcnt lgkmcnt(7)\n"
      "v_exp_f32 v185, v185\n"
      "v_mfma_f32_16x16x32_bf16 v[192:195], a[68:71], a[0:3], v[224:227]\n"
      "v_exp_f32 v186, v186\n"
      "v_cvt_pk_bf16_f32 v80, v184, v185\n"
      "v_mfma_f32_16x16x32_bf16 v[200:203], a[68:71], a[16:19], v[228:231]\n"
      "ds_read_b128 a[88:91], v243 offset:24576\n"
      "ds_read_b128 a[92:95], v240 offset:28672\n"
      "v_mfma_f32_16x16x32_bf16 v[208:211], a[68:71], a[32:35], v[232:235]\n"
      "s_waitcnt lgkmcnt(6)\n"
      "v_exp_f32 v187, v187\n"
      "s_nop 0\n"
      "v_cvt_pk_bf16_f32 v81, v186, v187\n"
      "v_mfma_f32_16x16x32_bf16 a[120:123], a[72:75], v[24:27], a[120:123]\n"
      "v_exp_f32 v172, v172\n"
      "v_mfma_f32_16x16x32_bf16 a[128:131], a[72:75], v[28:31], a[128:131]\n"
      "v_exp_f32 v173, v173\n"
      "v_mfma_f32_16x16x32_bf16 a[136:139], a[72:75], v[32:35], a[136:139]\n"
      "s_waitcnt lgkmcnt(5)\n"
      "v_exp_f32 v174, v174\n"
      "v_mfma_f32_16x16x32_bf16 v[192:195], a[76:79], a[4:7], v[192:195]\n"
      "ds_read_b128 a[64:67], v241 offset:28672\n"
      "v_cvt_pk_bf16_f32 v74, v172, v173\n"
      "v_mfma_f32_16x16x32_bf16 v[200:203], a[76:79], a[20:23], v[200:203]\n"
      "v_exp_f32 v175, v175\n"
      "v_mfma_f32_16x16x32_bf16 v[208:211], a[76:79], a[36:39], v[208:211]\n"
      "v_exp_f32 v180, v180\n"
      "v_mfma_f32_16x16x32_bf16 a[96:99], a[112:115], v[60:63], a[96:99]\n"
      "s_waitcnt lgkmcnt(5)\n"
      "ds_read_b128 a[68:71], v242 offset:28672\n"
      "v_cvt_pk_bf16_f32 v75, v174, v175\n"
      "v_mfma_f32_16x16x32_bf16 v[192:195], a[80:83], a[8:11], v[192:195]\n"
      "v_exp_f32 v181, v181\n"
      "v_mfma_f32_16x16x32_bf16 v[200:203], a[80:83], a[24:27], v[200:203]\n"
      "v_exp_f32 v182, v182\n"
      "v_mfma_f32_16x16x32_bf16 v[208:211], a[80:83], a[40:43], v[208:211]\n"
      "s_waitcnt lgkmcnt(4)\n"
      "ds_read_b64_tr_b16 a[72:73], v255 offset:8192\n"
      "v_cvt_pk_bf16_f32 v78, v180, v181\n"
      "v_mfma_f32_16x16x32_bf16 a[140:143], a[84:87], v[24:27], a[140:143]\n"
      "v_exp_f32 v183, v183\n"
      "v_mfma_f32_16x16x32_bf16 a[148:151], a[84:87], v[28:31], a[148:151]\n"
      "ds_read_b64_tr_b16 a[74:75], v255 offset:10240\n"
      "ds_read_b128 a[76:79], v243 offset:28672\n"
      "v_mfma_f32_16x16x32_bf16 a[156:159], a[84:87], v[32:35], a[156:159]\n"
      "s_waitcnt lgkmcnt(6)\n"
      "v_exp_f32 v188, v188\n"
      "v_mfma_f32_16x16x32_bf16 v[192:195], a[88:91], a[12:15], v[192:195]\n"
      "s_mov_b32 s59, 5120\n"
      "v_cvt_pk_bf16_f32 v79, v182, v183\n"
      "v_add_u32 v240, 4294901760, v240\n"
      "v_mfma_f32_16x16x32_bf16 v[200:203], a[88:91], a[28:31], v[200:203]\n"
      "v_exp_f32 v189, v189\n"
      "v_mfma_f32_16x16x32_bf16 v[208:211], a[88:91], a[44:47], v[208:211]\n"
      "v_exp_f32 v190, v190\n"
      "v_mfma_f32_16x16x32_bf16 a[100:103], a[112:115], v[64:67], a[100:103]\n"
      "s_waitcnt lgkmcnt(5)\n"
      "v_add_u32 v241, 4294901760, v241\n"
      "v_add_u32 v242, 4294901760, v242\n"
      "v_add_u32 v243, 4294901760, v243\n"
      "v_mfma_f32_16x16x32_bf16 v[196:199], a[92:95], a[0:3], v[224:227]\n"
      "v_add_u32 v244, 4294901760, v244\n"
      "v_add_u32 v245, 4294901760, v245\n"
      "v_add_u32 v246, 4294901760, v246\n"
      "v_mfma_f32_16x16x32_bf16 v[204:207], a[92:95], a[16:19], v[228:231]\n"
      "v_add_u32 v247, 4294901760, v247\n"
      "v_cvt_pk_bf16_f32 v82, v188, v189\n"
      "v_mfma_f32_16x16x32_bf16 v[212:215], a[92:95], a[32:35], v[232:235]\n"
      "s_waitcnt lgkmcnt(4)\n"
      "v_exp_f32 v191, v191\n"
      "s_nop 0\n"
      "v_cvt_pk_bf16_f32 v83, v190, v191\n"
      "v_mfma_f32_16x16x32_bf16 v[196:199], a[64:67], a[4:7], v[196:199]\n"
      "buffer_load_dword a200, v152, %[rWo], s59 offen\n"
      "v_mfma_f32_16x16x32_bf16 v[204:207], a[64:67], a[20:23], v[204:207]\n"
      "v_exp_f32 v192, v192\n"
      "v_mfma_f32_16x16x32_bf16 v[212:215], a[64:67], a[36:39], v[212:215]\n"
      "s_waitcnt vmcnt(11) lgkmcnt(0)\n"
      "s_barrier\n"
      "v_exp_f32 v193, v193\n"
      "v_mfma_f32_16x16x32_bf16 a[104:107], a[112:115], v[68:71], a[104:107]\n"
      "s_add_u32 m0, s43, 65536\n"
      "v_exp_f32 v194, v194\n"
      "v_cvt_pk_bf16_f32 v84, v192, v193\n"
      "v_mfma_f32_16x16x32_bf16 v[196:199], a[68:71], a[8:11], v[196:199]\n"
      "s_mov_b32 s46, 65536\n"
      "ds_read_b128 a[80:83], v240 offset:32768\n"
      "v_exp_f32 v195, v195\n"
      "v_mfma_f32_16x16x32_bf16 v[204:207], a[68:71], a[24:27], v[204:207]\n"
      "ds_read_b64_tr_b16 a[84:85], v252 offset:12288\n"
      "ds_read_b64_tr_b16 a[86:87], v252 offset:14336\n"
      "ds_read_b128 a[88:91], v241 offset:32768\n"
      "v_mfma_f32_16x16x32_bf16 v[212:215], a[68:71], a[40:43], v[212:215]\n"
      "v_exp_f32 v200, v200\n"
      "v_cvt_pk_bf16_f32 v85, v194, v195\n"
      "v_mfma_f32_16x16x32_bf16 a[144:147], a[72:75], v[24:27], a[144:147]\n"
      "buffer_load_dwordx4 v249, %[rV], s46 offen nt lds\n"
      "v_exp_f32 v201, v201\n"
      "v_mfma_f32_16x16x32_bf16 a[152:155], a[72:75], v[28:31], a[152:155]\n"
      "ds_read_b128 a[92:95], v242 offset:32768\n"
      "v_exp_f32 v202, v202\n"
      "v_mfma_f32_16x16x32_bf16 a[160:163], a[72:75], v[32:35], a[160:163]\n"
      "ds_read_b64_tr_b16 a[64:65], v253 offset:12288\n"
      "v_cvt_pk_bf16_f32 v88, v200, v201\n"
      "v_mfma_f32_16x16x32_bf16 v[196:199], a[76:79], a[12:15], v[196:199]\n"
      "v_exp_f32 v203, v203\n"
      "v_mfma_f32_16x16x32_bf16 v[204:207], a[76:79], a[28:31], v[204:207]\n"
      "v_exp_f32 v208, v208\n"
      "v_cvt_pk_bf16_f32 v89, v202, v203\n"
      "v_mfma_f32_16x16x32_bf16 v[212:215], a[76:79], a[44:47], v[212:215]\n"
      "ds_read_b64_tr_b16 a[66:67], v253 offset:14336\n"
      "ds_read_b128 a[68:71], v243 offset:32768\n"
      "v_mfma_f32_16x16x32_bf16 a[108:111], a[112:115], v[216:219], a[108:111]\n"
      "s_waitcnt lgkmcnt(7)\n"
      "v_exp_f32 v209, v209\n"
      "v_mfma_f32_16x16x32_bf16 v[168:171], a[80:83], a[0:3], v[224:227]\n"
      "v_exp_f32 v210, v210\n"
      "v_cvt_pk_bf16_f32 v92, v208, v209\n"
      "v_mfma_f32_16x16x32_bf16 v[176:179], a[80:83], a[16:19], v[228:231]\n"
      "v_exp_f32 v211, v211\n"
      "v_mfma_f32_16x16x32_bf16 v[184:187], a[80:83], a[32:35], v[232:235]\n"
      "s_waitcnt lgkmcnt(5)\n"
      "ds_read_b128 a[72:75], v240 offset:36864\n"
      "v_cvt_pk_bf16_f32 v93, v210, v211\n"
      "v_mfma_f32_16x16x32_bf16 a[116:119], a[84:87], v[36:39], a[116:119]\n"
      "s_add_u32 m0, s43, 69632\n"
      "v_exp_f32 v196, v196\n"
      "v_mfma_f32_16x16x32_bf16 a[124:127], a[84:87], v[40:43], a[124:127]\n"
      "s_mov_b32 s46, 73728\n"
      "v_exp_f32 v197, v197\n"
      "v_mfma_f32_16x16x32_bf16 a[132:135], a[84:87], v[44:47], a[132:135]\n"
      "s_sleep 16\n"
      "s_waitcnt lgkmcnt(5)\n"
      "v_exp_f32 v198, v198\n"
      "v_mfma_f32_16x16x32_bf16 v[168:171], a[88:91], a[4:7], v[168:171]\n"
      "ds_read_b128 a[76:79], v241 offset:36864\n"
      "v_cvt_pk_bf16_f32 v86, v196, v197\n"
      "v_mfma_f32_16x16x32_bf16 v[176:179], a[88:91], a[20:23], v[176:179]\n"
      "v_exp_f32 v199, v199\n"
      "v_mfma_f32_16x16x32_bf16 v[184:187], a[88:91], a[36:39], v[184:187]\n"
      "v_exp_f32 v204, v204\n"
      "v_mfma_f32_16x16x32_bf16 a[96:99], a[112:115], v[72:75], a[96:99]\n"
      "s_waitcnt lgkmcnt(5)\n"
      "buffer_load_dwordx4 v249, %[rV], s46 offen nt lds\n"
      "v_cvt_pk_bf16_f32 v87, v198, v199\n"
      "v_mfma_f32_16x16x32_bf16 v[168:171], a[92:95], a[8:11], v[168:171]\n"
      "v_exp_f32 v205, v205\n"
      "v_mfma_f32_16x16x32_bf16 v[176:179], a[92:95], a[24:27], v[176:179]\n"
      "ds_read_b128 a[80:83], v242 offset:36864\n"
      "ds_read_b64_tr_b16 a[84:85], v254 offset:12288\n"
      "v_mfma_f32_16x16x32_bf16 v[184:187], a[92:95], a[40:43], v[184:187]\n"
      "s_waitcnt lgkmcnt(5)\n"
      "v_exp_f32 v206, v206\n"
      "v_mfma_f32_16x16x32_bf16 a[120:123], a[64:67], v[36:39], a[120:123]\n"
      "ds_read_b64_tr_b16 a[86:87], v254 offset:14336\n"
      "v_cvt_pk_bf16_f32 v90, v204, v205\n"
      "v_mfma_f32_16x16x32_bf16 a[128:131], a[64:67], v[40:43], a[128:131]\n"
      "v_exp_f32 v207, v207\n"
      "v_mfma_f32_16x16x32_bf16 a[136:139], a[64:67], v[44:47], a[136:139]\n"
      "s_waitcnt lgkmcnt(5)\n"
      "v_exp_f32 v212, v212\n"
      "v_mfma_f32_16x16x32_bf16 v[168:171], a[68:71], a[12:15], v[168:171]\n"
      "ds_read_b128 a[88:91], v243 offset:36864\n"
      "v_cvt_pk_bf16_f32 v91, v206, v207\n"
      "v_mfma_f32_16x16x32_bf16 v[176:179], a[68:71], a[28:31], v[176:179]\n"
      "s_mov_b32 s59, 6144\n"
      "v_exp_f32 v213, v213\n"
      "v_mfma_f32_16x16x32_bf16 v[184:187], a[68:71], a[44:47], v[184:187]\n"
      "v_exp_f32 v214, v214\n"
      "v_mfma_f32_16x16x32_bf16 a[100:103], a[112:115], v[76:79], a[100:103]\n"
      "s_waitcnt lgkmcnt(5)\n"
      "ds_read_b128 a[92:95], v240 offset:40960\n"
      "v_cvt_pk_bf16_f32 v94, v212, v213\n"
      "v_mfma_f32_16x16x32_bf16 v[172:175], a[72:75], a[0:3], v[224:227]\n"
      "s_add_u32 m0, s43, 73728\n"
      "ds_read_b128 a[168:171], v244 offset:32768\n"
      "buffer_load_dword a200, v152, %[rWo], s59 offen\n"
      "v_mfma_f32_16x16x32_bf16 v[180:183], a[72:75], a[16:19], v[228:231]\n"
      "s_mov_b32 s46, 81920\n"
      "v_exp_f32 v215, v215\n"
      "v_mfma_f32_16x16x32_bf16 v[188:191], a[72:75], a[32:35], v[232:235]\n"
      "s_waitcnt lgkmcnt(6)\n"
      "ds_read_b64_tr_b16 a[64:65], v255 offset:12288\n"
      "v_cvt_pk_bf16_f32 v95, v214, v215\n"
      "v_mfma_f32_16x16x32_bf16 v[172:175], a[76:79], a[4:7], v[172:175]\n"
      "v_exp_f32 v168, v168\n"
      "v_mfma_f32_16x16x32_bf16 v[180:183], a[76:79], a[20:23], v[180:183]\n"
      "ds_read_b64_tr_b16 a[66:67], v255 offset:14336\n"
      "v_exp_f32 v169, v169\n"
      "v_mfma_f32_16x16x32_bf16 v[188:191], a[76:79], a[36:39], v[188:191]\n"
      "v_exp_f32 v170, v170\n"
      "v_mfma_f32_16x16x32_bf16 a[104:107], a[112:115], v[80:83], a[104:107]\n"
      "s_waitcnt lgkmcnt(7)\n"
      "buffer_load_dwordx4 v249, %[rV], s46 offen nt lds\n"
      "v_cvt_pk_bf16_f32 v96, v168, v169\n"
      "v_mfma_f32_16x16x32_bf16 v[172:175], a[80:83], a[8:11], v[172:175]\n"
      "ds_read_b128 a[68:71], v241 offset:40960\n"
      "v_exp_f32 v171, v171\n"
      "v_mfma_f32_16x16x32_bf16 v[180:183], a[80:83], a[24:27], v[180:183]\n"
      "ds_read_b128 a[172:175], v245 offset:32768\n"
      "v_exp_f32 v176, v176\n"
      "v_mfma_f32_16x16x32_bf16 v[188:191], a[80:83], a[40:43], v[188:191]\n"
      "s_waitcnt lgkmcnt(7)\n"
      "ds_read_b128 a[72:75], v242 offset:40960\n"
      "v_cvt_pk_bf16_f32 v97, v170, v171\n"
      "v_mfma_f32_16x16x32_bf16 a[140:143], a[84:87], v[36:39], a[140:143]\n"
      "v_exp_f32 v177, v177\n"
      "v_mfma_f32_16x16x32_bf16 a[148:151], a[84:87], v[40:43], a[148:151]\n"
      "v_exp_f32 v178, v178\n"
      "v_cvt_pk_bf16_f32 v100, v176, v177\n"
      "v_mfma_f32_16x16x32_bf16 a[156:159], a[84:87], v[44:47], a[156:159]\n"
      "s_waitcnt lgkmcnt(7)\n"
      "ds_read_b128 a[176:179], v246 offset:32768\n"
      "v_exp_f32 v179, v179\n"
      "v_mfma_f32_16x16x32_bf16 v[172:175], a[88:91], a[12:15], v[172:175]\n"
      "v_exp_f32 v184, v184\n"
      "v_mfma_f32_16x16x32_bf16 v[180:183], a[88:91], a[28:31], v[180:183]\n"
      "ds_read_b64_tr_b16 a[76:77], v252 offset:16384\n"
      "v_cvt_pk_bf16_f32 v101, v178, v179\n"
      "v_mfma_f32_16x16x32_bf16 v[188:191], a[88:91], a[44:47], v[188:191]\n"
      "s_waitcnt lgkmcnt(8)\n"
      "v_exp_f32 v185, v185\n"
      "v_mfma_f32_16x16x32_bf16 v[192:195], a[92:95], a[0:3], v[224:227]\n"
      "s_add_u32 m0, s43, 77824\n"
      "v_exp_f32 v186, v186\n"
      "v_cvt_pk_bf16_f32 v104, v184, v185\n"
      "v_mfma_f32_16x16x32_bf16 v[200:203], a[92:95], a[16:19], v[228:231]\n"
      "s_mov_b32 s46, 90112\n"
      "ds_read_b64_tr_b16 a[78:79], v252 offset:18432\n"
      "ds_read_b128 a[80:83], v243 offset:40960\n"
      "v_mfma_f32_16x16x32_bf16 v[208:211], a[92:95], a[32:35], v[232:235]\n"
      "s_waitcnt lgkmcnt(9)\n"
      "v_exp_f32 v187, v187\n"
      "s_nop 0\n"
      "v_cvt_pk_bf16_f32 v105, v186, v187\n"
      "v_mfma_f32_16x16x32_bf16 v[216:219], a[168:171], a[48:51], v[236:239]\n"
      "s_waitcnt lgkmcnt(7)\n"
      "ds_read_b128 a[180:183], v247 offset:32768\n"
      "v_exp_f32 v172, v172\n"
      "v_mfma_f32_16x16x32_bf16 a[144:147], a[64:67], v[36:39], a[144:147]\n"
      "v_exp_f32 v173, v173\n"
      "v_mfma_f32_16x16x32_bf16 a[152:155], a[64:67], v[40:43], a[152:155]\n"
      "v_exp_f32 v174, v174\n"
      "v_mfma_f32_16x16x32_bf16 a[160:163], a[64:67], v[44:47], a[160:163]\n"
      "s_waitcnt lgkmcnt(7)\n"
      "buffer_load_dwordx4 v249, %[rV], s46 offen nt lds\n"
      "v_cvt_pk_bf16_f32 v98, v172, v173\n"
      "v_mfma_f32_16x16x32_bf16 v[192:195], a[68:71], a[4:7], v[192:195]\n"
      "v_exp_f32 v175, v175\n"
      "v_mfma_f32_16x16x32_bf16 v[200:203], a[68:71], a[20:23], v[200:203]\n"
      "ds_read_b128 a[84:87], v240 offset:45056\n"
      "ds_read_b128 a[184:187], v244 offset:36864\n"
      "v_mfma_f32_16x16x32_bf16 v[208:211], a[68:71], a[36:39], v[208:211]\n"
      "s_waitcnt lgkmcnt(8)\n"
      "v_exp_f32 v180, v180\n"
      "v_mfma_f32_16x16x32_bf16 v[216:219], a[172:175], a[52:55], v[216:219]\n"
      "ds_read_b128 a[88:91], v241 offset:45056\n"
      "v_cvt_pk_bf16_f32 v99, v174, v175\n"
      "v_mfma_f32_16x16x32_bf16 a[96:99], a[112:115], v[84:87], a[96:99]\n"
      "s_waitcnt lgkmcnt(8)\n"
      "v_exp_f32 v181, v181\n"
      "v_mfma_f32_16x16x32_bf16 v[192:195], a[72:75], a[8:11], v[192:195]\n"
      "ds_read_b128 a[188:191], v245 offset:36864\n"
      "ds_read_b128 a[92:95], v242 offset:45056\n"
      "v_mfma_f32_16x16x32_bf16 v[200:203], a[72:75], a[24:27], v[200:203]\n"
      "v_exp_f32 v182, v182\n"
      "v_mfma_f32_16x16x32_bf16 v[208:211], a[72:75], a[40:43], v[208:211]\n"
      "s_waitcnt lgkmcnt(9)\n"
      "ds_read_b128 a[192:195], v246 offset:36864\n"
      "v_cvt_pk_bf16_f32 v102, v180, v181\n"
      "v_mfma_f32_16x16x32_bf16 v[216:219], a[176:179], a[56:59], v[216:219]\n"
      "s_waitcnt lgkmcnt(8)\n"
      "v_exp_f32 v183, v183\n"
      "v_mfma_f32_16x16x32_bf16 a[116:119], a[76:79], v[48:51], a[116:119]\n"
      "s_add_u32 m0, s43, 81920\n"
      "v_exp_f32 v188, v188\n"
      "v_mfma_f32_16x16x32_bf16 a[124:127], a[76:79], v[52:55], a[124:127]\n"
      "s_mov_b32 s46, 98304\n"
      "ds_read_b64_tr_b16 a[64:65], v253 offset:16384\n"
      "v_cvt_pk_bf16_f32 v103, v182, v183\n"
      "v_mfma_f32_16x16x32_bf16 a[132:135], a[76:79], v[56:59], a[132:135]\n"
      "s_waitcnt lgkmcnt(8)\n"
      "v_exp_f32 v189, v189\n"
      "v_mfma_f32_16x16x32_bf16 v[192:195], a[80:83], a[12:15], v[192:195]\n"
      "ds_read_b64_tr_b16 a[66:67], v253 offset:18432\n"
      "buffer_load_dwordx4 v249, %[rV], s46 offen nt lds\n"
      "v_mfma_f32_16x16x32_bf16 v[200:203], a[80:83], a[28:31], v[200:203]\n"
      "s_mov_b32 s59, 7168\n"
      "v_exp_f32 v190, v190\n"
      "v_mfma_f32_16x16x32_bf16 v[208:211], a[80:83], a[44:47], v[208:211]\n"
      "s_waitcnt lgkmcnt(8)\n"
      "ds_read_b128 a[68:71], v243 offset:45056\n"
      "v_cvt_pk_bf16_f32 v106, v188, v189\n"
      "v_mfma_f32_16x16x32_bf16 v[216:219], a[180:183], a[60:63], v[216:219]\n"
      "v_exp_f32 v191, v191\n"
      "v_mfma_f32_16x16x32_bf16 a[100:103], a[112:115], v[88:91], a[100:103]\n"
      "s_waitcnt lgkmcnt(8)\n"
      "ds_read_b128 a[196:199], v247 offset:36864\n"
      "v_cvt_pk_bf16_f32 v107, v190, v191\n"
      "v_mfma_f32_16x16x32_bf16 v[196:199], a[84:87], a[0:3], v[224:227]\n"
      "buffer_load_dword a200, v152, %[rWo], s59 offen\n"
      "ds_read_b128 a[72:75], v240 offset:49152\n"
      "v_mfma_f32_16x16x32_bf16 v[204:207], a[84:87], a[16:19], v[228:231]\n"
      "v_exp_f32 v192, v192\n"
      "v_mfma_f32_16x16x32_bf16 v[212:215], a[84:87], a[32:35], v[232:235]\n"
      "s_waitcnt lgkmcnt(9)\n"
      "v_exp_f32 v193, v193\n"
      "v_mfma_f32_16x16x32_bf16 v[220:223], a[184:187], a[48:51], v[236:239]\n"
      "s_waitcnt lgkmcnt(8)\n"
      "v_exp_f32 v194, v194\n"
      "v_mfma_f32_16x16x32_bf16 v[196:199], a[88:91], a[4:7], v[196:199]\n"
      "ds_read_b64_tr_b16 a[76:77], v254 offset:16384\n"
      "v_cvt_pk_bf16_f32 v108, v192, v193\n"
      "v_mfma_f32_16x16x32_bf16 v[204:207], a[88:91], a[20:23], v[204:207]\n"
      "v_exp_f32 v195, v195\n"
      "v_mfma_f32_16x16x32_bf16 v[212:215], a[88:91], a[36:39], v[212:215]\n"
      "s_waitcnt lgkmcnt(8)\n"
      "v_exp_f32 v200, v200\n"
      "v_mfma_f32_16x16x32_bf16 v[220:223], a[188:191], a[52:55], v[220:223]\n"
      "ds_read_b64_tr_b16 a[78:79], v254 offset:18432\n"
      "v_cvt_pk_bf16_f32 v109, v194, v195\n"
      "v_mfma_f32_16x16x32_bf16 a[104:107], a[112:115], v[92:95], a[104:107]\n"
      "s_waitcnt lgkmcnt(8)\n"
      "v_exp_f32 v201, v201\n"
      "v_mfma_f32_16x16x32_bf16 v[196:199], a[92:95], a[8:11], v[196:199]\n"
      "s_add_u32 m0, s43, 86016\n"
      "v_exp_f32 v202, v202\n"
      "v_mfma_f32_16x16x32_bf16 v[204:207], a[92:95], a[24:27], v[204:207]\n"
      "s_mov_b32 s46, 106496\n"
      "ds_read_b128 a[80:83], v241 offset:49152\n"
      "v_cvt_pk_bf16_f32 v112, v200, v201\n"
      "v_mfma_f32_16x16x32_bf16 v[212:215], a[92:95], a[40:43], v[212:215]\n"
      "s_waitcnt lgkmcnt(8)\n"
      "v_exp_f32 v203, v203\n"
      "v_mfma_f32_16x16x32_bf16 v[220:223], a[192:195], a[56:59], v[220:223]\n"
      "s_waitcnt lgkmcnt(6)\n"
      "v_exp_f32 v208, v208\n"
      "v_mfma_f32_16x16x32_bf16 a[120:123], a[64:67], v[48:51], a[120:123]\n"
      "ds_read_b128 a[84:87], v242 offset:49152\n"
      "v_cvt_pk_bf16_f32 v113, v202, v203\n"
      "v_mfma_f32_16x16x32_bf16 a[128:131], a[64:67], v[52:55], a[128:131]\n"
      "v_exp_f32 v209, v209\n"
      "v_mfma_f32_16x16x32_bf16 a[136:139], a[64:67], v[56:59], a[136:139]\n"
      "s_waitcnt lgkmcnt(6)\n"
      "buffer_load_dwordx4 v249, %[rV], s46 offen nt lds\n"
      "v_exp_f32 v210, v210\n"
      "v_mfma_f32_16x16x32_bf16 v[196:199], a[68:71], a[12:15], v[196:199]\n"
      "ds_read_b64_tr_b16 a[88:89], v255 offset:16384\n"
      "v_cvt_pk_bf16_f32 v116, v208, v209\n"
      "v_mfma_f32_16x16x32_bf16 v[204:207], a[68:71], a[28:31], v[204:207]\n"
      "ds_read_b64_tr_b16 a[90:91], v255 offset:18432\n"
      "v_exp_f32 v211, v211\n"
      "v_mfma_f32_16x16x32_bf16 v[212:215], a[68:71], a[44:47], v[212:215]\n"
      "s_waitcnt lgkmcnt(7)\n"
      "v_exp_f32 v216, v216\n"
      "v_cvt_pk_bf16_f32 v117, v210, v211\n"
      "v_mfma_f32_16x16x32_bf16 v[220:223], a[196:199], a[60:63], v[220:223]\n"
      "s_waitcnt lgkmcnt(6)\n"
      "v_exp_f32 v217, v217\n"
      "v_mfma_f32_16x16x32_bf16 v[168:171], a[72:75], a[0:3], v[224:227]\n"
      "ds_read_b128 a[92:95], v243 offset:49152\n"
      "v_exp_f32 v218, v218\n"
      "v_mfma_f32_16x16x32_bf16 v[176:179], a[72:75], a[16:19], v[228:231]\n"
      "ds_read_b128 a[64:67], v240 offset:53248\n"
      "v_cvt_pk_bf16_f32 v216, v216, v217\n"
      "v_mfma_f32_16x16x32_bf16 v[184:187], a[72:75], a[32:35], v[232:235]\n"
      "s_waitcnt lgkmcnt(6)\n"
      "v_exp_f32 v219, v219\n"
      "s_nop 0\n"
      "v_cvt_pk_bf16_f32 v217, v218, v219\n"
      "v_mfma_f32_16x16x32_bf16 a[140:143], a[76:79], v[48:51], a[140:143]\n"
      "v_exp_f32 v196, v196\n"
      "v_mfma_f32_16x16x32_bf16 a[148:151], a[76:79], v[52:55], a[148:151]\n"
      "v_exp_f32 v197, v197\n"
      "v_mfma_f32_16x16x32_bf16 a[156:159], a[76:79], v[56:59], a[156:159]\n"
      "s_waitcnt lgkmcnt(5)\n"
      "v_exp_f32 v198, v198\n"
      "v_cvt_pk_bf16_f32 v110, v196, v197\n"
      "v_mfma_f32_16x16x32_bf16 v[168:171], a[80:83], a[4:7], v[168:171]\n"
      "s_add_u32 m0, s43, 90112\n"
      "v_exp_f32 v199, v199\n"
      "v_mfma_f32_16x16x32_bf16 v[176:179], a[80:83], a[20:23], v[176:179]\n"
      "s_mov_b32 s46, 114688\n"
      "ds_read_b128 a[68:71], v241 offset:53248\n"
      "v_exp_f32 v204, v204\n"
      "v_mfma_f32_16x16x32_bf16 v[184:187], a[80:83], a[36:39], v[184:187]\n"
      "ds_read_b128 a[72:75], v242 offset:53248\n"
      "v_cvt_pk_bf16_f32 v111, v198, v199\n"
      "v_mfma_f32_16x16x32_bf16 a[96:99], a[112:115], v[96:99], a[96:99]\n"
      "s_waitcnt lgkmcnt(6)\n"
      "v_exp_f32 v205, v205\n"
      "v_mfma_f32_16x16x32_bf16 v[168:171], a[84:87], a[8:11], v[168:171]\n"
      "v_exp_f32 v206, v206\n"
      "v_cvt_pk_bf16_f32 v114, v204, v205\n"
      "v_mfma_f32_16x16x32_bf16 v[176:179], a[84:87], a[24:27], v[176:179]\n"
      "v_exp_f32 v207, v207\n"
      "v_mfma_f32_16x16x32_bf16 v[184:187], a[84:87], a[40:43], v[184:187]\n"
      "s_waitcnt lgkmcnt(4)\n"
      "buffer_load_dwordx4 v249, %[rV], s46 offen nt lds\n"
      "ds_read_b64_tr_b16 a[76:77], v252 offset:20480\n"
      "v_mfma_f32_16x16x32_bf16 a[144:147], a[88:91], v[48:51], a[144:147]\n"
      "v_exp_f32 v212, v212\n"
      "v_cvt_pk_bf16_f32 v115, v206, v207\n"
      "v_mfma_f32_16x16x32_bf16 a[152:155], a[88:91], v[52:55], a[152:155]\n"
      "ds_read_b64_tr_b16 a[78:79], v252 offset:22528\n"
      "v_exp_f32 v213, v213\n"
      "v_mfma_f32_16x16x32_bf16 a[160:163], a[88:91], v[56:59], a[160:163]\n"
      "s_waitcnt lgkmcnt(5)\n"
      "v_exp_f32 v214, v214\n"
      "v_cvt_pk_bf16_f32 v118, v212, v213\n"
      "v_mfma_f32_16x16x32_bf16 v[168:171], a[92:95], a[12:15], v[168:171]\n"
      "v_exp_f32 v215, v215\n"
      "v_mfma_f32_16x16x32_bf16 v[176:179], a[92:95], a[28:31], v[176:179]\n"
      "ds_read_b128 a[80:83], v243 offset:53248\n"
      "v_exp_f32 v220, v220\n"
      "v_mfma_f32_16x16x32_bf16 v[184:187], a[92:95], a[44:47], v[184:187]\n"
      "ds_read_b128 a[84:87], v240 offset:57344\n"
      "v_cvt_pk_bf16_f32 v119, v214, v215\n"
      "v_mfma_f32_16x16x32_bf16 a[100:103], a[112:115], v[100:103], a[100:103]\n"
      "s_waitcnt lgkmcnt(6)\n"
      "v_exp_f32 v221, v221\n"
      "v_mfma_f32_16x16x32_bf16 v[172:175], a[64:67], a[0:3], v[224:227]\n"
      "v_exp_f32 v222, v222\n"
      "v_cvt_pk_bf16_f32 v218, v220, v221\n"
      "v_mfma_f32_16x16x32_bf16 v[180:183], a[64:67], a[16:19], v[228:231]\n"
      "s_add_u32 m0, s43, 94208\n"
      "v_exp_f32 v223, v223\n"
      "v_mfma_f32_16x16x32_bf16 v[188:191], a[64:67], a[32:35], v[232:235]\n"
      "s_waitcnt lgkmcnt(5)\n"
      "ds_read_b64_tr_b16 a[88:89], v253 offset:20480\n"
      "v_cvt_pk_bf16_f32 v219, v222, v223\n"
      "v_mfma_f32_16x16x32_bf16 v[172:175], a[68:71], a[4:7], v[172:175]\n"
      "s_mov_b32 s46, 122880\n"
      "ds_write_b128 v251, v[216:219] offset:8192\n"
      "ds_read_b64_tr_b16 a[90:91], v253 offset:22528\n"
      "v_mfma_f32_16x16x32_bf16 v[180:183], a[68:71], a[20:23], v[180:183]\n"
      "v_exp_f32 v168, v168\n"
      "v_mfma_f32_16x16x32_bf16 v[188:191], a[68:71], a[36:39], v[188:191]\n"
      "v_exp_f32 v169, v169\n"
      "v_mfma_f32_16x16x32_bf16 a[104:107], a[112:115], v[104:107], a[104:107]\n"
      "s_waitcnt lgkmcnt(7)\n"
      "v_exp_f32 v170, v170\n"
      "v_cvt_pk_bf16_f32 v120, v168, v169\n"
      "v_mfma_f32_16x16x32_bf16 v[172:175], a[72:75], a[8:11], v[172:175]\n"
      "ds_read_b128 a[92:95], v241 offset:57344\n"
      "v_exp_f32 v171, v171\n"
      "v_mfma_f32_16x16x32_bf16 v[180:183], a[72:75], a[24:27], v[180:183]\n"
      "buffer_load_dwordx4 v249, %[rV], s46 offen nt lds\n"
      "v_exp_f32 v176, v176\n"
      "v_mfma_f32_16x16x32_bf16 v[188:191], a[72:75], a[40:43], v[188:191]\n"
      "s_waitcnt lgkmcnt(6)\n"
      "ds_read_b128 a[64:67], v242 offset:57344\n"
      "v_cvt_pk_bf16_f32 v121, v170, v171\n"
      "v_mfma_f32_16x16x32_bf16 a[116:119], a[76:79], v[60:63], a[116:119]\n"
      "v_exp_f32 v177, v177\n"
      "v_mfma_f32_16x16x32_bf16 a[124:127], a[76:79], v[64:67], a[124:127]\n"
      "v_exp_f32 v178, v178\n"
      "v_cvt_pk_bf16_f32 v124, v176, v177\n"
      "v_mfma_f32_16x16x32_bf16 a[132:135], a[76:79], v[68:71], a[132:135]\n"
      "s_waitcnt lgkmcnt(6)\n"
      "v_exp_f32 v179, v179\n"
      "v_mfma_f32_16x16x32_bf16 v[172:175], a[80:83], a[12:15], v[172:175]\n"
      "ds_read_b64_tr_b16 a[68:69], v254 offset:20480\n"
      "v_exp_f32 v184, v184\n"
      "v_mfma_f32_16x16x32_bf16 v[180:183], a[80:83], a[28:31], v[180:183]\n"
      "ds_read_b64_tr_b16 a[70:71], v254 offset:22528\n"
      "v_cvt_pk_bf16_f32 v125, v178, v179\n"
      "v_mfma_f32_16x16x32_bf16 v[188:191], a[80:83], a[44:47], v[188:191]\n"
      "s_waitcnt lgkmcnt(7)\n"
      "v_exp_f32 v185, v185\n"
      "v_mfma_f32_16x16x32_bf16 v[192:195], a[84:87], a[0:3], v[224:227]\n"
      "v_exp_f32 v186, v186\n"
      "v_cvt_pk_bf16_f32 v128, v184, v185\n"
      "v_mfma_f32_16x16x32_bf16 v[200:203], a[84:87], a[16:19], v[228:231]\n"
      "ds_read_b128 a[72:75], v243 offset:57344\n"
      "ds_read_b128 a[76:79], v240 offset:61440\n"
      "v_mfma_f32_16x16x32_bf16 v[208:211], a[84:87], a[32:35], v[232:235]\n"
      "s_waitcnt lgkmcnt(6)\n"
      "v_exp_f32 v187, v187\n"
      "s_nop 0\n"
      "v_cvt_pk_bf16_f32 v129, v186, v187\n"
      "v_mfma_f32_16x16x32_bf16 a[120:123], a[88:91], v[60:63], a[120:123]\n"
      "v_exp_f32 v172, v172\n"
      "v_mfma_f32_16x16x32_bf16 a[128:131], a[88:91], v[64:67], a[128:131]\n"
      "v_exp_f32 v173, v173\n"
      "v_mfma_f32_16x16x32_bf16 a[136:139], a[88:91], v[68:71], a[136:139]\n"
      "s_waitcnt lgkmcnt(5)\n"
      "v_exp_f32 v174, v174\n"
      "v_mfma_f32_16x16x32_bf16 v[192:195], a[92:95], a[4:7], v[192:195]\n"
      "ds_read_b128 a[80:83], v241 offset:61440\n"
      "v_cvt_pk_bf16_f32 v122, v172, v173\n"
      "v_mfma_f32_16x16x32_bf16 v[200:203], a[92:95], a[20:23], v[200:203]\n"
      "v_exp_f32 v175, v175\n"
      "v_mfma_f32_16x16x32_bf16 v[208:211], a[92:95], a[36:39], v[208:211]\n"
      "v_exp_f32 v180, v180\n"
      "v_mfma_f32_16x16x32_bf16 a[96:99], a[112:115], v[108:111], a[96:99]\n"
      "s_waitcnt lgkmcnt(5)\n"
      "ds_read_b128 a[84:87], v242 offset:61440\n"
      "v_cvt_pk_bf16_f32 v123, v174, v175\n"
      "v_mfma_f32_16x16x32_bf16 v[192:195], a[64:67], a[8:11], v[192:195]\n"
      "v_exp_f32 v181, v181\n"
      "v_mfma_f32_16x16x32_bf16 v[200:203], a[64:67], a[24:27], v[200:203]\n"
      "v_exp_f32 v182, v182\n"
      "v_mfma_f32_16x16x32_bf16 v[208:211], a[64:67], a[40:43], v[208:211]\n"
      "s_waitcnt lgkmcnt(4)\n"
      "ds_read_b64_tr_b16 a[88:89], v255 offset:20480\n"
      "v_cvt_pk_bf16_f32 v126, v180, v181\n"
      "v_mfma_f32_16x16x32_bf16 a[140:143], a[68:71], v[60:63], a[140:143]\n"
      "v_exp_f32 v183, v183\n"
      "v_mfma_f32_16x16x32_bf16 a[148:151], a[68:71], v[64:67], a[148:151]\n"
      "ds_read_b64_tr_b16 a[90:91], v255 offset:22528\n"
      "ds_read_b128 a[92:95], v243 offset:61440\n"
      "v_mfma_f32_16x16x32_bf16 a[156:159], a[68:71], v[68:71], a[156:159]\n"
      "s_waitcnt lgkmcnt(6)\n"
      "v_exp_f32 v188, v188\n"
      "v_mfma_f32_16x16x32_bf16 v[192:195], a[72:75], a[12:15], v[192:195]\n"
      "v_cvt_pk_bf16_f32 v127, v182, v183\n"
      "v_mfma_f32_16x16x32_bf16 v[200:203], a[72:75], a[28:31], v[200:203]\n"
      "v_exp_f32 v189, v189\n"
      "v_mfma_f32_16x16x32_bf16 v[208:211], a[72:75], a[44:47], v[208:211]\n"
      "v_exp_f32 v190, v190\n"
      "v_mfma_f32_16x16x32_bf16 a[100:103], a[112:115], v[112:115], a[100:103]\n"
      "s_waitcnt lgkmcnt(5)\n"
      "v_cvt_pk_bf16_f32 v130, v188, v189\n"
      "v_mfma_f32_16x16x32_bf16 v[196:199], a[76:79], a[0:3], v[224:227]\n"
      "v_exp_f32 v191, v191\n"
      "v_mfma_f32_16x16x32_bf16 v[204:207], a[76:79], a[16:19], v[228:231]\n"
      "v_cvt_pk_bf16_f32 v131, v190, v191\n"
      "v_mfma_f32_16x16x32_bf16 v[212:215], a[76:79], a[32:35], v[232:235]\n"
      "s_waitcnt lgkmcnt(4)\n"
      "v_exp_f32 v192, v192\n"
      "v_mfma_f32_16x16x32_bf16 v[196:199], a[80:83], a[4:7], v[196:199]\n"
      "v_exp_f32 v193, v193\n"
      "v_mfma_f32_16x16x32_bf16 v[204:207], a[80:83], a[20:23], v[204:207]\n"
      "v_exp_f32 v194, v194\n"
      "v_mfma_f32_16x16x32_bf16 v[212:215], a[80:83], a[36:39], v[212:215]\n"
      "s_waitcnt vmcnt(11) lgkmcnt(0)\n"
      "s_barrier\n"
      "ds_read_b128 a[64:67], v240 offset:0\n"
      "v_cvt_pk_bf16_f32 v132, v192, v193\n"
      "v_mfma_f32_16x16x32_bf16 a[104:107], a[112:115], v[116:119], a[104:107]\n"
      "s_add_u32 m0, s43, 32768\n"
      "ds_read_b64_tr_b16 a[68:69], v252 offset:24576\n"
      "v_exp_f32 v195, v195\n"
      "v_mfma_f32_16x16x32_bf16 v[196:199], a[84:87], a[8:11], v[196:199]\n"
      "s_mov_b32 s46, 128\n"
      "ds_read_b64_tr_b16 a[70:71], v252 offset:26624\n"
      "ds_read_b128 a[72:75], v241 offset:0\n"
      "v_mfma_f32_16x16x32_bf16 v[204:207], a[84:87], a[24:27], v[204:207]\n"
      "v_exp_f32 v200, v200\n"
      "v_mfma_f32_16x16x32_bf16 v[212:215], a[84:87], a[40:43], v[212:215]\n"
      "buffer_load_dwordx4 v249, %[rV], s46 offen nt lds\n"
      "v_cvt_pk_bf16_f32 v133, v194, v195\n"
      "v_mfma_f32_16x16x32_bf16 a[144:147], a[88:91], v[60:63], a[144:147]\n"
      "v_exp_f32 v201, v201\n"
      "v_mfma_f32_16x16x32_bf16 a[152:155], a[88:91], v[64:67], a[152:155]\n"
      "ds_read_b128 a[76:79], v242 offset:0\n"
      "v_exp_f32 v202, v202\n"
      "v_mfma_f32_16x16x32_bf16 a[160:163], a[88:91], v[68:71], a[160:163]\n"
      "ds_read_b64_tr_b16 a[80:81], v253 offset:24576\n"
      "v_cvt_pk_bf16_f32 v136, v200, v201\n"
      "v_mfma_f32_16x16x32_bf16 v[196:199], a[92:95], a[12:15], v[196:199]\n"
      "v_exp_f32 v203, v203\n"
      "v_mfma_f32_16x16x32_bf16 v[204:207], a[92:95], a[28:31], v[204:207]\n"
      "v_exp_f32 v208, v208\n"
      "v_cvt_pk_bf16_f32 v137, v202, v203\n"
      "v_mfma_f32_16x16x32_bf16 v[212:215], a[92:95], a[44:47], v[212:215]\n"
      "ds_read_b64_tr_b16 a[82:83], v253 offset:26624\n"
      "ds_read_b128 a[84:87], v243 offset:0\n"
      "v_mfma_f32_16x16x32_bf16 a[108:111], a[112:115], v[216:219], a[108:111]\n"
      "s_waitcnt lgkmcnt(7)\n"
      "v_exp_f32 v209, v209\n"
      "v_mfma_f32_16x16x32_bf16 v[168:171], a[64:67], a[0:3], v[224:227]\n"
      "v_exp_f32 v210, v210\n"
      "v_cvt_pk_bf16_f32 v140, v208, v209\n"
      "v_mfma_f32_16x16x32_bf16 v[176:179], a[64:67], a[16:19], v[228:231]\n"
      "v_exp_f32 v211, v211\n"
      "v_mfma_f32_16x16x32_bf16 v[184:187], a[64:67], a[32:35], v[232:235]\n"
      "s_waitcnt lgkmcnt(5)\n"
      "ds_read_b128 a[88:91], v240 offset:4096\n"
      "v_cvt_pk_bf16_f32 v141, v210, v211\n"
      "v_mfma_f32_16x16x32_bf16 a[116:119], a[68:71], v[72:75], a[116:119]\n"
      "s_add_u32 m0, s43, 36864\n"
      "v_exp_f32 v196, v196\n"
      "v_mfma_f32_16x16x32_bf16 a[124:127], a[68:71], v[76:79], a[124:127]\n"
      "s_mov_b32 s46, 8320\n"
      "v_exp_f32 v197, v197\n"
      "v_mfma_f32_16x16x32_bf16 a[132:135], a[68:71], v[80:83], a[132:135]\n"
      "s_waitcnt lgkmcnt(5)\n"
      "v_exp_f32 v198, v198\n"
      "v_mfma_f32_16x16x32_bf16 v[168:171], a[72:75], a[4:7], v[168:171]\n"
      "ds_read_b128 a[92:95], v241 offset:4096\n"
      "v_cvt_pk_bf16_f32 v134, v196, v197\n"
      "v_mfma_f32_16x16x32_bf16 v[176:179], a[72:75], a[20:23], v[176:179]\n"
      "v_exp_f32 v199, v199\n"
      "v_mfma_f32_16x16x32_bf16 v[184:187], a[72:75], a[36:39], v[184:187]\n"
      "v_exp_f32 v204, v204\n"
      "v_mfma_f32_16x16x32_bf16 a[96:99], a[112:115], v[120:123], a[96:99]\n"
      "s_waitcnt lgkmcnt(5)\n"
      "buffer_load_dwordx4 v249, %[rV], s46 offen nt lds\n"
      "v_cvt_pk_bf16_f32 v135, v198, v199\n"
      "v_mfma_f32_16x16x32_bf16 v[168:171], a[76:79], a[8:11], v[168:171]\n"
      "v_exp_f32 v205, v205\n"
      "v_mfma_f32_16x16x32_bf16 v[176:179], a[76:79], a[24:27], v[176:179]\n"
      "ds_read_b128 a[64:67], v242 offset:4096\n"
      "ds_read_b64_tr_b16 a[68:69], v254 offset:24576\n"
      "v_mfma_f32_16x16x32_bf16 v[184:187], a[76:79], a[40:43], v[184:187]\n"
      "s_waitcnt lgkmcnt(5)\n"
      "v_exp_f32 v206, v206\n"
      "v_mfma_f32_16x16x32_bf16 a[120:123], a[80:83], v[72:75], a[120:123]\n"
      "ds_read_b64_tr_b16 a[70:71], v254 offset:26624\n"
      "v_cvt_pk_bf16_f32 v138, v204, v205\n"
      "v_mfma_f32_16x16x32_bf16 a[128:131], a[80:83], v[76:79], a[128:131]\n"
      "v_exp_f32 v207, v207\n"
      "v_mfma_f32_16x16x32_bf16 a[136:139], a[80:83], v[80:83], a[136:139]\n"
      "s_waitcnt lgkmcnt(5)\n"
      "v_exp_f32 v212, v212\n"
      "v_mfma_f32_16x16x32_bf16 v[168:171], a[84:87], a[12:15], v[168:171]\n"
      "ds_read_b128 a[72:75], v243 offset:4096\n"
      "v_cvt_pk_bf16_f32 v139, v206, v207\n"
      "v_mfma_f32_16x16x32_bf16 v[176:179], a[84:87], a[28:31], v[176:179]\n"
      "v_exp_f32 v213, v213\n"
      "v_mfma_f32_16x16x32_bf16 v[184:187], a[84:87], a[44:47], v[184:187]\n"
      "v_exp_f32 v214, v214\n"
      "v_mfma_f32_16x16x32_bf16 a[100:103], a[112:115], v[124:127], a[100:103]\n"
      "s_waitcnt lgkmcnt(5)\n"
      "ds_read_b128 a[76:79], v240 offset:8192\n"
      "v_cvt_pk_bf16_f32 v142, v212, v213\n"
      "v_mfma_f32_16x16x32_bf16 v[172:175], a[88:91], a[0:3], v[224:227]\n"
      "s_add_u32 m0, s43, 40960\n"
      "ds_read_b128 a[168:171], v244 offset:0\n"
      "ds_read_b64_tr_b16 a[80:81], v255 offset:24576\n"
      "v_mfma_f32_16x16x32_bf16 v[180:183], a[88:91], a[16:19], v[228:231]\n"
      "s_mov_b32 s46, 16512\n"
      "v_exp_f32 v215, v215\n"
      "v_mfma_f32_16x16x32_bf16 v[188:191], a[88:91], a[32:35], v[232:235]\n"
      "s_waitcnt lgkmcnt(7)\n"
      "ds_read_b64_tr_b16 a[82:83], v255 offset:26624\n"
      "v_cvt_pk_bf16_f32 v143, v214, v215\n"
      "v_mfma_f32_16x16x32_bf16 v[172:175], a[92:95], a[4:7], v[172:175]\n"
      "v_exp_f32 v168, v168\n"
      "v_mfma_f32_16x16x32_bf16 v[180:183], a[92:95], a[20:23], v[180:183]\n"
      "v_exp_f32 v169, v169\n"
      "v_mfma_f32_16x16x32_bf16 v[188:191], a[92:95], a[36:39], v[188:191]\n"
      "v_exp_f32 v170, v170\n"
      "v_mfma_f32_16x16x32_bf16 a[104:107], a[112:115], v[128:131], a[104:107]\n"
      "s_waitcnt lgkmcnt(7)\n"
      "buffer_load_dwordx4 v249, %[rV], s46 offen nt lds\n"
      "v_cvt_pk_bf16_f32 v144, v168, v169\n"
      "v_mfma_f32_16x16x32_bf16 v[172:175], a[64:67], a[8:11], v[172:175]\n"
      "ds_read_b128 a[84:87], v241 offset:8192\n"
      "v_exp_f32 v171, v171\n"
      "v_mfma_f32_16x16x32_bf16 v[180:183], a[64:67], a[24:27], v[180:183]\n"
      "ds_read_b128 a[172:175], v245 offset:0\n"
      "v_exp_f32 v176, v176\n"
      "v_mfma_f32_16x16x32_bf16 v[188:191], a[64:67], a[40:43], v[188:191]\n"
      "s_waitcnt lgkmcnt(7)\n"
      "ds_read_b128 a[88:91], v242 offset:8192\n"
      "v_cvt_pk_bf16_f32 v145, v170, v171\n"
      "v_mfma_f32_16x16x32_bf16 a[140:143], a[68:71], v[72:75], a[140:143]\n"
      "v_exp_f32 v177, v177\n"
      "v_mfma_f32_16x16x32_bf16 a[148:151], a[68:71], v[76:79], a[148:151]\n"
      "v_exp_f32 v178, v178\n"
      "v_cvt_pk_bf16_f32 v148, v176, v177\n"
      "v_mfma_f32_16x16x32_bf16 a[156:159], a[68:71], v[80:83], a[156:159]\n"
      "s_waitcnt lgkmcnt(7)\n"
      "ds_read_b128 a[176:179], v246 offset:0\n"
      "v_exp_f32 v179, v179\n"
      "v_mfma_f32_16x16x32_bf16 v[172:175], a[72:75], a[12:15], v[172:175]\n"
      "v_exp_f32 v184, v184\n"
      "v_mfma_f32_16x16x32_bf16 v[180:183], a[72:75], a[28:31], v[180:183]\n"
      "ds_read_b64_tr_b16 a[92:93], v252 offset:28672\n"
      "v_cvt_pk_bf16_f32 v149, v178, v179\n"
      "v_mfma_f32_16x16x32_bf16 v[188:191], a[72:75], a[44:47], v[188:191]\n"
      "s_waitcnt lgkmcnt(8)\n"
      "v_exp_f32 v185, v185\n"
      "v_mfma_f32_16x16x32_bf16 v[192:195], a[76:79], a[0:3], v[224:227]\n"
      "s_add_u32 m0, s43, 45056\n"
      "v_exp_f32 v186, v186\n"
      "v_cvt_pk_bf16_f32 v152, v184, v185\n"
      "v_mfma_f32_16x16x32_bf16 v[200:203], a[76:79], a[16:19], v[228:231]\n"
      "s_mov_b32 s46, 24704\n"
      "ds_read_b64_tr_b16 a[94:95], v252 offset:30720\n"
      "ds_read_b128 a[64:67], v243 offset:8192\n"
      "v_mfma_f32_16x16x32_bf16 v[208:211], a[76:79], a[32:35], v[232:235]\n"
      "s_waitcnt lgkmcnt(9)\n"
      "v_exp_f32 v187, v187\n"
      "s_nop 0\n"
      "v_cvt_pk_bf16_f32 v153, v186, v187\n"
      "v_mfma_f32_16x16x32_bf16 v[216:219], a[168:171], a[48:51], v[236:239]\n"
      "s_waitcnt lgkmcnt(7)\n"
      "ds_read_b128 a[180:183], v247 offset:0\n"
      "v_exp_f32 v172, v172\n"
      "v_mfma_f32_16x16x32_bf16 a[144:147], a[80:83], v[72:75], a[144:147]\n"
      "v_exp_f32 v173, v173\n"
      "v_mfma_f32_16x16x32_bf16 a[152:155], a[80:83], v[76:79], a[152:155]\n"
      "v_exp_f32 v174, v174\n"
      "v_mfma_f32_16x16x32_bf16 a[160:163], a[80:83], v[80:83], a[160:163]\n"
      "s_waitcnt lgkmcnt(7)\n"
      "buffer_load_dwordx4 v249, %[rV], s46 offen nt lds\n"
      "v_cvt_pk_bf16_f32 v146, v172, v173\n"
      "v_mfma_f32_16x16x32_bf16 v[192:195], a[84:87], a[4:7], v[192:195]\n"
      "v_exp_f32 v175, v175\n"
      "v_mfma_f32_16x16x32_bf16 v[200:203], a[84:87], a[20:23], v[200:203]\n"
      "ds_read_b128 a[68:71], v240 offset:12288\n"
      "ds_read_b128 a[184:187], v244 offset:4096\n"
      "v_mfma_f32_16x16x32_bf16 v[208:211], a[84:87], a[36:39], v[208:211]\n"
      "s_waitcnt lgkmcnt(8)\n"
      "v_exp_f32 v180, v180\n"
      "v_mfma_f32_16x16x32_bf16 v[216:219], a[172:175], a[52:55], v[216:219]\n"
      "ds_read_b128 a[72:75], v241 offset:12288\n"
      "v_cvt_pk_bf16_f32 v147, v174, v175\n"
      "v_mfma_f32_16x16x32_bf16 a[96:99], a[112:115], v[132:135], a[96:99]\n"
      "s_waitcnt lgkmcnt(8)\n"
      "v_exp_f32 v181, v181\n"
      "v_mfma_f32_16x16x32_bf16 v[192:195], a[88:91], a[8:11], v[192:195]\n"
      "ds_read_b128 a[188:191], v245 offset:4096\n"
      "ds_read_b128 a[76:79], v242 offset:12288\n"
      "v_mfma_f32_16x16x32_bf16 v[200:203], a[88:91], a[24:27], v[200:203]\n"
      "v_exp_f32 v182, v182\n"
      "v_mfma_f32_16x16x32_bf16 v[208:211], a[88:91], a[40:43], v[208:211]\n"
      "s_waitcnt lgkmcnt(9)\n"
      "ds_read_b128 a[192:195], v246 offset:4096\n"
      "v_cvt_pk_bf16_f32 v150, v180, v181\n"
      "v_mfma_f32_16x16x32_bf16 v[216:219], a[176:179], a[56:59], v[216:219]\n"
      "s_waitcnt lgkmcnt(8)\n"
      "v_exp_f32 v183, v183\n"
      "v_mfma_f32_16x16x32_bf16 a[116:119], a[92:95], v[84:87], a[116:119]\n"
      "s_add_u32 m0, s43, 49152\n"
      "v_exp_f32 v188, v188\n"
      "v_mfma_f32_16x16x32_bf16 a[124:127], a[92:95], v[88:91], a[124:127]\n"
      "s_mov_b32 s46, 32896\n"
      "ds_read_b64_tr_b16 a[80:81], v253 offset:28672\n"
      "v_cvt_pk_bf16_f32 v151, v182, v183\n"
      "v_mfma_f32_16x16x32_bf16 a[132:135], a[92:95], v[92:95], a[132:135]\n"
      "s_waitcnt lgkmcnt(8)\n"
      "v_exp_f32 v189, v189\n"
      "v_mfma_f32_16x16x32_bf16 v[192:195], a[64:67], a[12:15], v[192:195]\n"
      "ds_read_b64_tr_b16 a[82:83], v253 offset:30720\n"
      "buffer_load_dwordx4 v249, %[rV], s46 offen nt lds\n"
      "v_mfma_f32_16x16x32_bf16 v[200:203], a[64:67], a[28:31], v[200:203]\n"
      "v_exp_f32 v190, v190\n"
      "v_mfma_f32_16x16x32_bf16 v[208:211], a[64:67], a[44:47], v[208:211]\n"
      "s_waitcnt lgkmcnt(8)\n"
      "ds_read_b128 a[84:87], v243 offset:12288\n"
      "v_cvt_pk_bf16_f32 v154, v188, v189\n"
      "v_mfma_f32_16x16x32_bf16 v[216:219], a[180:183], a[60:63], v[216:219]\n"
      "v_exp_f32 v191, v191\n"
      "v_mfma_f32_16x16x32_bf16 a[100:103], a[112:115], v[136:139], a[100:103]\n"
      "s_waitcnt lgkmcnt(8)\n"
      "ds_read_b128 a[196:199], v247 offset:4096\n"
      "v_cvt_pk_bf16_f32 v155, v190, v191\n"
      "v_mfma_f32_16x16x32_bf16 v[196:199], a[68:71], a[0:3], v[224:227]\n"
      "ds_read_b128 a[88:91], v240 offset:16384\n"
      "ds_read_b64_tr_b16 a[92:93], v254 offset:28672\n"
      "v_mfma_f32_16x16x32_bf16 v[204:207], a[68:71], a[16:19], v[228:231]\n"
      "v_exp_f32 v192, v192\n"
      "v_mfma_f32_16x16x32_bf16 v[212:215], a[68:71], a[32:35], v[232:235]\n"
      "s_waitcnt lgkmcnt(10)\n"
      "v_exp_f32 v193, v193\n"
      "v_mfma_f32_16x16x32_bf16 v[220:223], a[184:187], a[48:51], v[236:239]\n"
      "s_waitcnt lgkmcnt(9)\n"
      "v_exp_f32 v194, v194\n"
      "v_mfma_f32_16x16x32_bf16 v[196:199], a[72:75], a[4:7], v[196:199]\n"
      "ds_read_b64_tr_b16 a[94:95], v254 offset:30720\n"
      "v_cvt_pk_bf16_f32 v156, v192, v193\n"
      "v_mfma_f32_16x16x32_bf16 v[204:207], a[72:75], a[20:23], v[204:207]\n"
      "v_exp_f32 v195, v195\n"
      "v_mfma_f32_16x16x32_bf16 v[212:215], a[72:75], a[36:39], v[212:215]\n"
      "s_waitcnt lgkmcnt(9)\n"
      "v_exp_f32 v200, v200\n"
      "v_mfma_f32_16x16x32_bf16 v[220:223], a[188:191], a[52:55], v[220:223]\n"
      "ds_read_b128 a[64:67], v241 offset:16384\n"
      "v_cvt_pk_bf16_f32 v157, v194, v195\n"
      "v_mfma_f32_16x16x32_bf16 a[104:107], a[112:115], v[140:143], a[104:107]\n"
      "s_waitcnt lgkmcnt(9)\n"
      "v_exp_f32 v201, v201\n"
      "v_mfma_f32_16x16x32_bf16 v[196:199], a[76:79], a[8:11], v[196:199]\n"
      "s_add_u32 m0, s43, 53248\n"
      "v_exp_f32 v202, v202\n"
      "v_mfma_f32_16x16x32_bf16 v[204:207], a[76:79], a[24:27], v[204:207]\n"
      "s_mov_b32 s46, 41088\n"
      "ds_read_b128 a[68:71], v242 offset:16384\n"
      "v_cvt_pk_bf16_f32 v160, v200, v201\n"
      "v_mfma_f32_16x16x32_bf16 v[212:215], a[76:79], a[40:43], v[212:215]\n"
      "s_waitcnt lgkmcnt(9)\n"
      "v_exp_f32 v203, v203\n"
      "v_mfma_f32_16x16x32_bf16 v[220:223], a[192:195], a[56:59], v[220:223]\n"
      "s_waitcnt lgkmcnt(7)\n"
      "v_exp_f32 v208, v208\n"
      "v_mfma_f32_16x16x32_bf16 a[120:123], a[80:83], v[84:87], a[120:123]\n"
      "buffer_load_dwordx4 v249, %[rV], s46 offen nt lds\n"
      "v_cvt_pk_bf16_f32 v161, v202, v203\n"
      "v_mfma_f32_16x16x32_bf16 a[128:131], a[80:83], v[88:91], a[128:131]\n"
      "v_exp_f32 v209, v209\n"
      "v_mfma_f32_16x16x32_bf16 a[136:139], a[80:83], v[92:95], a[136:139]\n"
      "s_waitcnt lgkmcnt(6)\n"
      "v_exp_f32 v210, v210\n"
      "v_cvt_pk_bf16_f32 v164, v208, v209\n"
      "v_mfma_f32_16x16x32_bf16 v[196:199], a[84:87], a[12:15], v[196:199]\n"
      "v_exp_f32 v211, v211\n"
      "v_mfma_f32_16x16x32_bf16 v[204:207], a[84:87], a[28:31], v[204:207]\n"
      "ds_read_b64_tr_b16 a[72:73], v255 offset:28672\n"
      "v_exp_f32 v216, v216\n"
      "v_mfma_f32_16x16x32_bf16 v[212:215], a[84:87], a[44:47], v[212:215]\n"
      "s_waitcnt lgkmcnt(6)\n"
      "ds_read_b64_tr_b16 a[74:75], v255 offset:30720\n"
      "v_cvt_pk_bf16_f32 v165, v210, v211\n"
      "v_mfma_f32_16x16x32_bf16 v[220:223], a[196:199], a[60:63], v[220:223]\n"
      "s_waitcnt lgkmcnt(6)\n"
      "v_exp_f32 v217, v217\n"
      "v_mfma_f32_16x16x32_bf16 v[168:171], a[88:91], a[0:3], v[224:227]\n"
      "ds_read_b128 a[76:79], v243 offset:16384\n"
      "v_exp_f32 v218, v218\n"
      "v_mfma_f32_16x16x32_bf16 v[176:179], a[88:91], a[16:19], v[228:231]\n"
      "ds_read_b128 a[80:83], v240 offset:20480\n"
      "v_cvt_pk_bf16_f32 v216, v216, v217\n"
      "v_mfma_f32_16x16x32_bf16 v[184:187], a[88:91], a[32:35], v[232:235]\n"
      "s_waitcnt lgkmcnt(6)\n"
      "v_exp_f32 v219, v219\n"
      "s_nop 0\n"
      "v_cvt_pk_bf16_f32 v217, v218, v219\n"
      "v_mfma_f32_16x16x32_bf16 a[140:143], a[92:95], v[84:87], a[140:143]\n"
      "v_exp_f32 v196, v196\n"
      "v_mfma_f32_16x16x32_bf16 a[148:151], a[92:95], v[88:91], a[148:151]\n"
      "v_exp_f32 v197, v197\n"
      "v_mfma_f32_16x16x32_bf16 a[156:159], a[92:95], v[92:95], a[156:159]\n"
      "s_waitcnt lgkmcnt(5)\n"
      "v_exp_f32 v198, v198\n"
      "v_cvt_pk_bf16_f32 v158, v196, v197\n"
      "v_mfma_f32_16x16x32_bf16 v[168:171], a[64:67], a[4:7], v[168:171]\n"
      "s_add_u32 m0, s43, 57344\n"
      "v_exp_f32 v199, v199\n"
      "v_mfma_f32_16x16x32_bf16 v[176:179], a[64:67], a[20:23], v[176:179]\n"
      "s_mov_b32 s46, 49280\n"
      "ds_read_b128 a[84:87], v241 offset:20480\n"
      "v_exp_f32 v204, v204\n"
      "v_mfma_f32_16x16x32_bf16 v[184:187], a[64:67], a[36:39], v[184:187]\n"
      "ds_read_b128 a[88:91], v242 offset:20480\n"
      "v_cvt_pk_bf16_f32 v159, v198, v199\n"
      "v_mfma_f32_16x16x32_bf16 a[96:99], a[112:115], v[144:147], a[96:99]\n"
      "s_waitcnt lgkmcnt(6)\n"
      "v_exp_f32 v205, v205\n"
      "v_mfma_f32_16x16x32_bf16 v[168:171], a[68:71], a[8:11], v[168:171]\n"
      "v_exp_f32 v206, v206\n"
      "v_cvt_pk_bf16_f32 v162, v204, v205\n"
      "v_mfma_f32_16x16x32_bf16 v[176:179], a[68:71], a[24:27], v[176:179]\n"
      "v_exp_f32 v207, v207\n"
      "v_mfma_f32_16x16x32_bf16 v[184:187], a[68:71], a[40:43], v[184:187]\n"
      "s_waitcnt lgkmcnt(4)\n"
      "buffer_load_dwordx4 v249, %[rV], s46 offen nt lds\n"
      "ds_read_b128 a[92:95], v243 offset:20480\n"
      "v_mfma_f32_16x16x32_bf16 a[144:147], a[72:75], v[84:87], a[144:147]\n"
      "v_exp_f32 v212, v212\n"
      "v_cvt_pk_bf16_f32 v163, v206, v207\n"
      "v_mfma_f32_16x16x32_bf16 a[152:155], a[72:75], v[88:91], a[152:155]\n"
      "v_exp_f32 v213, v213\n"
      "v_mfma_f32_16x16x32_bf16 a[160:163], a[72:75], v[92:95], a[160:163]\n"
      "s_waitcnt lgkmcnt(4)\n"
      "v_exp_f32 v214, v214\n"
      "v_cvt_pk_bf16_f32 v166, v212, v213\n"
      "v_mfma_f32_16x16x32_bf16 v[168:171], a[76:79], a[12:15], v[168:171]\n"
      "v_exp_f32 v215, v215\n"
      "v_mfma_f32_16x16x32_bf16 v[176:179], a[76:79], a[28:31], v[176:179]\n"
      "ds_read_b128 a[64:67], v240 offset:24576\n"
      "v_exp_f32 v220, v220\n"
      "v_mfma_f32_16x16x32_bf16 v[184:187], a[76:79], a[44:47], v[184:187]\n"
      "ds_read_b128 a[68:71], v241 offset:24576\n"
      "v_cvt_pk_bf16_f32 v167, v214, v215\n"
      "v_mfma_f32_16x16x32_bf16 a[100:103], a[112:115], v[148:151], a[100:103]\n"
      "s_waitcnt lgkmcnt(5)\n"
      "v_exp_f32 v221, v221\n"
      "v_mfma_f32_16x16x32_bf16 v[172:175], a[80:83], a[0:3], v[224:227]\n"
      "v_exp_f32 v222, v222\n"
      "v_cvt_pk_bf16_f32 v218, v220, v221\n"
      "v_mfma_f32_16x16x32_bf16 v[180:183], a[80:83], a[16:19], v[228:231]\n"
      "s_add_u32 m0, s43, 61440\n"
      "v_exp_f32 v223, v223\n"
      "v_mfma_f32_16x16x32_bf16 v[188:191], a[80:83], a[32:35], v[232:235]\n"
      "s_waitcnt lgkmcnt(4)\n"
      "ds_read_b128 a[72:75], v242 offset:24576\n"
      "v_cvt_pk_bf16_f32 v219, v222, v223\n"
      "v_mfma_f32_16x16x32_bf16 v[172:175], a[84:87], a[4:7], v[172:175]\n"
      "s_mov_b32 s46, 57472\n"
      "ds_write_b128 v251, v[216:219] offset:12288\n"
      "ds_read_b128 a[76:79], v243 offset:24576\n"
      "v_mfma_f32_16x16x32_bf16 v[180:183], a[84:87], a[20:23], v[180:183]\n"
      "v_exp_f32 v168, v168\n"
      "v_mfma_f32_16x16x32_bf16 v[188:191], a[84:87], a[36:39], v[188:191]\n"
      "v_exp_f32 v169, v169\n"
      "v_mfma_f32_16x16x32_bf16 a[104:107], a[112:115], v[152:155], a[104:107]\n"
      "s_waitcnt lgkmcnt(6)\n"
      "v_exp_f32 v170, v170\n"
      "v_cvt_pk_bf16_f32 v168, v168, v169\n"
      "v_mfma_f32_16x16x32_bf16 v[172:175], a[88:91], a[8:11], v[172:175]\n"
      "v_exp_f32 v171, v171\n"
      "v_mfma_f32_16x16x32_bf16 v[180:183], a[88:91], a[24:27], v[180:183]\n"
      "v_exp_f32 v176, v176\n"
      "v_cvt_pk_bf16_f32 v169, v170, v171\n"
      "v_mfma_f32_16x16x32_bf16 v[188:191], a[88:91], a[40:43], v[188:191]\n"
      "s_waitcnt lgkmcnt(5)\n"
      "buffer_load_dwordx4 v249, %[rV], s46 offen nt lds\n"
      "v_exp_f32 v177, v177\n"
      "v_mfma_f32_16x16x32_bf16 v[172:175], a[92:95], a[12:15], v[172:175]\n"
      "v_exp_f32 v178, v178\n"
      "v_cvt_pk_bf16_f32 v176, v176, v177\n"
      "v_mfma_f32_16x16x32_bf16 v[180:183], a[92:95], a[28:31], v[180:183]\n"
      "ds_read_b128 a[80:83], v240 offset:28672\n"
      "v_exp_f32 v179, v179\n"
      "v_mfma_f32_16x16x32_bf16 v[188:191], a[92:95], a[44:47], v[188:191]\n"
      "s_waitcnt lgkmcnt(5)\n"
      "v_exp_f32 v184, v184\n"
      "v_cvt_pk_bf16_f32 v177, v178, v179\n"
      "v_mfma_f32_16x16x32_bf16 v[192:195], a[64:67], a[0:3], v[224:227]\n"
      "v_exp_f32 v185, v185\n"
      "v_mfma_f32_16x16x32_bf16 v[200:203], a[64:67], a[16:19], v[228:231]\n"
      "v_exp_f32 v186, v186\n"
      "v_cvt_pk_bf16_f32 v184, v184, v185\n"
      "v_mfma_f32_16x16x32_bf16 v[208:211], a[64:67], a[32:35], v[232:235]\n"
      "s_waitcnt lgkmcnt(4)\n"
      "ds_read_b128 a[84:87], v241 offset:28672\n"
      "v_exp_f32 v187, v187\n"
      "v_mfma_f32_16x16x32_bf16 v[192:195], a[68:71], a[4:7], v[192:195]\n"
      "ds_read_b128 a[88:91], v242 offset:28672\n"
      "v_cvt_pk_bf16_f32 v185, v186, v187\n"
      "v_mfma_f32_16x16x32_bf16 v[200:203], a[68:71], a[20:23], v[200:203]\n"
      "v_exp_f32 v172, v172\n"
      "v_mfma_f32_16x16x32_bf16 v[208:211], a[68:71], a[36:39], v[208:211]\n"
      "v_exp_f32 v173, v173\n"
      "v_mfma_f32_16x16x32_bf16 a[96:99], a[112:115], v[156:159], a[96:99]\n"
      "s_waitcnt lgkmcnt(5)\n"
      "v_exp_f32 v174, v174\n"
      "v_cvt_pk_bf16_f32 v170, v172, v173\n"
      "v_mfma_f32_16x16x32_bf16 v[192:195], a[72:75], a[8:11], v[192:195]\n"
      "v_exp_f32 v175, v175\n"
      "v_mfma_f32_16x16x32_bf16 v[200:203], a[72:75], a[24:27], v[200:203]\n"
      "v_exp_f32 v180, v180\n"
      "v_cvt_pk_bf16_f32 v171, v174, v175\n"
      "v_mfma_f32_16x16x32_bf16 v[208:211], a[72:75], a[40:43], v[208:211]\n"
      "s_waitcnt lgkmcnt(3)\n"
      "ds_read_b128 a[92:95], v243 offset:28672\n"
      "v_exp_f32 v181, v181\n"
      "v_mfma_f32_16x16x32_bf16 v[192:195], a[76:79], a[12:15], v[192:195]\n"
      "v_exp_f32 v182, v182\n"
      "v_cvt_pk_bf16_f32 v178, v180, v181\n"
      "v_mfma_f32_16x16x32_bf16 v[200:203], a[76:79], a[28:31], v[200:203]\n"
      "v_exp_f32 v183, v183\n"
      "v_accvgpr_read_b32 v244, a240\n"
      "v_mfma_f32_16x16x32_bf16 v[208:211], a[76:79], a[44:47], v[208:211]\n"
      "v_accvgpr_read_b32 v245, a241\n"
      "v_accvgpr_read_b32 v246, a243\n"
      "v_accvgpr_read_b32 v240, a244\n"
      "v_accvgpr_read_b32 v241, a246\n"
      "v_mfma_f32_16x16x32_bf16 a[100:103], a[112:115], v[160:163], a[100:103]\n"
      "s_waitcnt lgkmcnt(3)\n"
      "v_accvgpr_read_b32 v242, a249\n"
      "v_accvgpr_read_b32 v247, a253\n"
      "v_accvgpr_read_b32 v248, a242\n"
      "v_accvgpr_read_b32 v243, a201\n"
      "v_mfma_f32_16x16x32_bf16 v[196:199], a[80:83], a[0:3], v[224:227]\n"
      "v_add_u32 v243, 1, v243\n"
      "v_add_u32 v252, 4294901760, v252\n"
      "v_add_u32 v253, 4294901760, v253\n"
      "v_add_u32 v254, 4294901760, v254\n"
      "v_mfma_f32_16x16x32_bf16 v[204:207], a[80:83], a[16:19], v[228:231]\n"
      "v_add_u32 v255, 4294901760, v255\n"
      "v_add_u32 v244, 131072, v244\n"
      "v_exp_f32 v188, v188\n"
      "v_cvt_pk_bf16_f32 v179, v182, v183\n"
      "v_mfma_f32_16x16x32_bf16 v[212:215], a[80:83], a[32:35], v[232:235]\n"
      "s_waitcnt vmcnt(8) lgkmcnt(0)\n"
      "s_barrier\n"
      "v_exp_f32 v189, v189\n"
      "v_exp_f32 v190, v190\n"
      "v_mfma_f32_16x16x32_bf16 v[196:199], a[84:87], a[4:7], v[196:199]\n"
      "s_add_u32 m0, s43, 0\n"
      "v_cvt_pk_bf16_f32 v186, v188, v189\n"
      "v_exp_f32 v191, v191\n"
      "s_nop 0\n"
      "v_cvt_pk_bf16_f32 v187, v190, v191\n"
      "v_mfma_f32_16x16x32_bf16 v[204:207], a[84:87], a[20:23], v[204:207]\n"
      "s_mov_b32 s46, 65664\n"
      "ds_read_b64_tr_b16 a[64:65], v252 offset:0\n"
      "v_exp_f32 v192, v192\n"
      "v_mfma_f32_16x16x32_bf16 v[212:215], a[84:87], a[36:39], v[212:215]\n"
      "ds_read_b64_tr_b16 a[66:67], v252 offset:2048\n"
      "ds_read_b64_tr_b16 a[68:69], v253 offset:0\n"
      "v_exp_f32 v193, v193\n"
      "v_mfma_f32_16x16x32_bf16 a[104:107], a[112:115], v[164:167], a[104:107]\n"
      "ds_read_b64_tr_b16 a[70:71], v253 offset:2048\n"
      "ds_read_b64_tr_b16 a[168:169], v244 offset:0\n"
      "ds_read_b64_tr_b16 a[170:171], v244 offset:2048\n"
      "ds_read_b128 a[184:187], v250 offset:0\n"
      "v_mfma_f32_16x16x32_bf16 v[196:199], a[88:91], a[8:11], v[196:199]\n"
      "buffer_load_dwordx4 v249, %[rV], s46 offen nt lds\n"
      "v_exp_f32 v194, v194\n"
      "v_cvt_pk_bf16_f32 v192, v192, v193\n"
      "v_mfma_f32_16x16x32_bf16 v[204:207], a[88:91], a[24:27], v[204:207]\n"
      "s_add_u32 m0, s43, 4096\n"
      "ds_read_b64_tr_b16 a[72:73], v252 offset:4096\n"
      "v_exp_f32 v195, v195\n"
      "v_mfma_f32_16x16x32_bf16 v[212:215], a[88:91], a[40:43], v[212:215]\n"
      "s_mov_b32 s46, 73856\n"
      "ds_read_b64_tr_b16 a[74:75], v252 offset:6144\n"
      "v_exp_f32 v200, v200\n"
      "v_mfma_f32_16x16x32_bf16 v[196:199], a[92:95], a[12:15], v[196:199]\n"
      "ds_read_b64_tr_b16 a[76:77], v253 offset:4096\n"
      "v_cvt_pk_bf16_f32 v193, v194, v195\n"
      "v_exp_f32 v201, v201\n"
      "v_mfma_f32_16x16x32_bf16 v[204:207], a[92:95], a[28:31], v[204:207]\n"
      "v_exp_f32 v202, v202\n"
      "v_cvt_pk_bf16_f32 v200, v200, v201\n"
      "v_mfma_f32_16x16x32_bf16 v[212:215], a[92:95], a[44:47], v[212:215]\n"
      "ds_read_b64_tr_b16 a[78:79], v253 offset:6144\n"
      "v_exp_f32 v203, v203\n"
      "v_mfma_f32_16x16x32_bf16 a[108:111], a[112:115], v[216:219], a[108:111]\n"
      "s_waitcnt lgkmcnt(9)\n"
      "ds_read_b64_tr_b16 a[172:173], v244 offset:4096\n"
      "ds_read_b64_tr_b16 a[174:175], v244 offset:6144\n"
      "v_exp_f32 v208, v208\n"
      "v_mfma_f32_16x16x32_bf16 a[116:119], a[64:67], v[96:99], a[116:119]\n"
      "ds_read_b128 a[188:191], v250 offset:1024\n"
      "buffer_load_dwordx4 v249, %[rV], s46 offen nt lds\n"
      "v_cvt_pk_bf16_f32 v201, v202, v203\n"
      "v_mfma_f32_16x16x32_bf16 a[124:127], a[64:67], v[100:103], a[124:127]\n"
      "s_add_u32 m0, s43, 8192\n"
      "ds_read_b64_tr_b16 a[80:81], v252 offset:8192\n"
      "v_exp_f32 v209, v209\n"
      "v_mfma_f32_16x16x32_bf16 a[132:135], a[64:67], v[104:107], a[132:135]\n"
      "s_mov_b32 s46, 82048\n"
      "v_exp_f32 v210, v210\n"
      "v_cvt_pk_bf16_f32 v208, v208, v209\n"
      "v_mfma_f32_16x16x32_bf16 a[96:99], a[112:115], v[168:171], a[96:99]\n"
      "s_waitcnt lgkmcnt(11)\n"
      "ds_read_b64_tr_b16 a[82:83], v252 offset:10240\n"
      "v_exp_f32 v211, v211\n"
      "v_mfma_f32_16x16x32_bf16 a[120:123], a[68:71], v[96:99], a[120:123]\n"
      "ds_read_b64_tr_b16 a[84:85], v253 offset:8192\n"
      "v_cvt_pk_bf16_f32 v209, v210, v211\n"
      "v_exp_f32 v196, v196\n"
      "v_mfma_f32_16x16x32_bf16 a[128:131], a[68:71], v[100:103], a[128:131]\n"
      "ds_read_b64_tr_b16 a[86:87], v253 offset:10240\n"
      "v_exp_f32 v197, v197\n"
      "v_mfma_f32_16x16x32_bf16 a[136:139], a[68:71], v[104:107], a[136:139]\n"
      "s_waitcnt lgkmcnt(11)\n"
      "ds_read_b64_tr_b16 a[176:177], v244 offset:8192\n"
      "ds_read_b64_tr_b16 a[178:179], v244 offset:10240\n"
      "ds_read_b128 a[192:195], v250 offset:2048\n"
      "v_mfma_f32_16x16x32_bf16 a[164:167], a[168:171], a[184:187], 0\n"
      "v_exp_f32 v198, v198\n"
      "v_cvt_pk_bf16_f32 v194, v196, v197\n"
      "v_mfma_f32_16x16x32_bf16 a[100:103], a[112:115], v[176:179], a[100:103]\n"
      "s_waitcnt lgkmcnt(12)\n"
      "buffer_load_dwordx4 v249, %[rV], s46 offen nt lds\n"
      "v_exp_f32 v199, v199\n"
      "v_mfma_f32_16x16x32_bf16 a[116:119], a[72:75], v[108:111], a[116:119]\n"
      "v_exp_f32 v204, v204\n"
      "v_cvt_pk_bf16_f32 v195, v198, v199\n"
      "v_mfma_f32_16x16x32_bf16 a[124:127], a[72:75], v[112:115], a[124:127]\n"
      "s_add_u32 m0, s43, 12288\n"
      "ds_read_b64_tr_b16 a[88:89], v252 offset:12288\n"
      "ds_read_b64_tr_b16 a[90:91], v252 offset:14336\n"
      "v_mfma_f32_16x16x32_bf16 a[132:135], a[72:75], v[116:119], a[132:135]\n"
      "s_mov_b32 s46, 90240\n"
      "v_exp_f32 v205, v205\n"
      "v_mfma_f32_16x16x32_bf16 a[104:107], a[112:115], v[184:187], a[104:107]\n"
      "s_waitcnt lgkmcnt(12)\n"
      "v_exp_f32 v206, v206\n"
      "v_cvt_pk_bf16_f32 v202, v204, v205\n"
      "v_mfma_f32_16x16x32_bf16 a[120:123], a[76:79], v[108:111], a[120:123]\n"
      "ds_read_b64_tr_b16 a[92:93], v253 offset:12288\n"
      "ds_read_b64_tr_b16 a[94:95], v253 offset:14336\n"
      "v_mfma_f32_16x16x32_bf16 a[128:131], a[76:79], v[112:115], a[128:131]\n"
      "v_exp_f32 v207, v207\n"
      "v_mfma_f32_16x16x32_bf16 a[136:139], a[76:79], v[116:119], a[136:139]\n"
      "s_waitcnt lgkmcnt(11)\n"
      "ds_read_b64_tr_b16 a[180:181], v244 offset:12288\n"
      "ds_read_b64_tr_b16 a[182:183], v244 offset:14336\n"
      "ds_read_b128 a[196:199], v250 offset:3072\n"
      "v_exp_f32 v212, v212\n"
      "v_mfma_f32_16x16x32_bf16 a[164:167], a[172:175], a[188:191], a[164:167]\n"
      "s_waitcnt lgkmcnt(12)\n"
      "ds_read_b64_tr_b16 a[64:65], v252 offset:16384\n"
      "v_cvt_pk_bf16_f32 v203, v206, v207\n"
      "v_exp_f32 v213, v213\n"
      "v_mfma_f32_16x16x32_bf16 a[116:119], a[80:83], v[120:123], a[116:119]\n"
      "ds_read_b64_tr_b16 a[66:67], v252 offset:18432\n"
      "v_exp_f32 v214, v214\n"
      "v_mfma_f32_16x16x32_bf16 a[124:127], a[80:83], v[124:127], a[124:127]\n"
      "buffer_load_dwordx4 v249, %[rV], s46 offen nt lds\n"
      "v_cvt_pk_bf16_f32 v210, v212, v213\n"
      "v_mfma_f32_16x16x32_bf16 a[132:135], a[80:83], v[128:131], a[132:135]\n"
      "s_waitcnt lgkmcnt(12)\n"
      "v_exp_f32 v215, v215\n"
      "s_nop 0\n"
      "v_cvt_pk_bf16_f32 v211, v214, v215\n"
      "v_mfma_f32_16x16x32_bf16 a[120:123], a[84:87], v[120:123], a[120:123]\n"
      "s_add_u32 m0, s43, 16384\n"
      "ds_read_b64_tr_b16 a[68:69], v253 offset:16384\n"
      "ds_read_b64_tr_b16 a[70:71], v253 offset:18432\n"
      "v_mfma_f32_16x16x32_bf16 a[128:131], a[84:87], v[124:127], a[128:131]\n"
      "s_mov_b32 s46, 98432\n"
      "buffer_load_dwordx4 v249, %[rV], s46 offen nt lds\n"
      "v_accvgpr_read_b32 v220, a108\n"
      "v_mfma_f32_16x16x32_bf16 a[136:139], a[84:87], v[128:131], a[136:139]\n"
      "s_waitcnt lgkmcnt(11)\n"
      "ds_read_b64_tr_b16 a[168:169], v244 offset:16384\n"
      "ds_read_b64_tr_b16 a[170:171], v244 offset:18432\n"
      "ds_read_b128 a[184:187], v250 offset:4096\n"
      "v_mfma_f32_16x16x32_bf16 a[164:167], a[176:179], a[192:195], a[164:167]\n"
      "s_waitcnt lgkmcnt(12)\n"
      "ds_read_b64_tr_b16 a[72:73], v252 offset:20480\n"
      "ds_read_b64_tr_b16 a[74:75], v252 offset:22528\n"
      "v_mfma_f32_16x16x32_bf16 a[116:119], a[88:91], v[132:135], a[116:119]\n"
      "v_lshrrev_b32 v221, 4, s43\n"
      "v_add_u32 v221, v221, v246\n"
      "v_mfma_f32_16x16x32_bf16 a[124:127], a[88:91], v[136:139], a[124:127]\n"
      "v_mfma_f32_16x16x32_bf16 a[132:135], a[88:91], v[140:143], a[132:135]\n"
      "s_waitcnt lgkmcnt(12)\n"
      "ds_write_b32 v221, v220 offset:20480\n"
      "ds_read_b64_tr_b16 a[76:77], v253 offset:20480\n"
      "v_mfma_f32_16x16x32_bf16 a[120:123], a[92:95], v[132:135], a[120:123]\n"
      "s_add_u32 m0, s43, 20480\n"
      "v_mfma_f32_16x16x32_bf16 a[128:131], a[92:95], v[136:139], a[128:131]\n"
      "s_mov_b32 s46, 106624\n"
      "buffer_load_dwordx4 v249, %[rV], s46 offen nt lds\n"
      "v_mfma_f32_16x16x32_bf16 a[136:139], a[92:95], v[140:143], a[136:139]\n"
      "s_waitcnt lgkmcnt(11)\n"
      "ds_read_b64_tr_b16 a[78:79], v253 offset:22528\n"
      "ds_read_b64_tr_b16 a[172:173], v244 offset:20480\n"
      "ds_read_b64_tr_b16 a[174:175], v244 offset:22528\n"
      "v_mfma_f32_16x16x32_bf16 a[164:167], a[180:183], a[196:199], a[164:167]\n"
      "s_waitcnt lgkmcnt(12)\n"
      "ds_read_b128 a[188:191], v250 offset:5120\n"
      "ds_read_b64_tr_b16 a[80:81], v252 offset:24576\n"
      "v_mfma_f32_16x16x32_bf16 a[116:119], a[64:67], v[144:147], a[116:119]\n"
      "v_mfma_f32_16x16x32_bf16 a[124:127], a[64:67], v[148:151], a[124:127]\n"
      "v_mfma_f32_16x16x32_bf16 a[132:135], a[64:67], v[152:155], a[132:135]\n"
      "v_mfma_f32_16x16x32_bf16 a[96:99], a[112:115], v[192:195], a[96:99]\n"
      "s_waitcnt lgkmcnt(12)\n"
      "ds_read_b64_tr_b16 a[82:83], v252 offset:26624\n"
      "ds_read_b64_tr_b16 a[84:85], v253 offset:24576\n"
      "v_mfma_f32_16x16x32_bf16 a[120:123], a[68:71], v[144:147], a[120:123]\n"
      "s_add_u32 m0, s43, 24576\n"
      "v_mfma_f32_16x16x32_bf16 a[128:131], a[68:71], v[148:151], a[128:131]\n"
      "s_mov_b32 s46, 114816\n"
      "buffer_load_dwordx4 v249, %[rV], s46 offen nt lds\n"
      "v_mfma_f32_16x16x32_bf16 a[136:139], a[68:71], v[152:155], a[136:139]\n"
      "s_waitcnt lgkmcnt(11)\n"
      "ds_read_b64_tr_b16 a[86:87], v253 offset:26624\n"
      "ds_read_b64_tr_b16 a[176:177], v244 offset:24576\n"
      "ds_read_b64_tr_b16 a[178:179], v244 offset:26624\n"
      "v_mfma_f32_16x16x32_bf16 a[164:167], a[168:171], a[184:187], a[164:167]\n"
      "v_mfma_f32_16x16x32_bf16 a[100:103], a[112:115], v[200:203], a[100:103]\n"
      "s_waitcnt lgkmcnt(12)\n"
      "ds_read_b128 a[192:195], v250 offset:6144\n"
      "ds_read_b64_tr_b16 a[88:89], v252 offset:28672\n"
      "v_mfma_f32_16x16x32_bf16 a[116:119], a[72:75], v[156:159], a[116:119]\n"
      "v_mfma_f32_16x16x32_bf16 a[124:127], a[72:75], v[160:163], a[124:127]\n"
      "v_mfma_f32_16x16x32_bf16 a[132:135], a[72:75], v[164:167], a[132:135]\n"
      "v_mfma_f32_16x16x32_bf16 a[104:107], a[112:115], v[208:211], a[104:107]\n"
      "s_waitcnt lgkmcnt(11)\n"
      "ds_read_b64_tr_b16 a[90:91], v252 offset:30720\n"
      "ds_read_b64_tr_b16 a[92:93], v253 offset:28672\n"
      "ds_read_b64_tr_b16 a[94:95], v253 offset:30720\n"
      "v_mfma_f32_16x16x32_bf16 a[120:123], a[76:79], v[156:159], a[120:123]\n"
      "s_add_u32 m0, s43, 28672\n"
      "v_mfma_f32_16x16x32_bf16 a[128:131], a[76:79], v[160:163], a[128:131]\n"
      "s_mov_b32 s46, 123008\n"
      "buffer_load_dwordx4 v249, %[rV], s46 offen nt lds\n"
      "v_mfma_f32_16x16x32_bf16 a[136:139], a[76:79], v[164:167], a[136:139]\n"
      "s_waitcnt lgkmcnt(11)\n"
      "ds_read_b64_tr_b16 a[180:181], v244 offset:28672\n"
      "ds_read_b64_tr_b16 a[182:183], v244 offset:30720\n"
      "ds_read_b128 a[196:199], v250 offset:7168\n"
      "v_add_u32 v244, 4294901760, v244\n"
      "v_mfma_f32_16x16x32_bf16 a[164:167], a[172:175], a[188:191], a[164:167]\n"
      "s_waitcnt lgkmcnt(12)\n"
      "ds_read_b64_tr_b16 a[64:65], v254 offset:0\n"
      "ds_read_b64_tr_b16 a[66:67], v254 offset:2048\n"
      "v_mfma_f32_16x16x32_bf16 a[116:119], a[80:83], v[168:171], a[116:119]\n"
      "v_mfma_f32_16x16x32_bf16 a[124:127], a[80:83], v[176:179], a[124:127]\n"
      "v_mfma_f32_16x16x32_bf16 a[132:135], a[80:83], v[184:187], a[132:135]\n"
      "s_waitcnt lgkmcnt(2)\n"
      "s_barrier\n"
      "ds_read_b64_tr_b16 a[68:69], v255 offset:0\n"
      "ds_read_b64_tr_b16 a[70:71], v255 offset:2048\n"
      "ds_read_b64_tr_b16 a[168:169], v244 offset:0\n"
      "v_mfma_f32_16x16x32_bf16 a[120:123], a[84:87], v[168:171], a[120:123]\n"
      "ds_read_b64_tr_b16 a[170:171], v244 offset:2048\n"
      "ds_read_b128 a[184:187], v250 offset:8192\n"
      "v_mfma_f32_16x16x32_bf16 a[128:131], a[84:87], v[176:179], a[128:131]\n"
      "ds_read_b64_tr_b16 a[72:73], v254 offset:4096\n"
      "v_accvgpr_read_b32 v224, a96\n"
      "v_mfma_f32_16x16x32_bf16 a[136:139], a[84:87], v[184:187], a[136:139]\n"
      "ds_read_b64_tr_b16 a[74:75], v254 offset:6144\n"
      "v_accvgpr_read_b32 v225, a100\n"
      "v_mfma_f32_16x16x32_bf16 a[164:167], a[176:179], a[192:195], a[164:167]\n"
      "ds_read_b32 v236, v246 offset:20480\n"
      "v_accvgpr_read_b32 v226, a104\n"
      "v_mfma_f32_16x16x32_bf16 a[116:119], a[88:91], v[192:195], a[116:119]\n"
      "ds_read_b32 v237, v246 offset:20544\n"
      "ds_read_b32 v238, v246 offset:20608\n"
      "ds_read_b32 v239, v246 offset:20672\n"
      "v_mfma_f32_16x16x32_bf16 a[124:127], a[88:91], v[200:203], a[124:127]\n"
      "ds_read_b64_tr_b16 a[76:77], v255 offset:4096\n"
      "v_rcp_f32 v224, v224\n"
      "v_mfma_f32_16x16x32_bf16 a[132:135], a[88:91], v[208:211], a[132:135]\n"
      "v_rcp_f32 v225, v225\n"
      "v_mfma_f32_16x16x32_bf16 a[120:123], a[92:95], v[192:195], a[120:123]\n"
      "v_rcp_f32 v226, v226\n"
      "v_mfma_f32_16x16x32_bf16 a[128:131], a[92:95], v[200:203], a[128:131]\n"
      "v_mfma_f32_16x16x32_bf16 a[136:139], a[92:95], v[208:211], a[136:139]\n"
      "v_mfma_f32_16x16x32_bf16 a[164:167], a[180:183], a[196:199], a[164:167]\n"
      "s_waitcnt lgkmcnt(12)\n"
      "ds_read_b64_tr_b16 a[78:79], v255 offset:6144\n"
      "ds_read_b64_tr_b16 a[172:173], v244 offset:4096\n"
      "v_mfma_f32_16x16x32_bf16 a[140:143], a[64:67], v[96:99], a[140:143]\n"
      "v_mfma_f32_16x16x32_bf16 a[148:151], a[64:67], v[100:103], a[148:151]\n"
      "v_mfma_f32_16x16x32_bf16 a[156:159], a[64:67], v[104:107], a[156:159]\n"
      "s_waitcnt lgkmcnt(12)\n"
      "ds_read_b64_tr_b16 a[174:175], v244 offset:6144\n"
      "ds_read_b128 a[188:191], v250 offset:9216\n"
      "v_mfma_f32_16x16x32_bf16 a[144:147], a[68:71], v[96:99], a[144:147]\n"
      "v_accvgpr_read_b32 v228, a116\n"
      "v_accvgpr_read_b32 v229, a117\n"
      "v_mfma_f32_16x16x32_bf16 a[152:155], a[68:71], v[100:103], a[152:155]\n"
      "v_accvgpr_read_b32 v230, a118\n"
      "v_accvgpr_read_b32 v231, a119\n"
      "v_mfma_f32_16x16x32_bf16 a[160:163], a[68:71], v[104:107], a[160:163]\n"
      "s_waitcnt lgkmcnt(11)\n"
      "ds_read_b64_tr_b16 a[80:81], v254 offset:8192\n"
      "ds_read_b64_tr_b16 a[82:83], v254 offset:10240\n"
      "ds_read_b64_tr_b16 a[84:85], v255 offset:8192\n"
      "v_mfma_f32_16x16x32_bf16 a[164:167], a[168:171], a[184:187], a[164:167]\n"
      "s_waitcnt lgkmcnt(12)\n"
      "ds_read_b64_tr_b16 a[86:87], v255 offset:10240\n"
      "ds_read_b64_tr_b16 a[176:177], v244 offset:8192\n"
      "v_accvgpr_read_b32 v232, a120\n"
      "v_mfma_f32_16x16x32_bf16 a[140:143], a[72:75], v[108:111], a[140:143]\n"
      "v_accvgpr_read_b32 v233, a121\n"
      "v_accvgpr_read_b32 v234, a122\n"
      "v_mfma_f32_16x16x32_bf16 a[148:151], a[72:75], v[112:115], a[148:151]\n"
      "v_accvgpr_read_b32 v235, a123\n"
      "v_mul_f32 v228, v228, v224\n"
      "v_mfma_f32_16x16x32_bf16 a[156:159], a[72:75], v[116:119], a[156:159]\n"
      "s_waitcnt lgkmcnt(8)\n"
      "ds_read_b64_tr_b16 a[178:179], v244 offset:10240\n"
      "ds_read_b128 a[192:195], v250 offset:10240\n"
      "ds_read_b64_tr_b16 a[88:89], v254 offset:12288\n"
      "ds_read_b64_tr_b16 a[90:91], v254 offset:14336\n"
      "v_mfma_f32_16x16x32_bf16 a[144:147], a[76:79], v[108:111], a[144:147]\n"
      "ds_read_b64_tr_b16 a[92:93], v255 offset:12288\n"
      "ds_read_b64_tr_b16 a[94:95], v255 offset:14336\n"
      "v_add_f32 v236, v236, v237\n"
      "v_mfma_f32_16x16x32_bf16 a[152:155], a[76:79], v[112:115], a[152:155]\n"
      "v_mul_f32 v229, v229, v224\n"
      "v_mul_f32 v230, v230, v224\n"
      "v_add_f32 v238, v238, v239\n"
      "v_mfma_f32_16x16x32_bf16 a[160:163], a[76:79], v[116:119], a[160:163]\n"
      "s_waitcnt lgkmcnt(11)\n"
      "ds_read_b64_tr_b16 a[180:181], v244 offset:12288\n"
      "ds_read_b64_tr_b16 a[182:183], v244 offset:14336\n"
      "ds_read_b128 a[196:199], v250 offset:11264\n"
      "v_mfma_f32_16x16x32_bf16 a[164:167], a[172:175], a[188:191], a[164:167]\n"
      "s_waitcnt lgkmcnt(12)\n"
      "v_mul_f32 v231, v231, v224\n"
      "v_add_f32 v227, v236, v238\n"
      "v_mul_f32 v232, v232, v224\n"
      "v_mfma_f32_16x16x32_bf16 a[140:143], a[80:83], v[120:123], a[140:143]\n"
      "s_add_u32 m0, s43, 131072\n"
      "ds_read_b64_tr_b16 a[64:65], v254 offset:16384\n"
      "ds_read_b64_tr_b16 a[66:67], v254 offset:18432\n"
      "v_mul_f32 v233, v233, v224\n"
      "v_mfma_f32_16x16x32_bf16 a[148:151], a[80:83], v[124:127], a[148:151]\n"
      "s_xor_b32 s46, s48, 0\n"
      "v_rcp_f32 v227, v227\n"
      "v_mul_f32 v234, v234, v224\n"
      "v_mfma_f32_16x16x32_bf16 a[156:159], a[80:83], v[128:131], a[156:159]\n"
      "s_waitcnt lgkmcnt(12)\n"
      "v_mul_f32 v235, v235, v224\n"
      "v_cvt_pk_bf16_f32 v172, v228, v229\n"
      "v_cvt_pk_bf16_f32 v173, v230, v231\n"
      "v_mfma_f32_16x16x32_bf16 a[144:147], a[84:87], v[120:123], a[144:147]\n"
      "s_mov_b64 s[58:59], 0\n"
      "ds_read_b64_tr_b16 a[68:69], v255 offset:16384\n"
      "ds_read_b64_tr_b16 a[70:71], v255 offset:18432\n"
      "v_cmp_nlt_f32 vcc, 0x21800000, v224\n"
      "s_or_b64 s[58:59], s[58:59], vcc\n"
      "v_mfma_f32_16x16x32_bf16 a[152:155], a[84:87], v[124:127], a[152:155]\n"
      "v_cvt_pk_bf16_f32 v174, v232, v233\n"
      "v_cvt_pk_bf16_f32 v175, v234, v235\n"
      "v_mfma_f32_16x16x32_bf16 a[160:163], a[84:87], v[128:131], a[160:163]\n"
      "s_waitcnt lgkmcnt(11)\n"
      "ds_read_b64_tr_b16 a[168:169], v244 offset:16384\n"
      "ds_read_b64_tr_b16 a[170:171], v244 offset:18432\n"
      "ds_read_b128 a[184:187], v250 offset:12288\n"
      "v_cmp_nlt_f32 vcc, 0x21800000, v225\n"
      "s_or_b64 s[58:59], s[58:59], vcc\n"
      "v_mfma_f32_16x16x32_bf16 a[164:167], a[176:179], a[192:195], a[164:167]\n"
      "s_waitcnt lgkmcnt(12)\n"
      "s_add_u32 s44, s52, 0\n"
      "buffer_store_dwordx4 v[172:175], v248, %[rO], s44 offen sc1\n"
      "v_accvgpr_read_b32 v228, a124\n"
      "v_mfma_f32_16x16x32_bf16 a[140:143], a[88:91], v[132:135], a[140:143]\n"
      "buffer_load_dwordx4 v240, %[rWo], s46 offen lds\n"
      "ds_read_b64_tr_b16 a[72:73], v254 offset:20480\n"
      "ds_read_b64_tr_b16 a[74:75], v254 offset:22528\n"
      "v_accvgpr_read_b32 v229, a125\n"
      "v_mfma_f32_16x16x32_bf16 a[148:151], a[88:91], v[136:139], a[148:151]\n"
      "s_add_u32 m0, s43, 135168\n"
      "v_cmp_nlt_f32 vcc, 0x21800000, v226\n"
      "s_or_b64 s[58:59], s[58:59], vcc\n"
      "v_accvgpr_read_b32 v230, a126\n"
      "v_mfma_f32_16x16x32_bf16 a[156:159], a[88:91], v[140:143], a[156:159]\n"
      "s_waitcnt lgkmcnt(12)\n"
      "v_accvgpr_read_b32 v231, a127\n"
      "v_accvgpr_read_b32 v232, a128\n"
      "v_accvgpr_read_b32 v233, a129\n"
      "v_mfma_f32_16x16x32_bf16 a[144:147], a[92:95], v[132:135], a[144:147]\n"
      "s_xor_b32 s46, s48, 16384\n"
      "ds_read_b64_tr_b16 a[76:77], v255 offset:20480\n"
      "ds_read_b64_tr_b16 a[78:79], v255 offset:22528\n"
      "v_cmp_nlt_f32 vcc, 0x21800000, v227\n"
      "s_or_b64 s[58:59], s[58:59], vcc\n"
      "v_mfma_f32_16x16x32_bf16 a[152:155], a[92:95], v[136:139], a[152:155]\n"
      "v_accvgpr_read_b32 v234, a130\n"
      "v_accvgpr_read_b32 v235, a131\n"
      "v_mul_f32 v228, v228, v225\n"
      "v_mfma_f32_16x16x32_bf16 a[160:163], a[92:95], v[140:143], a[160:163]\n"
      "s_waitcnt lgkmcnt(11)\n"
      "ds_read_b64_tr_b16 a[172:173], v244 offset:20480\n"
      "ds_read_b64_tr_b16 a[174:175], v244 offset:22528\n"
      "ds_read_b128 a[188:191], v250 offset:13312\n"
      "v_cmp_ngt_f32 vcc, 4.0, v224\n"
      "s_or_b64 s[58:59], s[58:59], vcc\n"
      "v_mfma_f32_16x16x32_bf16 a[164:167], a[180:183], a[196:199], a[164:167]\n"
      "s_waitcnt lgkmcnt(12)\n"
      "v_mul_f32 v229, v229, v225\n"
      "v_mul_f32 v230, v230, v225\n"
      "v_mul_f32 v231, v231, v225\n"
      "v_mfma_f32_16x16x32_bf16 a[140:143], a[64:67], v[144:147], a[140:143]\n"
      "ds_read_b64_tr_b16 a[80:81], v254 offset:24576\n"
      "ds_read_b64_tr_b16 a[82:83], v254 offset:26624\n"
      "v_cmp_ngt_f32 vcc, 4.0, v225\n"
      "s_or_b64 s[58:59], s[58:59], vcc\n"
      "v_mfma_f32_16x16x32_bf16 a[148:151], a[64:67], v[148:151], a[148:151]\n"
      "buffer_load_dwordx4 v240, %[rWo], s46 offen lds\n"
      "v_mul_f32 v232, v232, v225\n"
      "v_mul_f32 v233, v233, v225\n"
      "v_mfma_f32_16x16x32_bf16 a[156:159], a[64:67], v[152:155], a[156:159]\n"
      "s_waitcnt lgkmcnt(12)\n"
      "ds_read_b64_tr_b16 a[84:85], v255 offset:24576\n"
      "v_mul_f32 v234, v234, v225\n"
      "v_mul_f32 v235, v235, v225\n"
      "v_mfma_f32_16x16x32_bf16 a[144:147], a[68:71], v[144:147], a[144:147]\n"
      "s_add_u32 m0, s43, 139264\n"
      "ds_read_b64_tr_b16 a[86:87], v255 offset:26624\n"
      "v_cmp_ngt_f32 vcc, 4.0, v226\n"
      "s_or_b64 s[58:59], s[58:59], vcc\n"
      "v_mfma_f32_16x16x32_bf16 a[152:155], a[68:71], v[148:151], a[152:155]\n"
      "s_xor_b32 s46, s48, 32768\n"
      "v_cvt_pk_bf16_f32 v188, v228, v229\n"
      "v_cvt_pk_bf16_f32 v189, v230, v231\n"
      "v_cvt_pk_bf16_f32 v190, v232, v233\n"
      "v_mfma_f32_16x16x32_bf16 a[160:163], a[68:71], v[152:155], a[160:163]\n"
      "s_waitcnt lgkmcnt(11)\n"
      "ds_read_b64_tr_b16 a[176:177], v244 offset:24576\n"
      "ds_read_b64_tr_b16 a[178:179], v244 offset:26624\n"
      "ds_read_b128 a[192:195], v250 offset:14336\n"
      "v_cmp_ngt_f32 vcc, 4.0, v227\n"
      "s_or_b64 s[58:59], s[58:59], vcc\n"
      "v_mfma_f32_16x16x32_bf16 a[164:167], a[168:171], a[184:187], a[164:167]\n"
      "s_waitcnt lgkmcnt(12)\n"
      "s_or_b32 s58, s58, s59\n"
      "v_cvt_pk_bf16_f32 v191, v234, v235\n"
      "v_mov_b32 v236, s58\n"
      "v_mfma_f32_16x16x32_bf16 a[140:143], a[72:75], v[156:159], a[140:143]\n"
      "buffer_store_dwordx4 v[188:191], v248, %[rO], s44 offen offset:1024 sc1\n"
      "ds_read_b64_tr_b16 a[88:89], v254 offset:28672\n"
      "ds_read_b64_tr_b16 a[90:91], v254 offset:30720\n"
      "v_accvgpr_read_b32 v228, a132\n"
      "v_mfma_f32_16x16x32_bf16 a[148:151], a[72:75], v[160:163], a[148:151]\n"
      "buffer_load_dwordx4 v240, %[rWo], s46 offen lds\n"
      "v_lshrrev_b32 v237, 8, s43\n"
      "v_accvgpr_read_b32 v229, a133\n"
      "v_mfma_f32_16x16x32_bf16 a[156:159], a[72:75], v[164:167], a[156:159]\n"
      "s_waitcnt lgkmcnt(12)\n"
      "v_accvgpr_read_b32 v230, a134\n"
      "v_add_u32 v237, 119040, v237\n"
      "v_accvgpr_read_b32 v231, a135\n"
      "v_mfma_f32_16x16x32_bf16 a[144:147], a[76:79], v[156:159], a[144:147]\n"
      "s_add_u32 m0, s43, 143360\n"
      "ds_write_b32 v237, v236 offset:0\n"
      "ds_read_b64_tr_b16 a[92:93], v255 offset:28672\n"
      "v_accvgpr_read_b32 v232, a136\n"
      "v_mfma_f32_16x16x32_bf16 a[152:155], a[76:79], v[160:163], a[152:155]\n"
      "s_xor_b32 s46, s48, 49152\n"
      "v_accvgpr_read_b32 v233, a137\n"
      "v_accvgpr_read_b32 v234, a138\n"
      "v_accvgpr_read_b32 v235, a139\n"
      "v_mfma_f32_16x16x32_bf16 a[160:163], a[76:79], v[164:167], a[160:163]\n"
      "s_waitcnt lgkmcnt(11)\n"
      "ds_read_b64_tr_b16 a[94:95], v255 offset:30720\n"
      "ds_read_b64_tr_b16 a[180:181], v244 offset:28672\n"
      "ds_read_b64_tr_b16 a[182:183], v244 offset:30720\n"
      "ds_read_b128 a[196:199], v250 offset:15360\n"
      "v_mfma_f32_16x16x32_bf16 a[164:167], a[172:175], a[188:191], a[164:167]\n"
      "s_waitcnt lgkmcnt(13)\n"
      "v_add_u32 v252, 4294901760, v252\n"
      "v_add_u32 v253, 4294901760, v253\n"
      "v_add_u32 v254, 4294901760, v254\n"
      "v_add_u32 v255, 4294901760, v255\n"
      "v_mfma_f32_16x16x32_bf16 a[140:143], a[80:83], v[168:171], a[140:143]\n"
      "v_add_u32 v244, 4294901760, v244\n"
      "v_mul_f32 v228, v228, v226\n"
      "v_mul_f32 v229, v229, v226\n"
      "v_mul_f32 v230, v230, v226\n"
      "v_mfma_f32_16x16x32_bf16 a[148:151], a[80:83], v[176:179], a[148:151]\n"
      "buffer_load_dwordx4 v240, %[rWo], s46 offen lds\n"
      "v_mul_f32 v231, v231, v226\n"
      "v_mul_f32 v232, v232, v226\n"
      "v_mfma_f32_16x16x32_bf16 a[156:159], a[80:83], v[184:187], a[156:159]\n"
      "s_waitcnt vmcnt(14) lgkmcnt(0)\n"
      "s_barrier\n"
      "v_mul_f32 v233, v233, v226\n"
      "v_mul_f32 v234, v234, v226\n"
      "v_mul_f32 v235, v235, v226\n"
      "v_mfma_f32_16x16x32_bf16 a[144:147], a[84:87], v[168:171], a[144:147]\n"
      "s_add_u32 m0, s43, 147456\n"
      "ds_read_b64_tr_b16 a[64:65], v252 offset:32768\n"
      "v_cvt_pk_bf16_f32 v204, v228, v229\n"
      "v_cvt_pk_bf16_f32 v205, v230, v231\n"
      "v_mfma_f32_16x16x32_bf16 a[152:155], a[84:87], v[176:179], a[152:155]\n"
      "s_xor_b32 s46, s48, 65536\n"
      "ds_read_b64_tr_b16 a[66:67], v252 offset:34816\n"
      "ds_read_b64_tr_b16 a[68:69], v253 offset:32768\n"
      "ds_read_b64_tr_b16 a[70:71], v253 offset:34816\n"
      "v_mfma_f32_16x16x32_bf16 a[160:163], a[84:87], v[184:187], a[160:163]\n"
      "ds_read_b64_tr_b16 a[168:169], v244 offset:32768\n"
      "ds_read_b64_tr_b16 a[170:171], v244 offset:34816\n"
      "ds_read_b128 a[184:187], v250 offset:0\n"
      "v_mfma_f32_16x16x32_bf16 a[164:167], a[176:179], a[192:195], a[164:167]\n"
      "ds_read_b64_tr_b16 a[72:73], v252 offset:36864\n"
      "v_cvt_pk_bf16_f32 v206, v232, v233\n"
      "v_cvt_pk_bf16_f32 v207, v234, v235\n"
      "v_mfma_f32_16x16x32_bf16 a[140:143], a[88:91], v[192:195], a[140:143]\n"
      "buffer_store_dwordx4 v[204:207], v248, %[rO], s44 offen offset:2048 sc1\n"
      "ds_read_b64_tr_b16 a[74:75], v252 offset:38912\n"
      "buffer_load_dwordx4 v240, %[rWo], s46 offen lds\n"
      "v_mfma_f32_16x16x32_bf16 a[148:151], a[88:91], v[200:203], a[148:151]\n"
      "s_add_u32 m0, s43, 151552\n"
      "ds_read_b64_tr_b16 a[76:77], v253 offset:36864\n"
      "ds_read_b64_tr_b16 a[78:79], v253 offset:38912\n"
      "v_mov_b32 v222, 119040\n"
      "v_mfma_f32_16x16x32_bf16 a[156:159], a[88:91], v[208:211], a[156:159]\n"
      "s_xor_b32 s46, s48, 81920\n"
      "ds_read_b128 v[236:239], v222 offset:0\n"
      "ds_read_b64_tr_b16 a[80:81], v252 offset:40960\n"
      "v_mfma_f32_16x16x32_bf16 a[144:147], a[92:95], v[192:195], a[144:147]\n"
      "ds_read_b64_tr_b16 a[82:83], v252 offset:43008\n"
      "buffer_load_dwordx4 v240, %[rWo], s46 offen lds\n"
      "v_mfma_f32_16x16x32_bf16 a[152:155], a[92:95], v[200:203], a[152:155]\n"
      "v_mfma_f32_16x16x32_bf16 a[160:163], a[92:95], v[208:211], a[160:163]\n"
      "v_mfma_f32_16x16x32_bf16 a[164:167], a[180:183], a[196:199], a[164:167]\n"
      "s_waitcnt lgkmcnt(12)\n"
      "ds_read_b64_tr_b16 a[84:85], v253 offset:40960\n"
      "ds_read_b64_tr_b16 a[86:87], v253 offset:43008\n"
      "v_mfma_f32_16x16x32_bf16 a[116:119], a[64:67], v[0:3], 0\n"
      "v_mfma_f32_16x16x32_bf16 a[124:127], a[64:67], v[4:7], 0\n"
      "s_add_u32 m0, s43, 155648\n"
      "v_mfma_f32_16x16x32_bf16 a[132:135], a[64:67], v[8:11], 0\n"
      "s_waitcnt lgkmcnt(12)\n"
      "ds_read_b64_tr_b16 a[172:173], v244 offset:40960\n"
      "ds_read_b64_tr_b16 a[174:175], v244 offset:43008\n"
      "v_mfma_f32_16x16x32_bf16 a[120:123], a[68:71], v[0:3], 0\n"
      "s_xor_b32 s46, s48, 98304\n"
      "v_accvgpr_read_b32 v228, a164\n"
      "v_accvgpr_read_b32 v229, a165\n"
      "v_mfma_f32_16x16x32_bf16 a[128:131], a[68:71], v[4:7], 0\n"
      "v_accvgpr_read_b32 v230, a166\n"
      "v_accvgpr_read_b32 v231, a167\n"
      "v_mfma_f32_16x16x32_bf16 a[136:139], a[68:71], v[8:11], 0\n"
      "s_waitcnt lgkmcnt(11)\n"
      "ds_read_b128 a[188:191], v250 offset:2048\n"
      "ds_read_b64_tr_b16 a[88:89], v252 offset:45056\n"
      "ds_read_b64_tr_b16 a[90:91], v252 offset:47104\n"
      "v_mfma_f32_16x16x32_bf16 a[96:99], a[168:171], a[184:187], 0\n"
      "s_waitcnt lgkmcnt(12)\n"
      "v_mul_f32 v228, v228, v227\n"
      "v_mul_f32 v229, v229, v227\n"
      "v_mul_f32 v230, v230, v227\n"
      "v_mfma_f32_16x16x32_bf16 a[116:119], a[72:75], v[12:15], a[116:119]\n"
      "ds_read_b64_tr_b16 a[92:93], v253 offset:45056\n"
      "v_mul_f32 v231, v231, v227\n"
      "v_cvt_pk_bf16_f32 v228, v228, v229\n"
      "v_mfma_f32_16x16x32_bf16 a[124:127], a[72:75], v[16:19], a[124:127]\n"
      "ds_read_b64_tr_b16 a[94:95], v253 offset:47104\n"
      "buffer_load_dwordx4 v240, %[rWo], s46 offen lds\n"
      "v_cvt_pk_bf16_f32 v229, v230, v231\n"
      "v_mfma_f32_16x16x32_bf16 a[132:135], a[72:75], v[20:23], a[132:135]\n"
      "s_waitcnt lgkmcnt(12)\n"
      "ds_write_b64 v247, v[228:229] offset:0\n"
      "v_accvgpr_read_b32 v228, a140\n"
      "v_accvgpr_read_b32 v229, a141\n"
      "v_mfma_f32_16x16x32_bf16 a[120:123], a[76:79], v[12:15], a[120:123]\n"
      "s_add_u32 m0, s43, 159744\n"
      "v_accvgpr_read_b32 v230, a142\n"
      "v_accvgpr_read_b32 v231, a143\n"
      "v_accvgpr_read_b32 v232, a144\n"
      "v_mfma_f32_16x16x32_bf16 a[128:131], a[76:79], v[16:19], a[128:131]\n"
      "s_xor_b32 s46, s48, 114688\n"
      "ds_read_b64_tr_b16 a[64:65], v252 offset:49152\n"
      "v_accvgpr_read_b32 v233, a145\n"
      "v_accvgpr_read_b32 v234, a146\n"
      "v_mfma_f32_16x16x32_bf16 a[136:139], a[76:79], v[20:23], a[136:139]\n"
      "s_waitcnt lgkmcnt(11)\n"
      "ds_read_b64_tr_b16 a[66:67], v252 offset:51200\n"
      "v_accvgpr_read_b32 v235, a147\n"
      "v_mul_f32 v228, v228, v224\n"
      "v_mfma_f32_16x16x32_bf16 a[116:119], a[80:83], v[24:27], a[116:119]\n"
      "v_mul_f32 v229, v229, v224\n"
      "v_mul_f32 v230, v230, v224\n"
      "v_mul_f32 v231, v231, v224\n"
      "v_mfma_f32_16x16x32_bf16 a[124:127], a[80:83], v[28:31], a[124:127]\n"
      "ds_read_b64_tr_b16 a[68:69], v253 offset:49152\n"
      "ds_read_b64_tr_b16 a[70:71], v253 offset:51200\n"
      "v_mul_f32 v232, v232, v224\n"
      "v_mfma_f32_16x16x32_bf16 a[132:135], a[80:83], v[32:35], a[132:135]\n"
      "s_waitcnt lgkmcnt(12)\n"
      "ds_read_b64_tr_b16 a[176:177], v244 offset:49152\n"
      "ds_read_b64_tr_b16 a[178:179], v244 offset:51200\n"
      "v_mul_f32 v233, v233, v224\n"
      "v_mfma_f32_16x16x32_bf16 a[120:123], a[84:87], v[24:27], a[120:123]\n"
      "v_mul_f32 v234, v234, v224\n"
      "v_mul_f32 v235, v235, v224\n"
      "v_cvt_pk_bf16_f32 v180, v228, v229\n"
      "v_mfma_f32_16x16x32_bf16 a[128:131], a[84:87], v[28:31], a[128:131]\n"
      "buffer_load_dwordx4 v240, %[rWo], s46 offen lds\n"
      "v_cvt_pk_bf16_f32 v181, v230, v231\n"
      "v_cvt_pk_bf16_f32 v182, v232, v233\n"
      "v_mfma_f32_16x16x32_bf16 a[136:139], a[84:87], v[32:35], a[136:139]\n"
      "s_waitcnt lgkmcnt(11)\n"
      "ds_read_b128 a[192:195], v250 offset:4096\n"
      "ds_read_b64_tr_b16 a[72:73], v252 offset:53248\n"
      "v_cvt_pk_bf16_f32 v183, v234, v235\n"
      "v_mfma_f32_16x16x32_bf16 a[96:99], a[172:175], a[188:191], a[96:99]\n"
      "s_waitcnt lgkmcnt(11)\n"
      "s_add_u32 s44, s52, 12288\n"
      "buffer_store_dwordx4 v[180:183], v248, %[rO], s44 offen sc1\n"
      "ds_read_b64_tr_b16 a[74:75], v252 offset:55296\n"
      "v_accvgpr_read_b32 v228, a148\n"
      "v_mfma_f32_16x16x32_bf16 a[116:119], a[88:91], v[36:39], a[116:119]\n"
      "v_accvgpr_read_b32 v229, a149\n"
      "v_accvgpr_read_b32 v230, a150\n"
      "v_accvgpr_read_b32 v231, a151\n"
      "v_mfma_f32_16x16x32_bf16 a[124:127], a[88:91], v[40:43], a[124:127]\n"
      "ds_read_b64_tr_b16 a[76:77], v253 offset:53248\n"
      "ds_read_b64_tr_b16 a[78:79], v253 offset:55296\n"
      "v_accvgpr_read_b32 v232, a152\n"
      "v_mfma_f32_16x16x32_bf16 a[132:135], a[88:91], v[44:47], a[132:135]\n"
      "s_waitcnt lgkmcnt(12)\n"
      "v_accvgpr_read_b32 v233, a153\n"
      "v_accvgpr_read_b32 v234, a154\n"
      "v_accvgpr_read_b32 v235, a155\n"
      "v_mfma_f32_16x16x32_bf16 a[120:123], a[92:95], v[36:39], a[120:123]\n"
      "s_add_u32 m0, s43, 65536\n"
      "ds_read_b64_tr_b16 a[80:81], v252 offset:57344\n"
      "ds_read_b64_tr_b16 a[82:83], v252 offset:59392\n"
      "v_mul_f32 v228, v228, v225\n"
      "v_mfma_f32_16x16x32_bf16 a[128:131], a[92:95], v[40:43], a[128:131]\n"
      "s_xor_b32 s46, s49, 0\n"
      "v_mul_f32 v229, v229, v225\n"
      "v_mul_f32 v230, v230, v225\n"
      "v_mfma_f32_16x16x32_bf16 a[136:139], a[92:95], v[44:47], a[136:139]\n"
      "s_waitcnt lgkmcnt(11)\n"
      "v_mul_f32 v231, v231, v225\n"
      "v_mul_f32 v232, v232, v225\n"
      "v_mfma_f32_16x16x32_bf16 a[116:119], a[64:67], v[48:51], a[116:119]\n"
      "ds_read_b64_tr_b16 a[84:85], v253 offset:57344\n"
      "ds_read_b64_tr_b16 a[86:87], v253 offset:59392\n"
      "ds_read_b64_tr_b16 a[180:181], v244 offset:57344\n"
      "v_mfma_f32_16x16x32_bf16 a[124:127], a[64:67], v[52:55], a[124:127]\n"
      "v_mul_f32 v233, v233, v225\n"
      "v_mul_f32 v234, v234, v225\n"
      "v_mfma_f32_16x16x32_bf16 a[132:135], a[64:67], v[56:59], a[132:135]\n"
      "s_waitcnt lgkmcnt(12)\n"
      "ds_read_b64_tr_b16 a[182:183], v244 offset:59392\n"
      "ds_read_b128 a[196:199], v250 offset:6144\n"
      "v_mul_f32 v235, v235, v225\n"
      "v_mfma_f32_16x16x32_bf16 a[120:123], a[68:71], v[48:51], a[120:123]\n"
      "buffer_load_dwordx4 v240, %[rWo], s46 offen lds\n"
      "v_cvt_pk_bf16_f32 v196, v228, v229\n"
      "v_cvt_pk_bf16_f32 v197, v230, v231\n"
      "v_mfma_f32_16x16x32_bf16 a[128:131], a[68:71], v[52:55], a[128:131]\n"
      "s_add_u32 m0, s43, 69632\n"
      "v_cvt_pk_bf16_f32 v198, v232, v233\n"
      "v_cvt_pk_bf16_f32 v199, v234, v235\n"
      "v_mfma_f32_16x16x32_bf16 a[136:139], a[68:71], v[56:59], a[136:139]\n"
      "s_waitcnt lgkmcnt(11)\n"
      "ds_read_b64_tr_b16 a[88:89], v252 offset:61440\n"
      "ds_read_b64_tr_b16 a[90:91], v252 offset:63488\n"
      "buffer_store_dwordx4 v[196:199], v248, %[rO], s44 offen offset:1024 sc1\n"
      "v_mfma_f32_16x16x32_bf16 a[96:99], a[176:179], a[192:195], a[96:99]\n"
      "s_waitcnt lgkmcnt(11)\n"
      "v_accvgpr_read_b32 v228, a156\n"
      "v_accvgpr_read_b32 v229, a157\n"
      "v_mfma_f32_16x16x32_bf16 a[116:119], a[72:75], v[60:63], a[116:119]\n"
      "s_xor_b32 s46, s49, 16384\n"
      "ds_read_b64_tr_b16 a[92:93], v253 offset:61440\n"
      "ds_read_b64_tr_b16 a[94:95], v253 offset:63488\n"
      "v_accvgpr_read_b32 v230, a158\n"
      "v_mfma_f32_16x16x32_bf16 a[124:127], a[72:75], v[64:67], a[124:127]\n"
      "v_accvgpr_read_b32 v231, a159\n"
      "v_accvgpr_read_b32 v232, a160\n"
      "v_mfma_f32_16x16x32_bf16 a[132:135], a[72:75], v[68:71], a[132:135]\n"
      "s_waitcnt lgkmcnt(11)\n"
      "v_accvgpr_read_b32 v233, a161\n"
      "v_accvgpr_read_b32 v234, a162\n"
      "v_mfma_f32_16x16x32_bf16 a[120:123], a[76:79], v[60:63], a[120:123]\n"
      "buffer_load_dwordx4 v240, %[rWo], s46 offen lds\n"
      "v_accvgpr_read_b32 v235, a163\n"
      "v_mfma_f32_16x16x32_bf16 a[128:131], a[76:79], v[64:67], a[128:131]\n"
      "s_add_u32 m0, s43, 73728\n"
      "v_mul_f32 v228, v228, v226\n"
      "v_mul_f32 v229, v229, v226\n"
      "v_mfma_f32_16x16x32_bf16 a[136:139], a[76:79], v[68:71], a[136:139]\n"
      "s_waitcnt lgkmcnt(9)\n"
      "v_mul_f32 v230, v230, v226\n"
      "v_mul_f32 v231, v231, v226\n"
      "v_mfma_f32_16x16x32_bf16 a[116:119], a[80:83], v[72:75], a[116:119]\n"
      "s_xor_b32 s46, s49, 32768\n"
      "v_mul_f32 v232, v232, v226\n"
      "v_mul_f32 v233, v233, v226\n"
      "v_mfma_f32_16x16x32_bf16 a[124:127], a[80:83], v[76:79], a[124:127]\n"
      "s_waitcnt vmcnt(15) lgkmcnt(0)\n"
      "s_barrier\n"
      "v_or3_b32 v236, v236, v237, v238\n"
      "v_or_b32 v236, v236, v239\n"
      "s_nop 0\n"
      "v_readfirstlane_b32 s58, v236\n"
      "s_nop 0\n"
      "s_andn2_b32 s58, s58, s42\n"
      "s_cbranch_scc1 .Lmha_redo\n"
      "ds_read_b128 a[60:63], v245 offset:0\n"
      "v_mul_f32 v234, v234, v226\n"
      "v_mfma_f32_16x16x32_bf16 a[132:135], a[80:83], v[80:83], a[132:135]\n"
      "s_nop 0\n"
      "ds_read_b128 a[232:235], v250 offset:24576\n"
      "ds_read_b128 a[236:239], v250 offset:25600\n"
      "ds_read_b64_tr_b16 a[64:65], v252 offset:0\n"
      "v_mfma_f32_16x16x32_bf16 a[120:123], a[84:87], v[72:75], a[120:123]\n"
      "ds_read_b64_tr_b16 a[66:67], v252 offset:2048\n"
      "ds_read_b64_tr_b16 a[68:69], v253 offset:0\n"
      "ds_read_b64_tr_b16 a[70:71], v253 offset:2048\n"
      "v_mfma_f32_16x16x32_bf16 a[128:131], a[84:87], v[76:79], a[128:131]\n"
      "ds_read_b64_tr_b16 a[168:169], v244 offset:0\n"
      "ds_read_b64_tr_b16 a[170:171], v244 offset:2048\n"
      "ds_read_b128 a[184:187], v250 offset:8192\n"
      "v_mfma_f32_16x16x32_bf16 a[136:139], a[84:87], v[80:83], a[136:139]\n"
      "v_mul_f32 v235, v235, v226\n"
      "v_cvt_pk_bf16_f32 v212, v228, v229\n"
      "v_cvt_pk_bf16_f32 v213, v230, v231\n"
      "v_mfma_f32_16x16x32_bf16 a[96:99], a[180:183], a[196:199], a[96:99]\n"
      "ds_read_b64_tr_b16 a[72:73], v252 offset:4096\n"
      "ds_read_b64_tr_b16 a[74:75], v252 offset:6144\n"
      "v_cvt_pk_bf16_f32 v214, v232, v233\n"
      "v_mfma_f32_16x16x32_bf16 a[116:119], a[88:91], v[84:87], a[116:119]\n"
      "buffer_load_dwordx4 v240, %[rWo], s46 offen lds\n"
      "ds_read_b64_tr_b16 a[76:77], v253 offset:4096\n"
      "v_cvt_pk_bf16_f32 v215, v234, v235\n"
      "v_mfma_f32_16x16x32_bf16 a[124:127], a[88:91], v[88:91], a[124:127]\n"
      "s_add_u32 m0, s43, 77824\n"
      "buffer_store_dwordx4 v[212:215], v248, %[rO], s44 offen offset:2048 sc1\n"
      "ds_read_b64_tr_b16 a[78:79], v253 offset:6144\n"
      "v_mfma_f32_16x16x32_bf16 a[132:135], a[88:91], v[92:95], a[132:135]\n"
      "s_xor_b32 s46, s49, 49152\n"
      "buffer_load_dwordx4 v240, %[rWo], s46 offen lds\n"
      "v_mfma_f32_16x16x32_bf16 a[120:123], a[92:95], v[84:87], a[120:123]\n"
      "v_mfma_f32_16x16x32_bf16 a[128:131], a[92:95], v[88:91], a[128:131]\n"
      "s_add_u32 s59, s37, 24576\n"
      "s_waitcnt lgkmcnt(13)\n"
      "buffer_store_dwordx4 a[60:63], v241, %[rO], s59 offen sc1\n"
      "v_mfma_f32_16x16x32_bf16 a[136:139], a[92:95], v[92:95], a[136:139]\n"
      "s_waitcnt lgkmcnt(9)\n"
      "ds_read_b64_tr_b16 a[80:81], v252 offset:8192\n"
      "ds_read_b64_tr_b16 a[82:83], v252 offset:10240\n"
      "v_mfma_f32_16x16x32_bf16 a[116:119], a[64:67], v[96:99], a[116:119]\n"
      "s_add_u32 m0, s43, 81920\n"
      "ds_read_b64_tr_b16 a[84:85], v253 offset:8192\n"
      "ds_read_b64_tr_b16 a[86:87], v253 offset:10240\n"
      "ds_read_b64_tr_b16 a[172:173], v244 offset:8192\n"
      "v_mfma_f32_16x16x32_bf16 a[124:127], a[64:67], v[100:103], a[124:127]\n"
      "s_xor_b32 s46, s50, 0\n"
      "buffer_load_dwordx4 v240, %[rWo], s46 offen lds\n"
      "v_mfma_f32_16x16x32_bf16 a[132:135], a[64:67], v[104:107], a[132:135]\n"
      "s_waitcnt lgkmcnt(12)\n"
      "ds_read_b64_tr_b16 a[174:175], v244 offset:10240\n"
      "ds_read_b128 a[188:191], v250 offset:10240\n"
      "v_mfma_f32_16x16x32_bf16 a[120:123], a[68:71], v[96:99], a[120:123]\n"
      "v_mfma_f32_16x16x32_bf16 a[128:131], a[68:71], v[100:103], a[128:131]\n"
      "v_mfma_f32_16x16x32_bf16 a[136:139], a[68:71], v[104:107], a[136:139]\n"
      "s_waitcnt lgkmcnt(11)\n"
      "ds_read_b64_tr_b16 a[88:89], v252 offset:12288\n"
      "ds_read_b64_tr_b16 a[90:91], v252 offset:14336\n"
      "v_mfma_f32_16x16x32_bf16 a[96:99], a[168:171], a[184:187], a[96:99]\n"
      "s_waitcnt lgkmcnt(11)\n"
      "ds_read_b64_tr_b16 a[92:93], v253 offset:12288\n"
      "ds_read_b64_tr_b16 a[94:95], v253 offset:14336\n"
      "v_mfma_f32_16x16x32_bf16 a[116:119], a[72:75], v[108:111], a[116:119]\n"
      "ds_read_b64_tr_b16 a[64:65], v252 offset:16384\n"
      "v_mfma_f32_16x16x32_bf16 a[124:127], a[72:75], v[112:115], a[124:127]\n"
      "s_add_u32 m0, s43, 86016\n"
      "v_mfma_f32_16x16x32_bf16 a[132:135], a[72:75], v[116:119], a[132:135]\n"
      "s_waitcnt lgkmcnt(12)\n"
      "ds_read_b64_tr_b16 a[66:67], v252 offset:18432\n"
      "ds_read_b64_tr_b16 a[68:69], v253 offset:16384\n"
      "v_mfma_f32_16x16x32_bf16 a[120:123], a[76:79], v[108:111], a[120:123]\n"
      "s_xor_b32 s46, s50, 16384\n"
      "buffer_load_dwordx4 v240, %[rWo], s46 offen lds\n"
      "v_mfma_f32_16x16x32_bf16 a[128:131], a[76:79], v[112:115], a[128:131]\n"
      "v_mfma_f32_16x16x32_bf16 a[136:139], a[76:79], v[116:119], a[136:139]\n"
      "s_waitcnt lgkmcnt(12)\n"
      "ds_read_b64_tr_b16 a[70:71], v253 offset:18432\n"
      "ds_read_b64_tr_b16 a[176:177], v244 offset:16384\n"
      "v_mfma_f32_16x16x32_bf16 a[116:119], a[80:83], v[120:123], a[116:119]\n"
      "v_mfma_f32_16x16x32_bf16 a[124:127], a[80:83], v[124:127], a[124:127]\n"
      "v_mfma_f32_16x16x32_bf16 a[132:135], a[80:83], v[128:131], a[132:135]\n"
      "s_waitcnt lgkmcnt(12)\n"
      "ds_read_b64_tr_b16 a[178:179], v244 offset:18432\n"
      "ds_read_b128 a[192:195], v250 offset:12288\n"
      "v_mfma_f32_16x16x32_bf16 a[120:123], a[84:87], v[120:123], a[120:123]\n"
      "s_add_u32 m0, s43, 90112\n"
      "v_mfma_f32_16x16x32_bf16 a[128:131], a[84:87], v[124:127], a[128:131]\n"
      "s_xor_b32 s46, s50, 32768\n"
      "buffer_load_dwordx4 v240, %[rWo], s46 offen lds\n"
      "v_mfma_f32_16x16x32_bf16 a[136:139], a[84:87], v[128:131], a[136:139]\n"
      "s_waitcnt lgkmcnt(11)\n"
      "ds_read_b64_tr_b16 a[72:73], v252 offset:20480\n"
      "ds_read_b64_tr_b16 a[74:75], v252 offset:22528\n"
      "v_mfma_f32_16x16x32_bf16 a[96:99], a[172:175], a[188:191], a[96:99]\n"
      "s_waitcnt lgkmcnt(11)\n"
      "ds_read_b64_tr_b16 a[76:77], v253 offset:20480\n"
      "ds_read_b64_tr_b16 a[78:79], v253 offset:22528\n"
      "v_mfma_f32_16x16x32_bf16 a[116:119], a[88:91], v[132:135], a[116:119]\n"
      "ds_read_b64_tr_b16 a[80:81], v252 offset:24576\n"
      "v_mfma_f32_16x16x32_bf16 a[124:127], a[88:91], v[136:139], a[124:127]\n"
      "v_mfma_f32_16x16x32_bf16 a[132:135], a[88:91], v[140:143], a[132:135]\n"
      "s_waitcnt lgkmcnt(12)\n"
      "ds_read_b64_tr_b16 a[82:83], v252 offset:26624\n"
      "ds_read_b64_tr_b16 a[84:85], v253 offset:24576\n"
      "v_mfma_f32_16x16x32_bf16 a[120:123], a[92:95], v[132:135], a[120:123]\n"
      "s_add_u32 m0, s43, 94208\n"
      "v_mfma_f32_16x16x32_bf16 a[128:131], a[92:95], v[136:139], a[128:131]\n"
      "s_xor_b32 s46, s50, 49152\n"
      "buffer_load_dwordx4 v240, %[rWo], s46 offen lds\n"
      "v_mfma_f32_16x16x32_bf16 a[136:139], a[92:95], v[140:143], a[136:139]\n"
      "s_waitcnt lgkmcnt(12)\n"
      "ds_read_b64_tr_b16 a[86:87], v253 offset:26624\n"
      "ds_read_b64_tr_b16 a[180:181], v244 offset:24576\n"
      "v_mfma_f32_16x16x32_bf16 a[116:119], a[64:67], v[144:147], a[116:119]\n"
      "v_mfma_f32_16x16x32_bf16 a[124:127], a[64:67], v[148:151], a[124:127]\n"
      "v_mfma_f32_16x16x32_bf16 a[132:135], a[64:67], v[152:155], a[132:135]\n"
      "s_waitcnt lgkmcnt(12)\n"
      "ds_read_b64_tr_b16 a[182:183], v244 offset:26624\n"
      "ds_read_b128 a[196:199], v250 offset:14336\n"
      "v_mfma_f32_16x16x32_bf16 a[120:123], a[68:71], v[144:147], a[120:123]\n"
      "v_mfma_f32_16x16x32_bf16 a[128:131], a[68:71], v[148:151], a[128:131]\n"
      "v_mfma_f32_16x16x32_bf16 a[136:139], a[68:71], v[152:155], a[136:139]\n"
      "s_waitcnt lgkmcnt(11)\n"
      "ds_read_b64_tr_b16 a[88:89], v252 offset:28672\n"
      "ds_read_b64_tr_b16 a[90:91], v252 offset:30720\n"
      "v_mfma_f32_16x16x32_bf16 a[96:99], a[176:179], a[192:195], a[96:99]\n"
      "s_waitcnt lgkmcnt(11)\n"
      "ds_read_b64_tr_b16 a[92:93], v253 offset:28672\n"
      "ds_read_b64_tr_b16 a[94:95], v253 offset:30720\n"
      "v_mfma_f32_16x16x32_bf16 a[116:119], a[72:75], v[156:159], a[116:119]\n"
      "ds_read_b64_tr_b16 a[64:65], v254 offset:32768\n"
      "v_mfma_f32_16x16x32_bf16 a[124:127], a[72:75], v[160:163], a[124:127]\n"
      "v_mfma_f32_16x16x32_bf16 a[132:135], a[72:75], v[164:167], a[132:135]\n"
      "s_waitcnt lgkmcnt(12)\n"
      "ds_read_b64_tr_b16 a[66:67], v254 offset:34816\n"
      "ds_read_b64_tr_b16 a[68:69], v255 offset:32768\n"
      "v_mfma_f32_16x16x32_bf16 a[120:123], a[76:79], v[156:159], a[120:123]\n"
      "v_mfma_f32_16x16x32_bf16 a[128:131], a[76:79], v[160:163], a[128:131]\n"
      "v_mfma_f32_16x16x32_bf16 a[136:139], a[76:79], v[164:167], a[136:139]\n"
      "s_waitcnt lgkmcnt(12)\n"
      "ds_read_b64_tr_b16 a[70:71], v255 offset:34816\n"
      "ds_read_b64_tr_b16 a[72:73], v254 offset:36864\n"
      "v_mfma_f32_16x16x32_bf16 a[116:119], a[80:83], v[168:171], a[116:119]\n"
      "v_mfma_f32_16x16x32_bf16 a[124:127], a[80:83], v[176:179], a[124:127]\n"
      "v_mfma_f32_16x16x32_bf16 a[132:135], a[80:83], v[184:187], a[132:135]\n"
      "s_waitcnt lgkmcnt(12)\n"
      "ds_read_b64_tr_b16 a[74:75], v254 offset:38912\n"
      "ds_read_b64_tr_b16 a[76:77], v255 offset:36864\n"
      "v_mfma_f32_16x16x32_bf16 a[120:123], a[84:87], v[168:171], a[120:123]\n"
      "v_mfma_f32_16x16x32_bf16 a[128:131], a[84:87], v[176:179], a[128:131]\n"
      "v_mfma_f32_16x16x32_bf16 a[136:139], a[84:87], v[184:187], a[136:139]\n"
      "s_waitcnt lgkmcnt(11)\n"
      "ds_read_b64_tr_b16 a[78:79], v255 offset:38912\n"
      "ds_read_b64_tr_b16 a[168:169], v244 offset:36864\n"
      "v_mfma_f32_16x16x32_bf16 a[96:99], a[180:183], a[196:199], a[96:99]\n"
      "s_waitcnt lgkmcnt(11)\n"
      "ds_read_b64_tr_b16 a[170:171], v244 offset:38912\n"
      "ds_read_b128 a[184:187], v250 offset:1024\n"
      "v_mfma_f32_16x16x32_bf16 a[116:119], a[88:91], v[192:195], a[116:119]\n"
      "ds_read_b64_tr_b16 a[80:81], v254 offset:40960\n"
      "v_mfma_f32_16x16x32_bf16 a[124:127], a[88:91], v[200:203], a[124:127]\n"
      "v_mfma_f32_16x16x32_bf16 a[132:135], a[88:91], v[208:211], a[132:135]\n"
      "s_waitcnt lgkmcnt(12)\n"
      "ds_read_b64_tr_b16 a[82:83], v254 offset:43008\n"
      "ds_read_b64_tr_b16 a[84:85], v255 offset:40960\n"
      "v_mfma_f32_16x16x32_bf16 a[120:123], a[92:95], v[192:195], a[120:123]\n"
      "v_mfma_f32_16x16x32_bf16 a[128:131], a[92:95], v[200:203], a[128:131]\n"
      "v_mfma_f32_16x16x32_bf16 a[136:139], a[92:95], v[208:211], a[136:139]\n"
      "s_waitcnt lgkmcnt(12)\n"
      "ds_read_b64_tr_b16 a[86:87], v255 offset:43008\n"
      "ds_read_b64_tr_b16 a[88:89], v254 offset:45056\n"
      "v_mfma_f32_16x16x32_bf16 a[140:143], a[64:67], v[0:3], 0\n"
      "v_mfma_f32_16x16x32_bf16 a[148:151], a[64:67], v[4:7], 0\n"
      "v_mfma_f32_16x16x32_bf16 a[156:159], a[64:67], v[8:11], 0\n"
      "s_waitcnt lgkmcnt(12)\n"
      "ds_read_b64_tr_b16 a[90:91], v254 offset:47104\n"
      "v_accvgpr_read_b32 v228, a116\n"
      "v_mfma_f32_16x16x32_bf16 a[144:147], a[68:71], v[0:3], 0\n"
      "v_accvgpr_read_b32 v229, a117\n"
      "v_accvgpr_read_b32 v230, a118\n"
      "v_mfma_f32_16x16x32_bf16 a[152:155], a[68:71], v[4:7], 0\n"
      "v_accvgpr_read_b32 v231, a119\n"
      "v_accvgpr_read_b32 v232, a120\n"
      "v_mfma_f32_16x16x32_bf16 a[160:163], a[68:71], v[8:11], 0\n"
      "s_waitcnt lgkmcnt(11)\n"
      "ds_read_b64_tr_b16 a[92:93], v255 offset:45056\n"
      "ds_read_b64_tr_b16 a[94:95], v255 offset:47104\n"
      "v_mfma_f32_16x16x32_bf16 a[140:143], a[72:75], v[12:15], a[140:143]\n"
      "ds_read_b64_tr_b16 a[172:173], v244 offset:45056\n"
      "v_accvgpr_read_b32 v233, a121\n"
      "v_mfma_f32_16x16x32_bf16 a[148:151], a[72:75], v[16:19], a[148:151]\n"
      "v_accvgpr_read_b32 v234, a122\n"
      "v_accvgpr_read_b32 v235, a123\n"
      "v_mfma_f32_16x16x32_bf16 a[156:159], a[72:75], v[20:23], a[156:159]\n"
      "s_waitcnt lgkmcnt(12)\n"
      "ds_read_b64_tr_b16 a[174:175], v244 offset:47104\n"
      "ds_read_b128 a[188:191], v250 offset:3072\n"
      "v_mfma_f32_16x16x32_bf16 a[144:147], a[76:79], v[12:15], a[144:147]\n"
      "v_mul_f32 v228, v228, v224\n"
      "v_mul_f32 v229, v229, v224\n"
      "v_mfma_f32_16x16x32_bf16 a[152:155], a[76:79], v[16:19], a[152:155]\n"
      "v_mul_f32 v230, v230, v224\n"
      "v_mul_f32 v231, v231, v224\n"
      "v_mfma_f32_16x16x32_bf16 a[160:163], a[76:79], v[20:23], a[160:163]\n"
      "s_waitcnt lgkmcnt(11)\n"
      "ds_read_b64_tr_b16 a[64:65], v254 offset:49152\n"
      "ds_read_b64_tr_b16 a[66:67], v254 offset:51200\n"
      "v_mfma_f32_16x16x32_bf16 a[96:99], a[168:171], a[184:187], a[96:99]\n"
      "s_waitcnt lgkmcnt(11)\n"
      "v_mul_f32 v232, v232, v224\n"
      "v_mul_f32 v233, v233, v224\n"
      "v_mfma_f32_16x16x32_bf16 a[140:143], a[80:83], v[24:27], a[140:143]\n"
      "ds_read_b64_tr_b16 a[68:69], v255 offset:49152\n"
      "ds_read_b64_tr_b16 a[70:71], v255 offset:51200\n"
      "v_mul_f32 v234, v234, v224\n"
      "v_mfma_f32_16x16x32_bf16 a[148:151], a[80:83], v[28:31], a[148:151]\n"
      "s_waitcnt vmcnt(0)\n"
      "s_barrier\n"
      "s_cmp_lg_u32 %[w], 0\n"
      "s_cbranch_scc1 .Lmha_0\n"
      "s_mov_b64 exec, s[56:57]\n"
      "buffer_store_dword v243, v242, %[rC], 0 offen offset:32 sc1\n"
      "s_mov_b64 exec, -1\n"
      ".Lmha_0:\n"
      "v_mul_f32 v235, v235, v224\n"
      "v_cvt_pk_bf16_f32 v216, v228, v229\n"
      "v_mfma_f32_16x16x32_bf16 a[156:159], a[80:83], v[32:35], a[156:159]\n"
      "s_waitcnt lgkmcnt(11)\n"
      "v_cvt_pk_bf16_f32 v217, v230, v231\n"
      "v_cvt_pk_bf16_f32 v218, v232, v233\n"
      "v_mfma_f32_16x16x32_bf16 a[144:147], a[84:87], v[24:27], a[144:147]\n"
      "ds_read_b64_tr_b16 a[72:73], v254 offset:53248\n"
      "ds_read_b64_tr_b16 a[74:75], v254 offset:55296\n"
      "v_mfma_f32_16x16x32_bf16 a[152:155], a[84:87], v[28:31], a[152:155]\n"
      "v_cvt_pk_bf16_f32 v219, v234, v235\n"
      "s_add_u32 s44, s52, 26624\n"
      "v_mfma_f32_16x16x32_bf16 a[160:163], a[84:87], v[32:35], a[160:163]\n"
      "s_waitcnt lgkmcnt(11)\n"
      "buffer_store_dwordx4 v[216:219], v248, %[rO], s44 offen sc1\n"
      "ds_read_b64_tr_b16 a[76:77], v255 offset:53248\n"
      "v_mfma_f32_16x16x32_bf16 a[140:143], a[88:91], v[36:39], a[140:143]\n"
      "ds_read_b64_tr_b16 a[78:79], v255 offset:55296\n"
      "ds_read_b64_tr_b16 a[176:177], v244 offset:53248\n"
      "v_mfma_f32_16x16x32_bf16 a[148:151], a[88:91], v[40:43], a[148:151]\n"
      "v_mfma_f32_16x16x32_bf16 a[156:159], a[88:91], v[44:47], a[156:159]\n"
      "s_waitcnt lgkmcnt(12)\n"
      "ds_read_b64_tr_b16 a[178:179], v244 offset:55296\n"
      "ds_read_b128 a[192:195], v250 offset:5120\n"
      "v_mfma_f32_16x16x32_bf16 a[144:147], a[92:95], v[36:39], a[144:147]\n"
      "v_mfma_f32_16x16x32_bf16 a[152:155], a[92:95], v[40:43], a[152:155]\n"
      "v_mfma_f32_16x16x32_bf16 a[160:163], a[92:95], v[44:47], a[160:163]\n"
      "s_waitcnt lgkmcnt(11)\n"
      "ds_read_b64_tr_b16 a[80:81], v254 offset:57344\n"
      "ds_read_b64_tr_b16 a[82:83], v254 offset:59392\n"
      "v_mfma_f32_16x16x32_bf16 a[96:99], a[172:175], a[188:191], a[96:99]\n"
      "s_waitcnt lgkmcnt(11)\n"
      "ds_read_b64_tr_b16 a[84:85], v255 offset:57344\n"
      "ds_read_b64_tr_b16 a[86:87], v255 offset:59392\n"
      "v_mfma_f32_16x16x32_bf16 a[140:143], a[64:67], v[48:51], a[140:143]\n"
      "ds_read_b64_tr_b16 a[88:89], v254 offset:61440\n"
      "v_accvgpr_read_b32 v228, a124\n"
      "v_mfma_f32_16x16x32_bf16 a[148:151], a[64:67], v[52:55], a[148:151]\n"
      "v_accvgpr_read_b32 v229, a125\n"
      "v_accvgpr_read_b32 v230, a126\n"
      "v_mfma_f32_16x16x32_bf16 a[156:159], a[64:67], v[56:59], a[156:159]\n"
      "s_waitcnt lgkmcnt(12)\n"
      "ds_read_b64_tr_b16 a[90:91], v254 offset:63488\n"
      "ds_read_b64_tr_b16 a[92:93], v255 offset:61440\n"
      "v_mfma_f32_16x16x32_bf16 a[144:147], a[68:71], v[48:51], a[144:147]\n"
      "v_accvgpr_read_b32 v231, a127\n"
      "v_accvgpr_read_b32 v232, a128\n"
      "v_mfma_f32_16x16x32_bf16 a[152:155], a[68:71], v[52:55], a[152:155]\n"
      "v_accvgpr_read_b32 v233, a129\n"
      "v_accvgpr_read_b32 v234, a130\n"
      "v_mfma_f32_16x16x32_bf16 a[160:163], a[68:71], v[56:59], a[160:163]\n"
      "s_waitcnt lgkmcnt(12)\n"
      "ds_read_b64_tr_b16 a[94:95], v255 offset:63488\n"
      "ds_read_b64_tr_b16 a[180:181], v244 offset:61440\n"
      "v_mfma_f32_16x16x32_bf16 a[140:143], a[72:75], v[60:63], a[140:143]\n"
      "v_accvgpr_read_b32 v235, a131\n"
      "v_mul_f32 v228, v228, v225\n"
      "v_mfma_f32_16x16x32_bf16 a[148:151], a[72:75], v[64:67], a[148:151]\n"
      "v_mul_f32 v229, v229, v225\n"
      "v_mul_f32 v230, v230, v225\n"
      "v_mfma_f32_16x16x32_bf16 a[156:159], a[72:75], v[68:71], a[156:159]\n"
      "s_waitcnt lgkmcnt(12)\n"
      "ds_read_b64_tr_b16 a[182:183], v244 offset:63488\n"
      "ds_read_b128 a[196:199], v250 offset:7168\n"
      "v_mfma_f32_16x16x32_bf16 a[144:147], a[76:79], v[60:63], a[144:147]\n"
      "v_mul_f32 v231, v231, v225\n"
      "v_mul_f32 v232, v232, v225\n"
      "v_mfma_f32_16x16x32_bf16 a[152:155], a[76:79], v[64:67], a[152:155]\n"
      "v_mul_f32 v233, v233, v225\n"
      "v_mul_f32 v234, v234, v225\n"
      "v_mfma_f32_16x16x32_bf16 a[160:163], a[76:79], v[68:71], a[160:163]\n"
      "s_waitcnt lgkmcnt(11)\n"
      "ds_read_b64_tr_b16 a[64:65], v254 offset:0\n"
      "ds_read_b64_tr_b16 a[66:67], v254 offset:2048\n"
      "v_mfma_f32_16x16x32_bf16 a[96:99], a[176:179], a[192:195], a[96:99]\n"
      "s_waitcnt lgkmcnt(11)\n"
      "ds_read_b64_tr_b16 a[68:69], v255 offset:0\n"
      "ds_read_b64_tr_b16 a[70:71], v255 offset:2048\n"
      "v_mfma_f32_16x16x32_bf16 a[140:143], a[80:83], v[72:75], a[140:143]\n"
      "ds_read_b64_tr_b16 a[72:73], v254 offset:4096\n"
      "v_mul_f32 v235, v235, v225\n"
      "v_mfma_f32_16x16x32_bf16 a[148:151], a[80:83], v[76:79], a[148:151]\n"
      "s_waitcnt lgkmcnt(5)\n"
      "s_barrier\n"
      "ds_read_b64_tr_b16 a[74:75], v254 offset:6144\n"
      "v_cvt_pk_bf16_f32 v220, v228, v229\n"
      "v_mfma_f32_16x16x32_bf16 a[156:159], a[80:83], v[80:83], a[156:159]\n"
      "v_cvt_pk_bf16_f32 v221, v230, v231\n"
      "v_cvt_pk_bf16_f32 v222, v232, v233\n"
      "v_mfma_f32_16x16x32_bf16 a[144:147], a[84:87], v[72:75], a[144:147]\n"
      "ds_read_b64_tr_b16 a[76:77], v255 offset:4096\n"
      "v_cvt_pk_bf16_f32 v223, v234, v235\n"
      "v_mfma_f32_16x16x32_bf16 a[152:155], a[84:87], v[76:79], a[152:155]\n"
      "buffer_store_dwordx4 v[220:223], v248, %[rO], s44 offen offset:1024 sc1\n"
      "ds_read_b64_tr_b16 a[78:79], v255 offset:6144\n"
      "v_mfma_f32_16x16x32_bf16 a[160:163], a[84:87], v[80:83], a[160:163]\n"
      "ds_read_b64_tr_b16 a[168:169], v244 offset:4096\n"
      "ds_read_b64_tr_b16 a[170:171], v244 offset:6144\n"
      "v_mfma_f32_16x16x32_bf16 a[140:143], a[88:91], v[84:87], a[140:143]\n"
      "ds_read_b128 a[184:187], v250 offset:9216\n"
      "ds_read_b64_tr_b16 a[80:81], v254 offset:8192\n"
      "v_mfma_f32_16x16x32_bf16 a[148:151], a[88:91], v[88:91], a[148:151]\n"
      "ds_read_b64_tr_b16 a[82:83], v254 offset:10240\n"
      "ds_read_b64_tr_b16 a[84:85], v255 offset:8192\n"
      "v_mfma_f32_16x16x32_bf16 a[156:159], a[88:91], v[92:95], a[156:159]\n"
      "v_mfma_f32_16x16x32_bf16 a[144:147], a[92:95], v[84:87], a[144:147]\n"
      "s_add_u32 m0, s43, 32768\n"
      "v_mfma_f32_16x16x32_bf16 a[152:155], a[92:95], v[88:91], a[152:155]\n"
      "s_xor_b32 s46, s50, 65536\n"
      "buffer_load_dwordx4 v240, %[rWo], s46 offen lds\n"
      "v_mfma_f32_16x16x32_bf16 a[160:163], a[92:95], v[92:95], a[160:163]\n"
      "v_mfma_f32_16x16x32_bf16 a[96:99], a[180:183], a[196:199], a[96:99]\n"
      "s_waitcnt lgkmcnt(12)\n"
      "ds_read_b64_tr_b16 a[86:87], v255 offset:10240\n"
      "ds_read_b64_tr_b16 a[88:89], v254 offset:12288\n"
      "v_mfma_f32_16x16x32_bf16 a[140:143], a[64:67], v[96:99], a[140:143]\n"
      "v_accvgpr_read_b32 v228, a132\n"
      "v_mfma_f32_16x16x32_bf16 a[148:151], a[64:67], v[100:103], a[148:151]\n"
      "v_accvgpr_read_b32 v229, a133\n"
      "v_mfma_f32_16x16x32_bf16 a[156:159], a[64:67], v[104:107], a[156:159]\n"
      "s_waitcnt lgkmcnt(12)\n"
      "ds_read_b64_tr_b16 a[90:91], v254 offset:14336\n"
      "ds_read_b64_tr_b16 a[92:93], v255 offset:12288\n"
      "v_mfma_f32_16x16x32_bf16 a[144:147], a[68:71], v[96:99], a[144:147]\n"
      "s_add_u32 m0, s43, 36864\n"
      "v_accvgpr_read_b32 v230, a134\n"
      "v_accvgpr_read_b32 v231, a135\n"
      "v_mfma_f32_16x16x32_bf16 a[152:155], a[68:71], v[100:103], a[152:155]\n"
      "s_xor_b32 s46, s50, 81920\n"
      "buffer_load_dwordx4 v240, %[rWo], s46 offen lds\n"
      "v_accvgpr_read_b32 v232, a136\n"
      "v_mfma_f32_16x16x32_bf16 a[160:163], a[68:71], v[104:107], a[160:163]\n"
      "s_waitcnt lgkmcnt(12)\n"
      "buffer_load_dword v251, v242, %[rC], 0 offen offset:32 sc1\n"
      "ds_read_b64_tr_b16 a[94:95], v255 offset:14336\n"
      "v_mfma_f32_16x16x32_bf16 a[140:143], a[72:75], v[108:111], a[140:143]\n"
      "ds_read_b64_tr_b16 a[172:173], v244 offset:12288\n"
      "v_accvgpr_read_b32 v233, a137\n"
      "v_mfma_f32_16x16x32_bf16 a[148:151], a[72:75], v[112:115], a[148:151]\n"
      "v_accvgpr_read_b32 v234, a138\n"
      "v_accvgpr_read_b32 v235, a139\n"
      "v_mfma_f32_16x16x32_bf16 a[156:159], a[72:75], v[116:119], a[156:159]\n"
      "s_waitcnt lgkmcnt(12)\n"
      "ds_read_b64_tr_b16 a[174:175], v244 offset:14336\n"
      "ds_read_b128 a[188:191], v250 offset:11264\n"
      "v_mfma_f32_16x16x32_bf16 a[144:147], a[76:79], v[108:111], a[144:147]\n"
      "s_add_u32 m0, s43, 40960\n"
      "v_mul_f32 v228, v228, v226\n"
      "v_mul_f32 v229, v229, v226\n"
      "v_mfma_f32_16x16x32_bf16 a[152:155], a[76:79], v[112:115], a[152:155]\n"
      "s_xor_b32 s46, s50, 98304\n"
      "buffer_load_dwordx4 v240, %[rWo], s46 offen lds\n"
      "v_mul_f32 v230, v230, v226\n"
      "v_mfma_f32_16x16x32_bf16 a[160:163], a[76:79], v[116:119], a[160:163]\n"
      "s_waitcnt lgkmcnt(11)\n"
      "ds_read_b64_tr_b16 a[64:65], v254 offset:16384\n"
      "ds_read_b64_tr_b16 a[66:67], v254 offset:18432\n"
      "v_mfma_f32_16x16x32_bf16 a[96:99], a[168:171], a[184:187], a[96:99]\n"
      "s_waitcnt lgkmcnt(11)\n"
      "ds_read_b64_tr_b16 a[68:69], v255 offset:16384\n"
      "ds_read_b64_tr_b16 a[70:71], v255 offset:18432\n"
      "v_mfma_f32_16x16x32_bf16 a[140:143], a[80:83], v[120:123], a[140:143]\n"
      "s_add_u32 m0, s43, 45056\n"
      "v_mul_f32 v231, v231, v226\n"
      "v_mul_f32 v232, v232, v226\n"
      "v_mfma_f32_16x16x32_bf16 a[148:151], a[80:83], v[124:127], a[148:151]\n"
      "s_xor_b32 s46, s50, 114688\n"
      "ds_read_b64_tr_b16 a[72:73], v254 offset:20480\n"
      "v_mul_f32 v233, v233, v226\n"
      "v_mfma_f32_16x16x32_bf16 a[156:159], a[80:83], v[128:131], a[156:159]\n"
      "s_waitcnt lgkmcnt(12)\n"
      "ds_read_b64_tr_b16 a[74:75], v254 offset:22528\n"
      "v_mul_f32 v234, v234, v226\n"
      "v_mfma_f32_16x16x32_bf16 a[144:147], a[84:87], v[120:123], a[144:147]\n"
      "v_mul_f32 v235, v235, v226\n"
      "v_cvt_pk_bf16_f32 v236, v228, v229\n"
      "v_mfma_f32_16x16x32_bf16 a[152:155], a[84:87], v[124:127], a[152:155]\n"
      "ds_read_b64_tr_b16 a[76:77], v255 offset:20480\n"
      "v_cvt_pk_bf16_f32 v237, v230, v231\n"
      "v_mfma_f32_16x16x32_bf16 a[160:163], a[84:87], v[128:131], a[160:163]\n"
      "s_waitcnt lgkmcnt(12)\n"
      "ds_read_b64_tr_b16 a[78:79], v255 offset:22528\n"
      "ds_read_b64_tr_b16 a[176:177], v244 offset:20480\n"
      "v_mfma_f32_16x16x32_bf16 a[140:143], a[88:91], v[132:135], a[140:143]\n"
      "buffer_load_dwordx4 v240, %[rWo], s46 offen lds\n"
      "v_cvt_pk_bf16_f32 v238, v232, v233\n"
      "v_mfma_f32_16x16x32_bf16 a[148:151], a[88:91], v[136:139], a[148:151]\n"
      "s_add_u32 m0, s43, 49152\n"
      "v_cvt_pk_bf16_f32 v239, v234, v235\n"
      "v_mfma_f32_16x16x32_bf16 a[156:159], a[88:91], v[140:143], a[156:159]\n"
      "s_waitcnt lgkmcnt(12)\n"
      "ds_read_b64_tr_b16 a[178:179], v244 offset:22528\n"
      "ds_read_b128 a[192:195], v250 offset:13312\n"
      "buffer_store_dwordx4 v[236:239], v248, %[rO], s44 offen offset:2048 sc1\n"
      "v_mfma_f32_16x16x32_bf16 a[144:147], a[92:95], v[132:135], a[144:147]\n"
      "s_xor_b32 s46, s51, 65536\n"
      "buffer_load_dwordx4 v240, %[rWo], s46 offen lds\n"
      "v_mfma_f32_16x16x32_bf16 a[152:155], a[92:95], v[136:139], a[152:155]\n"
      "v_mfma_f32_16x16x32_bf16 a[160:163], a[92:95], v[140:143], a[160:163]\n"
      "s_waitcnt lgkmcnt(11)\n"
      "ds_read_b64_tr_b16 a[80:81], v254 offset:24576\n"
      "ds_read_b64_tr_b16 a[82:83], v254 offset:26624\n"
      "v_mfma_f32_16x16x32_bf16 a[96:99], a[172:175], a[188:191], a[96:99]\n"
      "s_waitcnt lgkmcnt(11)\n"
      "ds_read_b64_tr_b16 a[84:85], v255 offset:24576\n"
      "ds_read_b64_tr_b16 a[86:87], v255 offset:26624\n"
      "v_mfma_f32_16x16x32_bf16 a[140:143], a[64:67], v[144:147], a[140:143]\n"
      "s_add_u32 m0, s43, 53248\n"
      "ds_read_b64_tr_b16 a[88:89], v254 offset:28672\n"
      "v_mfma_f32_16x16x32_bf16 a[148:151], a[64:67], v[148:151], a[148:151]\n"
      "s_xor_b32 s46, s51, 81920\n"
      "v_accvgpr_read_b32 v128, a245\n"
      "v_xor_b32 v129, 32, v128\n"
      "v_mfma_f32_16x16x32_bf16 a[156:159], a[64:67], v[152:155], a[156:159]\n"
      "s_waitcnt lgkmcnt(12)\n"
      "ds_read_b64_tr_b16 a[90:91], v254 offset:30720\n"
      "v_xor_b32 v130, 64, v128\n"
      "v_mfma_f32_16x16x32_bf16 a[144:147], a[68:71], v[144:147], a[144:147]\n"
      "v_xor_b32 v131, 96, v128\n"
      "v_xor_b32 v132, 128, v128\n"
      "v_mfma_f32_16x16x32_bf16 a[152:155], a[68:71], v[148:151], a[152:155]\n"
      "buffer_load_dwordx4 v240, %[rWo], s46 offen lds\n"
      "v_xor_b32 v133, 160, v128\n"
      "v_mfma_f32_16x16x32_bf16 a[160:163], a[68:71], v[152:155], a[160:163]\n"
      "s_waitcnt lgkmcnt(11)\n"
      "ds_read_b64_tr_b16 a[92:93], v255 offset:28672\n"
      "v_xor_b32 v134, 192, v128\n"
      "v_mfma_f32_16x16x32_bf16 a[140:143], a[72:75], v[156:159], a[140:143]\n"
      "s_add_u32 m0, s43, 57344\n"
      "ds_read_b64_tr_b16 a[94:95], v255 offset:30720\n"
      "ds_read_b64_tr_b16 a[180:181], v244 offset:28672\n"
      "v_mfma_f32_16x16x32_bf16 a[148:151], a[72:75], v[160:163], a[148:151]\n"
      "s_xor_b32 s46, s51, 98304\n"
      "v_xor_b32 v135, 224, v128\n"
      "v_add_u32 v136, 131072, v128\n"
      "v_mfma_f32_16x16x32_bf16 a[156:159], a[72:75], v[164:167], a[156:159]\n"
      "s_waitcnt lgkmcnt(12)\n"
      "ds_read_b64_tr_b16 a[182:183], v244 offset:30720\n"
      "ds_read_b128 a[196:199], v250 offset:15360\n"
      "v_add_u32 v137, 131072, v129\n"
      "v_mfma_f32_16x16x32_bf16 a[144:147], a[76:79], v[156:159], a[144:147]\n"
      "v_add_u32 v138, 131072, v130\n"
      "v_add_u32 v139, 131072, v131\n"
      "v_mfma_f32_16x16x32_bf16 a[152:155], a[76:79], v[160:163], a[152:155]\n"
      "v_add_u32 v140, 131072, v132\n"
      "v_add_u32 v141, 131072, v133\n"
      "v_mfma_f32_16x16x32_bf16 a[160:163], a[76:79], v[164:167], a[160:163]\n"
      "s_waitcnt lgkmcnt(11)\n"
      "buffer_load_dwordx4 v240, %[rWo], s46 offen lds\n"
      "v_add_u32 v142, 131072, v134\n"
      "v_mfma_f32_16x16x32_bf16 a[96:99], a[176:179], a[192:195], a[96:99]\n"
      "s_waitcnt lgkmcnt(9)\n"
      "v_add_u32 v143, 131072, v135\n"
      "v_mfma_f32_16x16x32_bf16 a[140:143], a[80:83], v[168:171], a[140:143]\n"
      "s_add_u32 m0, s43, 61440\n"
      "v_mfma_f32_16x16x32_bf16 a[148:151], a[80:83], v[176:179], a[148:151]\n"
      "s_waitcnt lgkmcnt(0)\n"
      "s_barrier\n"
      "ds_read_b64_tr_b16 a[64:65], v136 offset:0\n"
      "ds_read_b64_tr_b16 a[66:67], v136 offset:4096\n"
      "v_mfma_f32_16x16x32_bf16 a[156:159], a[80:83], v[184:187], a[156:159]\n"
      "s_xor_b32 s46, s51, 114688\n"
      "ds_read_b64_tr_b16 a[68:69], v137 offset:0\n"
      "ds_read_b64_tr_b16 a[70:71], v137 offset:4096\n"
      "v_mfma_f32_16x16x32_bf16 a[144:147], a[84:87], v[168:171], a[144:147]\n"
      "buffer_load_dwordx4 v240, %[rWo], s46 offen lds\n"
      "ds_read_b64_tr_b16 a[72:73], v138 offset:0\n"
      "v_mfma_f32_16x16x32_bf16 a[152:155], a[84:87], v[176:179], a[152:155]\n"
      "ds_read_b64_tr_b16 a[74:75], v138 offset:4096\n"
      "ds_read_b64_tr_b16 a[76:77], v139 offset:0\n"
      "v_mfma_f32_16x16x32_bf16 a[160:163], a[84:87], v[184:187], a[160:163]\n"
      "ds_read_b64_tr_b16 a[78:79], v139 offset:4096\n"
      "ds_read_b64_tr_b16 a[80:81], v140 offset:0\n"
      "v_mfma_f32_16x16x32_bf16 a[140:143], a[88:91], v[192:195], a[140:143]\n"
      "ds_read_b64_tr_b16 a[82:83], v140 offset:4096\n"
      "v_mfma_f32_16x16x32_bf16 a[148:151], a[88:91], v[200:203], a[148:151]\n"
      "ds_read_b64_tr_b16 a[84:85], v141 offset:0\n"
      "ds_read_b64_tr_b16 a[86:87], v141 offset:4096\n"
      "v_mfma_f32_16x16x32_bf16 a[156:159], a[88:91], v[208:211], a[156:159]\n"
      "v_mfma_f32_16x16x32_bf16 a[144:147], a[92:95], v[192:195], a[144:147]\n"
      "v_mfma_f32_16x16x32_bf16 a[152:155], a[92:95], v[200:203], a[152:155]\n"
      "ds_read_b64_tr_b16 a[88:89], v142 offset:0\n"
      "ds_read_b64_tr_b16 a[90:91], v142 offset:4096\n"
      "v_mfma_f32_16x16x32_bf16 a[160:163], a[92:95], v[208:211], a[160:163]\n"
      "s_waitcnt vmcnt(7)\n"
      "v_cmp_ne_u32 vcc, v251, v243\n"
      "s_cbranch_vccz .Lmha_1\n"
      "s_mov_b32 s45, 0x40000\n"
      ".Lmha_2:\n"
      "buffer_load_dword v251, v242, %[rC], 0 offen offset:32 sc1\n"
      "s_waitcnt vmcnt(0)\n"
      "v_cmp_ne_u32 vcc, v251, v243\n"
      "s_cbranch_vccz .Lmha_1\n"
      "s_sub_u32 s45, s45, 1\n"
      "s_cbranch_scc0 .Lmha_2\n"
      ".Lmha_1:\n"
      "v_mfma_f32_16x16x32_bf16 a[96:99], a[180:183], a[196:199], a[96:99]\n"
      "s_waitcnt lgkmcnt(12)\n"
      "s_add_u32 s47, s36, 24576\n"
      "buffer_load_dwordx4 a[212:215], v241, %[rO], s47 offen sc1\n"
      "buffer_load_dwordx4 a[228:231], v241, %[rO], s47 offen offset:1024 sc1\n"
      "v_mfma_f32_16x16x32_bf16 v[0:3], a[64:67], v[172:175], 0\n"
      "s_add_u32 s47, s53, 0\n"
      "buffer_load_dwordx4 a[200:203], v248, %[rO], s47 offen sc1\n"
      "buffer_load_dwordx4 a[204:207], v248, %[rO], s47 offen offset:1024 sc1\n"
      "v_mfma_f32_16x16x32_bf16 v[32:35], a[64:67], v[188:191], 0\n"
      "ds_read_b64_tr_b16 a[92:93], v143 offset:0\n"
      "ds_read_b64_tr_b16 a[94:95], v143 offset:4096\n"
      "v_mfma_f32_16x16x32_bf16 v[64:67], a[64:67], v[204:207], 0\n"
      "v_accvgpr_read_b32 v228, a96\n"
      "v_accvgpr_read_b32 v229, a97\n"
      "v_mfma_f32_16x16x32_bf16 v[96:99], a[64:67], a[232:235], 0\n"
      "s_waitcnt lgkmcnt(12)\n"
      "v_accvgpr_read_b32 v230, a98\n"
      "v_accvgpr_read_b32 v231, a99\n"
      "v_mfma_f32_16x16x32_bf16 v[4:7], a[68:71], v[172:175], 0\n"
      "v_mul_f32 v228, v228, v227\n"
      "v_mul_f32 v229, v229, v227\n"
      "v_mfma_f32_16x16x32_bf16 v[36:39], a[68:71], v[188:191], 0\n"
      "v_mul_f32 v230, v230, v227\n"
      "v_mul_f32 v231, v231, v227\n"
      "v_mfma_f32_16x16x32_bf16 v[68:71], a[68:71], v[204:207], 0\n"
      "v_cvt_pk_bf16_f32 v228, v228, v229\n"
      "v_cvt_pk_bf16_f32 v229, v230, v231\n"
      "v_mfma_f32_16x16x32_bf16 v[100:103], a[68:71], a[232:235], 0\n"
      "s_waitcnt lgkmcnt(10)\n"
      "ds_write_b64 v247, v[228:229] offset:2048\n"
      "v_accvgpr_read_b32 v228, a140\n"
      "v_mfma_f32_16x16x32_bf16 v[8:11], a[72:75], v[172:175], 0\n"
      "buffer_load_dwordx4 a[208:211], v248, %[rO], s47 offen offset:2048 sc1\n"
      "v_accvgpr_read_b32 v229, a141\n"
      "v_mfma_f32_16x16x32_bf16 v[40:43], a[72:75], v[188:191], 0\n"
      "s_add_u32 s47, s53, 12288\n"
      "v_accvgpr_read_b32 v230, a142\n"
      "v_accvgpr_read_b32 v231, a143\n"
      "v_accvgpr_read_b32 v232, a144\n"
      "v_mfma_f32_16x16x32_bf16 v[72:75], a[72:75], v[204:207], 0\n"
      "s_waitcnt lgkmcnt(9)\n"
      "v_accvgpr_read_b32 v233, a145\n"
      "v_accvgpr_read_b32 v234, a146\n"
      "v_accvgpr_read_b32 v235, a147\n"
      "v_mfma_f32_16x16x32_bf16 v[12:15], a[76:79], v[172:175], 0\n"
      "ds_read_b64_tr_b16 a[64:65], v136 offset:8192\n"
      "ds_read_b64_tr_b16 a[66:67], v136 offset:12288\n"
      "v_mul_f32 v228, v228, v224\n"
      "v_mfma_f32_16x16x32_bf16 v[44:47], a[76:79], v[188:191], 0\n"
      "buffer_load_dwordx4 a[216:219], v248, %[rO], s47 offen sc1\n"
      "v_mul_f32 v229, v229, v224\n"
      "v_mul_f32 v230, v230, v224\n"
      "v_mfma_f32_16x16x32_bf16 v[76:79], a[76:79], v[204:207], 0\n"
      "s_waitcnt lgkmcnt(9)\n"
      "v_mul_f32 v231, v231, v224\n"
      "v_mul_f32 v232, v232, v224\n"
      "v_mul_f32 v233, v233, v224\n"
      "v_mfma_f32_16x16x32_bf16 v[16:19], a[80:83], v[172:175], 0\n"
      "v_mul_f32 v234, v234, v224\n"
      "v_mul_f32 v235, v235, v224\n"
      "v_cvt_pk_bf16_f32 v104, v228, v229\n"
      "v_mfma_f32_16x16x32_bf16 v[48:51], a[80:83], v[188:191], 0\n"
      "ds_read_b64_tr_b16 a[68:69], v137 offset:8192\n"
      "ds_read_b64_tr_b16 a[70:71], v137 offset:12288\n"
      "v_cvt_pk_bf16_f32 v105, v230, v231\n"
      "v_mfma_f32_16x16x32_bf16 v[80:83], a[80:83], v[204:207], 0\n"
      "s_waitcnt lgkmcnt(9)\n"
      "buffer_load_dwordx4 a[220:223], v248, %[rO], s47 offen offset:1024 sc1\n"
      "v_cvt_pk_bf16_f32 v106, v232, v233\n"
      "v_cvt_pk_bf16_f32 v107, v234, v235\n"
      "v_mfma_f32_16x16x32_bf16 v[20:23], a[84:87], v[172:175], 0\n"
      "s_add_u32 s44, s52, 38912\n"
      "buffer_store_dwordx4 v[104:107], v248, %[rO], s44 offen sc1\n"
      "v_accvgpr_read_b32 v228, a148\n"
      "v_accvgpr_read_b32 v229, a149\n"
      "v_mfma_f32_16x16x32_bf16 v[52:55], a[84:87], v[188:191], 0\n"
      "v_accvgpr_read_b32 v230, a150\n"
      "v_accvgpr_read_b32 v231, a151\n"
      "v_accvgpr_read_b32 v232, a152\n"
      "v_mfma_f32_16x16x32_bf16 v[84:87], a[84:87], v[204:207], 0\n"
      "s_waitcnt lgkmcnt(7)\n"
      "ds_read_b64_tr_b16 a[72:73], v138 offset:8192\n"
      "ds_read_b64_tr_b16 a[74:75], v138 offset:12288\n"
      "v_accvgpr_read_b32 v233, a153\n"
      "v_mfma_f32_16x16x32_bf16 v[24:27], a[88:91], v[172:175], 0\n"
      "s_waitcnt lgkmcnt(6)\n"
      "s_barrier\n"
      "v_accvgpr_read_b32 v234, a154\n"
      "v_accvgpr_read_b32 v235, a155\n"
      "v_mul_f32 v228, v228, v225\n"
      "v_mfma_f32_16x16x32_bf16 v[56:59], a[88:91], v[188:191], 0\n"
      "buffer_load_dwordx4 a[224:227], v248, %[rO], s47 offen offset:2048 sc1\n"
      "v_mul_f32 v229, v229, v225\n"
      "v_mul_f32 v230, v230, v225\n"
      "v_mfma_f32_16x16x32_bf16 v[88:91], a[88:91], v[204:207], 0\n"
      "s_add_u32 s47, s54, 0\n"
      "ds_read_b128 a[60:63], v245 offset:2048\n"
      "ds_read_b128 a[52:55], v250 offset:26624\n"
      "ds_read_b128 a[56:59], v250 offset:27648\n"
      "v_mfma_f32_16x16x32_bf16 v[28:31], a[92:95], v[172:175], 0\n"
      "ds_read_b64_tr_b16 a[76:77], v139 offset:8192\n"
      "ds_read_b64_tr_b16 a[78:79], v139 offset:12288\n"
      "v_mul_f32 v231, v231, v225\n"
      "v_mfma_f32_16x16x32_bf16 v[60:63], a[92:95], v[188:191], 0\n"
      "v_mul_f32 v232, v232, v225\n"
      "v_mul_f32 v233, v233, v225\n"
      "v_mul_f32 v234, v234, v225\n"
      "v_mfma_f32_16x16x32_bf16 v[92:95], a[92:95], v[204:207], 0\n"
      "s_waitcnt lgkmcnt(9)\n"
      "buffer_load_dwordx4 a[4:7], v248, %[rO], s47 offen sc1\n"
      "ds_read_b64_tr_b16 a[80:81], v140 offset:8192\n"
      "v_mul_f32 v235, v235, v225\n"
      "v_mfma_f32_16x16x32_bf16 v[0:3], a[64:67], v[180:183], v[0:3]\n"
      "ds_read_b64_tr_b16 a[82:83], v140 offset:12288\n"
      "v_cvt_pk_bf16_f32 v108, v228, v229\n"
      "v_cvt_pk_bf16_f32 v109, v230, v231\n"
      "v_mfma_f32_16x16x32_bf16 v[32:35], a[64:67], v[196:199], v[32:35]\n"
      "ds_read_b64_tr_b16 a[84:85], v141 offset:8192\n"
      "v_cvt_pk_bf16_f32 v110, v232, v233\n"
      "v_cvt_pk_bf16_f32 v111, v234, v235\n"
      "v_mfma_f32_16x16x32_bf16 v[64:67], a[64:67], v[212:215], v[64:67]\n"
      "buffer_store_dwordx4 v[108:111], v248, %[rO], s44 offen offset:1024 sc1\n"
      "ds_read_b64_tr_b16 a[86:87], v141 offset:12288\n"
      "v_accvgpr_read_b32 v228, a156\n"
      "v_mfma_f32_16x16x32_bf16 v[96:99], a[64:67], a[236:239], v[96:99]\n"
      "s_waitcnt lgkmcnt(11)\n"
      "buffer_load_dwordx4 a[8:11], v248, %[rO], s47 offen offset:1024 sc1\n"
      "v_accvgpr_read_b32 v229, a157\n"
      "v_accvgpr_read_b32 v230, a158\n"
      "v_mfma_f32_16x16x32_bf16 v[4:7], a[68:71], v[180:183], v[4:7]\n"
      "s_add_u32 s59, s37, 51200\n"
      "v_accvgpr_read_b32 v231, a159\n"
      "v_accvgpr_read_b32 v232, a160\n"
      "v_accvgpr_read_b32 v233, a161\n"
      "v_mfma_f32_16x16x32_bf16 v[36:39], a[68:71], v[196:199], v[36:39]\n"
      "ds_read_b64_tr_b16 a[88:89], v142 offset:8192\n"
      "ds_read_b64_tr_b16 a[90:91], v142 offset:12288\n"
      "v_accvgpr_read_b32 v234, a162\n"
      "v_mfma_f32_16x16x32_bf16 v[68:71], a[68:71], v[212:215], v[68:71]\n"
      "v_accvgpr_read_b32 v235, a163\n"
      "v_mul_f32 v228, v228, v226\n"
      "v_mfma_f32_16x16x32_bf16 v[100:103], a[68:71], a[236:239], v[100:103]\n"
      "s_waitcnt lgkmcnt(11)\n"
      "buffer_load_dwordx4 a[12:15], v248, %[rO], s47 offen offset:2048 sc1\n"
      "v_mul_f32 v229, v229, v226\n"
      "v_mul_f32 v230, v230, v226\n"
      "v_mfma_f32_16x16x32_bf16 v[8:11], a[72:75], v[180:183], v[8:11]\n"
      "s_add_u32 s47, s54, 12288\n"
      "s_waitcnt lgkmcnt(10)\n"
      "buffer_store_dwordx4 a[60:63], v241, %[rO], s59 offen sc1\n"
      "ds_read_b64_tr_b16 a[92:93], v143 offset:8192\n"
      "ds_read_b64_tr_b16 a[94:95], v143 offset:12288\n"
      "v_mfma_f32_16x16x32_bf16 v[40:43], a[72:75], v[196:199], v[40:43]\n"
      "v_mul_f32 v231, v231, v226\n"
      "v_mul_f32 v232, v232, v226\n"
      "v_mul_f32 v233, v233, v226\n"
      "v_mfma_f32_16x16x32_bf16 v[72:75], a[72:75], v[212:215], v[72:75]\n"
      "s_waitcnt lgkmcnt(8)\n"
      "ds_read_b64_tr_b16 a[64:65], v138 offset:16384\n"
      "v_mul_f32 v234, v234, v226\n"
      "v_mul_f32 v235, v235, v226\n"
      "v_mfma_f32_16x16x32_bf16 v[12:15], a[76:79], v[180:183], v[12:15]\n"
      "ds_read_b64_tr_b16 a[66:67], v138 offset:20480\n"
      "buffer_load_dwordx4 a[20:23], v248, %[rO], s47 offen sc1\n"
      "v_cvt_pk_bf16_f32 v112, v228, v229\n"
      "v_mfma_f32_16x16x32_bf16 v[44:47], a[76:79], v[196:199], v[44:47]\n"
      "v_cvt_pk_bf16_f32 v113, v230, v231\n"
      "v_cvt_pk_bf16_f32 v114, v232, v233\n"
      "v_mfma_f32_16x16x32_bf16 v[76:79], a[76:79], v[212:215], v[76:79]\n"
      "s_waitcnt lgkmcnt(8)\n"
      "ds_read_b64_tr_b16 a[68:69], v139 offset:16384\n"
      "ds_read_b64_tr_b16 a[70:71], v139 offset:20480\n"
      "v_cvt_pk_bf16_f32 v115, v234, v235\n"
      "v_mfma_f32_16x16x32_bf16 v[16:19], a[80:83], v[180:183], v[16:19]\n"
      "buffer_store_dwordx4 v[112:115], v248, %[rO], s44 offen offset:2048 sc1\n"
      "buffer_load_dwordx4 a[24:27], v248, %[rO], s47 offen offset:1024 sc1\n"
      "v_mfma_f32_16x16x32_bf16 v[48:51], a[80:83], v[196:199], v[48:51]\n"
      "ds_read_b64_tr_b16 a[72:73], v140 offset:16384\n"
      "ds_read_b64_tr_b16 a[74:75], v140 offset:20480\n"
      "v_mfma_f32_16x16x32_bf16 v[80:83], a[80:83], v[212:215], v[80:83]\n"
      "s_waitcnt lgkmcnt(10)\n"
      "buffer_load_dwordx4 a[28:31], v248, %[rO], s47 offen offset:2048 sc1\n"
      "ds_read_b64_tr_b16 a[76:77], v141 offset:16384\n"
      "v_mfma_f32_16x16x32_bf16 v[20:23], a[84:87], v[180:183], v[20:23]\n"
      "ds_read_b64_tr_b16 a[78:79], v141 offset:20480\n"
      "ds_read_b64_tr_b16 a[80:81], v142 offset:16384\n"
      "v_mfma_f32_16x16x32_bf16 v[52:55], a[84:87], v[196:199], v[52:55]\n"
      "ds_read_b64_tr_b16 a[82:83], v142 offset:20480\n"
      "v_mfma_f32_16x16x32_bf16 v[84:87], a[84:87], v[212:215], v[84:87]\n"
      "s_waitcnt lgkmcnt(12)\n"
      "ds_read_b64_tr_b16 a[84:85], v143 offset:16384\n"
      "ds_read_b64_tr_b16 a[86:87], v143 offset:20480\n"
      "v_mfma_f32_16x16x32_bf16 v[24:27], a[88:91], v[180:183], v[24:27]\n"
      "v_mfma_f32_16x16x32_bf16 v[56:59], a[88:91], v[196:199], v[56:59]\n"
      "v_mfma_f32_16x16x32_bf16 v[88:91], a[88:91], v[212:215], v[88:91]\n"
      "s_waitcnt lgkmcnt(12)\n"
      "ds_read_b64_tr_b16 a[88:89], v138 offset:24576\n"
      "ds_read_b64_tr_b16 a[90:91], v138 offset:28672\n"
      "v_mfma_f32_16x16x32_bf16 v[28:31], a[92:95], v[180:183], v[28:31]\n"
      "v_mfma_f32_16x16x32_bf16 v[60:63], a[92:95], v[196:199], v[60:63]\n"
      "v_lshlrev_b32 v116, 1, s43\n"
      "v_add_u32 v116, v116, v250\n"
      "v_mfma_f32_16x16x32_bf16 v[92:95], a[92:95], v[212:215], v[92:95]\n"
      "s_waitcnt lgkmcnt(12)\n"
      "s_waitcnt vmcnt(17)\n"
      "ds_write_b128 v116, a[212:215] offset:0\n"
      "s_waitcnt vmcnt(16)\n"
      "ds_write_b128 v116, a[228:231] offset:1024\n"
      "v_mfma_f32_16x16x32_bf16 v[8:11], a[64:67], v[216:219], v[8:11]\n"
      "v_mfma_f32_16x16x32_bf16 v[40:43], a[64:67], v[220:223], v[40:43]\n"
      "v_mfma_f32_16x16x32_bf16 v[72:75], a[64:67], v[236:239], v[72:75]\n"
      "s_waitcnt lgkmcnt(12)\n"
      "ds_read_b64_tr_b16 a[92:93], v139 offset:24576\n"
      "ds_read_b64_tr_b16 a[94:95], v139 offset:28672\n"
      "v_mfma_f32_16x16x32_bf16 v[12:15], a[68:71], v[216:219], v[12:15]\n"
      "v_mfma_f32_16x16x32_bf16 v[44:47], a[68:71], v[220:223], v[44:47]\n"
      "v_mfma_f32_16x16x32_bf16 v[76:79], a[68:71], v[236:239], v[76:79]\n"
      "s_waitcnt lgkmcnt(12)\n"
      "ds_read_b64_tr_b16 a[64:65], v140 offset:24576\n"
      "ds_read_b64_tr_b16 a[66:67], v140 offset:28672\n"
      "v_mfma_f32_16x16x32_bf16 v[16:19], a[72:75], v[216:219], v[16:19]\n"
      "v_mfma_f32_16x16x32_bf16 v[48:51], a[72:75], v[220:223], v[48:51]\n"
      "v_mfma_f32_16x16x32_bf16 v[80:83], a[72:75], v[236:239], v[80:83]\n"
      "s_waitcnt lgkmcnt(12)\n"
      "ds_read_b64_tr_b16 a[68:69], v141 offset:24576\n"
      "ds_read_b64_tr_b16 a[70:71], v141 offset:28672\n"
      "v_mfma_f32_16x16x32_bf16 v[20:23], a[76:79], v[216:219], v[20:23]\n"
      "v_mfma_f32_16x16x32_bf16 v[52:55], a[76:79], v[220:223], v[52:55]\n"
      "v_mfma_f32_16x16x32_bf16 v[84:87], a[76:79], v[236:239], v[84:87]\n"
      "s_waitcnt lgkmcnt(12)\n"
      "ds_read_b64_tr_b16 a[72:73], v142 offset:24576\n"
      "ds_read_b64_tr_b16 a[74:75], v142 offset:28672\n"
      "v_mfma_f32_16x16x32_bf16 v[24:27], a[80:83], v[216:219], v[24:27]\n"
      "s_waitcnt lgkmcnt(8)\n"
      "s_barrier\n"
      "ds_read_b128 a[212:215], v250 offset:0\n"
      "ds_read_b64_tr_b16 a[76:77], v143 offset:24576\n"
      "v_mfma_f32_16x16x32_bf16 v[56:59], a[80:83], v[220:223], v[56:59]\n"
      "ds_read_b64_tr_b16 a[78:79], v143 offset:28672\n"
      "v_mfma_f32_16x16x32_bf16 v[88:91], a[80:83], v[236:239], v[88:91]\n"
      "ds_read_b64_tr_b16 a[80:81], v136 offset:16384\n"
      "ds_read_b64_tr_b16 a[82:83], v136 offset:20480\n"
      "v_mfma_f32_16x16x32_bf16 v[28:31], a[84:87], v[216:219], v[28:31]\n"
      "ds_read_b128 a[228:231], v250 offset:1024\n"
      "v_mfma_f32_16x16x32_bf16 v[60:63], a[84:87], v[220:223], v[60:63]\n"
      "v_mfma_f32_16x16x32_bf16 v[92:95], a[84:87], v[236:239], v[92:95]\n"
      "v_mfma_f32_16x16x32_bf16 v[8:11], a[88:91], v[104:107], v[8:11]\n"
      "v_mfma_f32_16x16x32_bf16 v[40:43], a[88:91], v[108:111], v[40:43]\n"
      "v_mfma_f32_16x16x32_bf16 v[72:75], a[88:91], v[112:115], v[72:75]\n"
      "s_waitcnt lgkmcnt(12)\n"
      "ds_read_b64_tr_b16 a[84:85], v137 offset:16384\n"
      "ds_read_b64_tr_b16 a[86:87], v137 offset:20480\n"
      "v_mfma_f32_16x16x32_bf16 v[12:15], a[92:95], v[104:107], v[12:15]\n"
      "v_mfma_f32_16x16x32_bf16 v[44:47], a[92:95], v[108:111], v[44:47]\n"
      "v_mfma_f32_16x16x32_bf16 v[76:79], a[92:95], v[112:115], v[76:79]\n"
      "s_waitcnt lgkmcnt(12)\n"
      "ds_read_b128 a[16:19], v250 offset:2048\n"
      "ds_read_b64_tr_b16 a[88:89], v136 offset:24576\n"
      "v_mfma_f32_16x16x32_bf16 v[16:19], a[64:67], v[104:107], v[16:19]\n"
      "v_mfma_f32_16x16x32_bf16 v[48:51], a[64:67], v[108:111], v[48:51]\n"
      "v_mfma_f32_16x16x32_bf16 v[80:83], a[64:67], v[112:115], v[80:83]\n"
      "s_waitcnt lgkmcnt(12)\n"
      "ds_read_b64_tr_b16 a[90:91], v136 offset:28672\n"
      "ds_read_b128 a[32:35], v250 offset:3072\n"
      "v_mfma_f32_16x16x32_bf16 v[20:23], a[68:71], v[104:107], v[20:23]\n"
      "v_mfma_f32_16x16x32_bf16 v[52:55], a[68:71], v[108:111], v[52:55]\n"
      "v_mfma_f32_16x16x32_bf16 v[84:87], a[68:71], v[112:115], v[84:87]\n"
      "s_waitcnt lgkmcnt(12)\n"
      "ds_read_b64_tr_b16 a[92:93], v137 offset:24576\n"
      "ds_read_b64_tr_b16 a[94:95], v137 offset:28672\n"
      "v_mfma_f32_16x16x32_bf16 v[24:27], a[72:75], v[104:107], v[24:27]\n"
      "v_add_u32 v136, 4294901760, v136\n"
      "v_add_u32 v137, 4294901760, v137\n"
      "v_mfma_f32_16x16x32_bf16 v[56:59], a[72:75], v[108:111], v[56:59]\n"
      "v_add_u32 v138, 4294901760, v138\n"
      "v_add_u32 v139, 4294901760, v139\n"
      "v_mfma_f32_16x16x32_bf16 v[88:91], a[72:75], v[112:115], v[88:91]\n"
      "s_waitcnt lgkmcnt(11)\n"
      "v_add_u32 v140, 4294901760, v140\n"
      "v_add_u32 v141, 4294901760, v141\n"
      "v_mfma_f32_16x16x32_bf16 v[28:31], a[76:79], v[104:107], v[28:31]\n"
      "v_add_u32 v142, 4294901760, v142\n"
      "v_add_u32 v143, 4294901760, v143\n"
      "v_mfma_f32_16x16x32_bf16 v[60:63], a[76:79], v[108:111], v[60:63]\n"
      "ds_read_b128 a[48:51], v250 offset:4096\n"
      "ds_read_b64_tr_b16 a[64:65], v138 offset:0\n"
      "v_mfma_f32_16x16x32_bf16 v[92:95], a[76:79], v[112:115], v[92:95]\n"
      "s_waitcnt lgkmcnt(11)\n"
      "ds_read_b64_tr_b16 a[66:67], v138 offset:4096\n"
      "ds_read_b128 a[108:111], v250 offset:5120\n"
      "v_mfma_f32_16x16x32_bf16 v[0:3], a[80:83], v[216:219], v[0:3]\n"
      "ds_read_b64_tr_b16 a[68:69], v139 offset:0\n"
      "v_accvgpr_read_b32 v252, a247\n"
      "v_mfma_f32_16x16x32_bf16 v[32:35], a[80:83], v[220:223], v[32:35]\n"
      "v_accvgpr_read_b32 v253, a248\n"
      "v_accvgpr_read_b32 v254, a250\n"
      "v_mfma_f32_16x16x32_bf16 v[64:67], a[80:83], v[236:239], v[64:67]\n"
      "s_waitcnt vmcnt(0)\n"
      "s_barrier\n"
      "s_cmp_lg_u32 %[w], 0\n"
      "s_cbranch_scc1 .Lmha_3\n"
      "s_mov_b64 exec, s[56:57]\n"
      "buffer_store_dword v243, v242, %[rC], 0 offen offset:64 sc1\n"
      "s_mov_b64 exec, -1\n"
      ".Lmha_3:\n"
      "v_accvgpr_read_b32 v255, a252\n"
      "v_mfma_f32_16x16x32_bf16 v[96:99], a[80:83], a[52:55], v[96:99]\n"
      "s_waitcnt lgkmcnt(11)\n"
      "s_add_u32 s47, s55, 0\n"
      "buffer_load_dwordx4 a[36:39], v248, %[rO], s47 offen sc1\n"
      "ds_read_b64_tr_b16 a[70:71], v139 offset:4096\n"
      "v_mfma_f32_16x16x32_bf16 v[4:7], a[84:87], v[216:219], v[4:7]\n"
      "s_add_u32 m0, s43, 0\n"
      "ds_read_b64_tr_b16 a[72:73], v140 offset:0\n"
      "ds_read_b64_tr_b16 a[74:75], v140 offset:4096\n"
      "v_mfma_f32_16x16x32_bf16 v[36:39], a[84:87], v[220:223], v[36:39]\n"
      "s_xor_b32 s46, s51, 0\n"
      "buffer_load_dwordx4 v240, %[rWo], s46 offen lds\n"
      "buffer_load_dwordx4 a[40:43], v248, %[rO], s47 offen offset:1024 sc1\n"
      "v_mfma_f32_16x16x32_bf16 v[68:71], a[84:87], v[236:239], v[68:71]\n"
      "s_add_u32 m0, s43, 4096\n"
      "buffer_load_dwordx4 a[44:47], v248, %[rO], s47 offen offset:2048 sc1\n"
      "v_add_u32 v255, 1, v255\n"
      "v_mfma_f32_16x16x32_bf16 v[100:103], a[84:87], a[52:55], v[100:103]\n"
      "s_waitcnt lgkmcnt(11)\n"
      "ds_read_b64_tr_b16 a[76:77], v141 offset:0\n"
      "ds_read_b64_tr_b16 a[78:79], v141 offset:4096\n"
      "v_mfma_f32_16x16x32_bf16 v[0:3], a[88:91], v[104:107], v[0:3]\n"
      "s_xor_b32 s46, s51, 16384\n"
      "ds_read_b64_tr_b16 a[80:81], v142 offset:0\n"
      "buffer_load_dwordx4 v240, %[rWo], s46 offen lds\n"
      "v_mfma_f32_16x16x32_bf16 v[32:35], a[88:91], v[108:111], v[32:35]\n"
      "v_mfma_f32_16x16x32_bf16 v[64:67], a[88:91], v[112:115], v[64:67]\n"
      "v_mfma_f32_16x16x32_bf16 v[96:99], a[88:91], a[56:59], v[96:99]\n"
      "s_waitcnt lgkmcnt(11)\n"
      "ds_read_b64_tr_b16 a[82:83], v142 offset:4096\n"
      "ds_read_b64_tr_b16 a[84:85], v143 offset:0\n"
      "v_mfma_f32_16x16x32_bf16 v[4:7], a[92:95], v[104:107], v[4:7]\n"
      "s_add_u32 s47, s55, 12288\n"
      "ds_read_b64_tr_b16 a[86:87], v143 offset:4096\n"
      "buffer_load_dwordx4 a[96:99], v248, %[rO], s47 offen sc1\n"
      "v_mfma_f32_16x16x32_bf16 v[36:39], a[92:95], v[108:111], v[36:39]\n"
      "s_add_u32 m0, s43, 8192\n"
      "v_mfma_f32_16x16x32_bf16 v[68:71], a[92:95], v[112:115], v[68:71]\n"
      "s_xor_b32 s46, s51, 32768\n"
      "buffer_load_dwordx4 v240, %[rWo], s46 offen lds\n"
      "v_mfma_f32_16x16x32_bf16 v[100:103], a[92:95], a[56:59], v[100:103]\n"
      "s_waitcnt lgkmcnt(11)\n"
      "ds_read_b64_tr_b16 a[88:89], v136 offset:0\n"
      "ds_read_b64_tr_b16 a[90:91], v136 offset:4096\n"
      "v_mfma_f32_16x16x32_bf16 v[8:11], a[64:67], a[200:203], v[8:11]\n"
      "ds_read_b64_tr_b16 a[92:93], v137 offset:0\n"
      "buffer_load_dwordx4 a[100:103], v248, %[rO], s47 offen offset:1024 sc1\n"
      "v_mfma_f32_16x16x32_bf16 v[40:43], a[64:67], a[204:207], v[40:43]\n"
      "buffer_load_dword v251, v242, %[rC], 0 offen offset:64 sc1\n"
      "v_mfma_f32_16x16x32_bf16 v[72:75], a[64:67], a[208:211], v[72:75]\n"
      "s_waitcnt lgkmcnt(11)\n"
      "ds_read_b64_tr_b16 a[94:95], v137 offset:4096\n"
      "v_mfma_f32_16x16x32_bf16 v[12:15], a[68:71], a[200:203], v[12:15]\n"
      "ds_read_b64_tr_b16 a[64:65], v136 offset:8192\n"
      "ds_read_b64_tr_b16 a[66:67], v136 offset:12288\n"
      "v_mfma_f32_16x16x32_bf16 v[44:47], a[68:71], a[204:207], v[44:47]\n"
      "v_mfma_f32_16x16x32_bf16 v[76:79], a[68:71], a[208:211], v[76:79]\n"
      "s_waitcnt lgkmcnt(12)\n"
      "buffer_load_dwordx4 a[104:107], v248, %[rO], s47 offen offset:2048 sc1\n"
      "v_mfma_f32_16x16x32_bf16 v[16:19], a[72:75], a[200:203], v[16:19]\n"
      "s_add_u32 m0, s43, 12288\n"
      "v_mfma_f32_16x16x32_bf16 v[48:51], a[72:75], a[204:207], v[48:51]\n"
      "s_xor_b32 s46, s51, 49152\n"
      "buffer_load_dwordx4 v240, %[rWo], s46 offen lds\n"
      "ds_read_b64_tr_b16 a[68:69], v137 offset:8192\n"
      "v_mfma_f32_16x16x32_bf16 v[80:83], a[72:75], a[208:211], v[80:83]\n"
      "s_waitcnt lgkmcnt(11)\n"
      "ds_read_b64_tr_b16 a[70:71], v137 offset:12288\n"
      "v_mfma_f32_16x16x32_bf16 v[20:23], a[76:79], a[200:203], v[20:23]\n"
      "v_mfma_f32_16x16x32_bf16 v[52:55], a[76:79], a[204:207], v[52:55]\n"
      "v_mfma_f32_16x16x32_bf16 v[84:87], a[76:79], a[208:211], v[84:87]\n"
      "s_waitcnt lgkmcnt(10)\n"
      "ds_read_b64_tr_b16 a[72:73], v138 offset:8192\n"
      "ds_read_b64_tr_b16 a[74:75], v138 offset:12288\n"
      "v_mfma_f32_16x16x32_bf16 v[24:27], a[80:83], a[200:203], v[24:27]\n"
      "s_add_u32 m0, s43, 16384\n"
      "v_mfma_f32_16x16x32_bf16 v[56:59], a[80:83], a[204:207], v[56:59]\n"
      "s_xor_b32 s46, s49, 65536\n"
      "buffer_load_dwordx4 v240, %[rWo], s46 offen lds\n"
      "v_mfma_f32_16x16x32_bf16 v[88:91], a[80:83], a[208:211], v[88:91]\n"
      "s_waitcnt lgkmcnt(10)\n"
      "ds_read_b64_tr_b16 a[76:77], v139 offset:8192\n"
      "ds_read_b64_tr_b16 a[78:79], v139 offset:12288\n"
      "v_mfma_f32_16x16x32_bf16 v[28:31], a[84:87], a[200:203], v[28:31]\n"
      "v_mfma_f32_16x16x32_bf16 v[60:63], a[84:87], a[204:207], v[60:63]\n"
      "v_mfma_f32_16x16x32_bf16 v[92:95], a[84:87], a[208:211], v[92:95]\n"
      "s_waitcnt lgkmcnt(10)\n"
      "ds_read_b64_tr_b16 a[80:81], v140 offset:8192\n"
      "ds_read_b64_tr_b16 a[82:83], v140 offset:12288\n"
      "v_mfma_f32_16x16x32_bf16 v[0:3], a[88:91], a[200:203], v[0:3]\n"
      "v_mfma_f32_16x16x32_bf16 v[32:35], a[88:91], a[204:207], v[32:35]\n"
      "v_mfma_f32_16x16x32_bf16 v[64:67], a[88:91], a[208:211], v[64:67]\n"
      "s_add_u32 m0, s43, 20480\n"
      "ds_read_b64_tr_b16 a[84:85], v141 offset:8192\n"
      "ds_read_b64_tr_b16 a[86:87], v141 offset:12288\n"
      "v_mfma_f32_16x16x32_bf16 v[96:99], a[88:91], a[212:215], v[96:99]\n"
      "s_waitcnt lgkmcnt(12)\n"
      "v_mfma_f32_16x16x32_bf16 v[4:7], a[92:95], a[200:203], v[4:7]\n"
      "s_xor_b32 s46, s49, 81920\n"
      "buffer_load_dwordx4 v240, %[rWo], s46 offen lds\n"
      "v_mfma_f32_16x16x32_bf16 v[36:39], a[92:95], a[204:207], v[36:39]\n"
      "ds_read_b64_tr_b16 a[88:89], v142 offset:8192\n"
      "ds_read_b64_tr_b16 a[90:91], v142 offset:12288\n"
      "v_mfma_f32_16x16x32_bf16 v[68:71], a[92:95], a[208:211], v[68:71]\n"
      "v_mfma_f32_16x16x32_bf16 v[100:103], a[92:95], a[212:215], v[100:103]\n"
      "s_waitcnt lgkmcnt(12)\n"
      "v_mfma_f32_16x16x32_bf16 v[0:3], a[64:67], a[216:219], v[0:3]\n"
      "ds_read_b64_tr_b16 a[92:93], v143 offset:8192\n"
      "ds_read_b64_tr_b16 a[94:95], v143 offset:12288\n"
      "v_mfma_f32_16x16x32_bf16 v[32:35], a[64:67], a[220:223], v[32:35]\n"
      "s_add_u32 m0, s43, 24576\n"
      "v_mfma_f32_16x16x32_bf16 v[64:67], a[64:67], a[224:227], v[64:67]\n"
      "s_xor_b32 s46, s49, 98304\n"
      "buffer_load_dwordx4 v240, %[rWo], s46 offen lds\n"
      "v_mfma_f32_16x16x32_bf16 v[96:99], a[64:67], a[228:231], v[96:99]\n"
      "s_waitcnt lgkmcnt(12)\n"
      "ds_read_b64_tr_b16 a[64:65], v138 offset:16384\n"
      "ds_read_b64_tr_b16 a[66:67], v138 offset:20480\n"
      "v_mfma_f32_16x16x32_bf16 v[4:7], a[68:71], a[216:219], v[4:7]\n"
      "v_mfma_f32_16x16x32_bf16 v[36:39], a[68:71], a[220:223], v[36:39]\n"
      "v_mfma_f32_16x16x32_bf16 v[68:71], a[68:71], a[224:227], v[68:71]\n"
      "v_mfma_f32_16x16x32_bf16 v[100:103], a[68:71], a[228:231], v[100:103]\n"
      "s_waitcnt lgkmcnt(12)\n"
      "ds_read_b64_tr_b16 a[68:69], v139 offset:16384\n"
      "ds_read_b64_tr_b16 a[70:71], v139 offset:20480\n"
      "v_mfma_f32_16x16x32_bf16 v[8:11], a[72:75], a[216:219], v[8:11]\n"
      "v_mfma_f32_16x16x32_bf16 v[40:43], a[72:75], a[220:223], v[40:43]\n"
      "s_add_u32 m0, s43, 28672\n"
      "v_mfma_f32_16x16x32_bf16 v[72:75], a[72:75], a[224:227], v[72:75]\n"
      "s_waitcnt lgkmcnt(12)\n"
      "ds_read_b64_tr_b16 a[72:73], v140 offset:16384\n"
      "ds_read_b64_tr_b16 a[74:75], v140 offset:20480\n"
      "v_mfma_f32_16x16x32_bf16 v[12:15], a[76:79], a[216:219], v[12:15]\n"
      "s_xor_b32 s46, s49, 114688\n"
      "buffer_load_dwordx4 v240, %[rWo], s46 offen lds\n"
      "v_mfma_f32_16x16x32_bf16 v[44:47], a[76:79], a[220:223], v[44:47]\n"
      "v_mfma_f32_16x16x32_bf16 v[76:79], a[76:79], a[224:227], v[76:79]\n"
      "s_waitcnt lgkmcnt(12)\n"
      "ds_read_b64_tr_b16 a[76:77], v141 offset:16384\n"
      "ds_read_b64_tr_b16 a[78:79], v141 offset:20480\n"
      "v_mfma_f32_16x16x32_bf16 v[16:19], a[80:83], a[216:219], v[16:19]\n"
      "v_mfma_f32_16x16x32_bf16 v[48:51], a[80:83], a[220:223], v[48:51]\n"
      "v_mfma_f32_16x16x32_bf16 v[80:83], a[80:83], a[224:227], v[80:83]\n"
      "s_waitcnt lgkmcnt(12)\n"
      "ds_read_b64_tr_b16 a[80:81], v142 offset:16384\n"
      "ds_read_b64_tr_b16 a[82:83], v142 offset:20480\n"
      "v_mfma_f32_16x16x32_bf16 v[20:23], a[84:87], a[216:219], v[20:23]\n"
      "v_mfma_f32_16x16x32_bf16 v[52:55], a[84:87], a[220:223], v[52:55]\n"
      "v_mfma_f32_16x16x32_bf16 v[84:87], a[84:87], a[224:227], v[84:87]\n"
      "s_waitcnt lgkmcnt(12)\n"
      "ds_read_b64_tr_b16 a[84:85], v143 offset:16384\n"
      "ds_read_b64_tr_b16 a[86:87], v143 offset:20480\n"
      "v_mfma_f32_16x16x32_bf16 v[24:27], a[88:91], a[216:219], v[24:27]\n"
      "s_waitcnt vmcnt(6)\n"
      "v_cmp_ne_u32 vcc, v251, v243\n"
      "s_cbranch_vccz .Lmha_4\n"
      "s_mov_b32 s45, 0x40000\n"
      ".Lmha_5:\n"
      "buffer_load_dword v251, v242, %[rC], 0 offen offset:64 sc1\n"
      "s_waitcnt vmcnt(0)\n"
      "v_cmp_ne_u32 vcc, v251, v243\n"
      "s_cbranch_vccz .Lmha_4\n"
      "s_sub_u32 s45, s45, 1\n"
      "s_cbranch_scc0 .Lmha_5\n"
      ".Lmha_4:\n"
      "v_mfma_f32_16x16x32_bf16 v[56:59], a[88:91], a[220:223], v[56:59]\n"
      "s_add_u32 s47, s36, 51200\n"
      "buffer_load_dwordx4 v[156:159], v241, %[rO], s47 offen sc1\n"
      "buffer_load_dwordx4 v[172:175], v241, %[rO], s47 offen offset:1024 sc1\n"
      "v_mfma_f32_16x16x32_bf16 v[88:91], a[88:91], a[224:227], v[88:91]\n"
      "s_waitcnt lgkmcnt(12)\n"
      "s_add_u32 s47, s53, 26624\n"
      "buffer_load_dwordx4 v[144:147], v248, %[rO], s47 offen sc1\n"
      "v_mfma_f32_16x16x32_bf16 v[28:31], a[92:95], a[216:219], v[28:31]\n"
      "buffer_load_dwordx4 v[148:151], v248, %[rO], s47 offen offset:1024 sc1\n"
      "ds_read_b64_tr_b16 a[88:89], v136 offset:16384\n"
      "v_mfma_f32_16x16x32_bf16 v[60:63], a[92:95], a[220:223], v[60:63]\n"
      "ds_read_b64_tr_b16 a[90:91], v136 offset:20480\n"
      "v_mfma_f32_16x16x32_bf16 v[92:95], a[92:95], a[224:227], v[92:95]\n"
      "s_waitcnt lgkmcnt(12)\n"
      "buffer_load_dwordx4 v[152:155], v248, %[rO], s47 offen offset:2048 sc1\n"
      "ds_read_b64_tr_b16 a[92:93], v137 offset:16384\n"
      "v_mfma_f32_16x16x32_bf16 v[8:11], a[64:67], a[4:7], v[8:11]\n"
      "ds_read_b64_tr_b16 a[94:95], v137 offset:20480\n"
      "v_mfma_f32_16x16x32_bf16 v[40:43], a[64:67], a[8:11], v[40:43]\n"
      "v_mfma_f32_16x16x32_bf16 v[72:75], a[64:67], a[12:15], v[72:75]\n"
      "s_waitcnt lgkmcnt(12)\n"
      "v_mfma_f32_16x16x32_bf16 v[12:15], a[68:71], a[4:7], v[12:15]\n"
      "ds_read_b64_tr_b16 a[64:65], v136 offset:24576\n"
      "ds_read_b64_tr_b16 a[66:67], v136 offset:28672\n"
      "v_mfma_f32_16x16x32_bf16 v[44:47], a[68:71], a[8:11], v[44:47]\n"
      "v_mfma_f32_16x16x32_bf16 v[76:79], a[68:71], a[12:15], v[76:79]\n"
      "s_waitcnt lgkmcnt(12)\n"
      "v_mfma_f32_16x16x32_bf16 v[16:19], a[72:75], a[4:7], v[16:19]\n"
      "s_add_u32 s47, s53, 38912\n"
      "buffer_load_dwordx4 v[160:163], v248, %[rO], s47 offen sc1\n"
      "v_mfma_f32_16x16x32_bf16 v[48:51], a[72:75], a[8:11], v[48:51]\n"
      "ds_read_b64_tr_b16 a[68:69], v137 offset:24576\n"
      "ds_read_b64_tr_b16 a[70:71], v137 offset:28672\n"
      "v_mfma_f32_16x16x32_bf16 v[80:83], a[72:75], a[12:15], v[80:83]\n"
      "s_waitcnt lgkmcnt(12)\n"
      "v_mfma_f32_16x16x32_bf16 v[20:23], a[76:79], a[4:7], v[20:23]\n"
      "v_mfma_f32_16x16x32_bf16 v[52:55], a[76:79], a[8:11], v[52:55]\n"
      "buffer_load_dwordx4 v[164:167], v248, %[rO], s47 offen offset:1024 sc1\n"
      "v_mfma_f32_16x16x32_bf16 v[84:87], a[76:79], a[12:15], v[84:87]\n"
      "s_waitcnt lgkmcnt(10)\n"
      "ds_read_b64_tr_b16 a[72:73], v138 offset:24576\n"
      "ds_read_b64_tr_b16 a[74:75], v138 offset:28672\n"
      "v_mfma_f32_16x16x32_bf16 v[24:27], a[80:83], a[4:7], v[24:27]\n"
      "v_mfma_f32_16x16x32_bf16 v[56:59], a[80:83], a[8:11], v[56:59]\n"
      "v_mfma_f32_16x16x32_bf16 v[88:91], a[80:83], a[12:15], v[88:91]\n"
      "s_waitcnt lgkmcnt(10)\n"
      "ds_read_b64_tr_b16 a[76:77], v139 offset:24576\n"
      "ds_read_b64_tr_b16 a[78:79], v139 offset:28672\n"
      "v_mfma_f32_16x16x32_bf16 v[28:31], a[84:87], a[4:7], v[28:31]\n"
      "v_mfma_f32_16x16x32_bf16 v[60:63], a[84:87], a[8:11], v[60:63]\n"
      "buffer_load_dwordx4 v[168:171], v248, %[rO], s47 offen offset:2048 sc1\n"
      "v_mfma_f32_16x16x32_bf16 v[92:95], a[84:87], a[12:15], v[92:95]\n"
      "s_waitcnt lgkmcnt(10)\n"
      "ds_read_b64_tr_b16 a[80:81], v140 offset:24576\n"
      "ds_read_b64_tr_b16 a[82:83], v140 offset:28672\n"
      "v_mfma_f32_16x16x32_bf16 v[0:3], a[88:91], a[4:7], v[0:3]\n"
      "v_mfma_f32_16x16x32_bf16 v[32:35], a[88:91], a[8:11], v[32:35]\n"
      "v_mfma_f32_16x16x32_bf16 v[64:67], a[88:91], a[12:15], v[64:67]\n"
      "ds_read_b64_tr_b16 a[84:85], v141 offset:24576\n"
      "ds_read_b64_tr_b16 a[86:87], v141 offset:28672\n"
      "v_mfma_f32_16x16x32_bf16 v[96:99], a[88:91], a[16:19], v[96:99]\n"
      "s_waitcnt lgkmcnt(12)\n"
      "v_mfma_f32_16x16x32_bf16 v[4:7], a[92:95], a[4:7], v[4:7]\n"
      "s_add_u32 s47, s54, 26624\n"
      "buffer_load_dwordx4 v[176:179], v248, %[rO], s47 offen sc1\n"
      "v_mfma_f32_16x16x32_bf16 v[36:39], a[92:95], a[8:11], v[36:39]\n"
      "ds_read_b64_tr_b16 a[88:89], v142 offset:24576\n"
      "ds_read_b64_tr_b16 a[90:91], v142 offset:28672\n"
      "v_mfma_f32_16x16x32_bf16 v[68:71], a[92:95], a[12:15], v[68:71]\n"
      "v_mfma_f32_16x16x32_bf16 v[100:103], a[92:95], a[16:19], v[100:103]\n"
      "s_waitcnt lgkmcnt(12)\n"
      "v_mfma_f32_16x16x32_bf16 v[0:3], a[64:67], a[20:23], v[0:3]\n"
      "ds_read_b64_tr_b16 a[92:93], v143 offset:24576\n"
      "ds_read_b64_tr_b16 a[94:95], v143 offset:28672\n"
      "v_mfma_f32_16x16x32_bf16 v[32:35], a[64:67], a[24:27], v[32:35]\n"
      "buffer_load_dwordx4 v[180:183], v248, %[rO], s47 offen offset:1024 sc1\n"
      "v_mfma_f32_16x16x32_bf16 v[64:67], a[64:67], a[28:31], v[64:67]\n"
      "v_mfma_f32_16x16x32_bf16 v[96:99], a[64:67], a[32:35], v[96:99]\n"
      "s_waitcnt lgkmcnt(12)\n"
      "v_mfma_f32_16x16x32_bf16 v[4:7], a[68:71], a[20:23], v[4:7]\n"
      "v_mfma_f32_16x16x32_bf16 v[36:39], a[68:71], a[24:27], v[36:39]\n"
      "v_mfma_f32_16x16x32_bf16 v[68:71], a[68:71], a[28:31], v[68:71]\n"
      "buffer_load_dwordx4 v[184:187], v248, %[rO], s47 offen offset:2048 sc1\n"
      "v_mfma_f32_16x16x32_bf16 v[100:103], a[68:71], a[32:35], v[100:103]\n"
      "s_waitcnt lgkmcnt(10)\n"
      "v_mfma_f32_16x16x32_bf16 v[8:11], a[72:75], a[20:23], v[8:11]\n"
      "v_mfma_f32_16x16x32_bf16 v[40:43], a[72:75], a[24:27], v[40:43]\n"
      "v_mfma_f32_16x16x32_bf16 v[72:75], a[72:75], a[28:31], v[72:75]\n"
      "s_waitcnt lgkmcnt(8)\n"
      "v_mfma_f32_16x16x32_bf16 v[12:15], a[76:79], a[20:23], v[12:15]\n"
      "v_mfma_f32_16x16x32_bf16 v[44:47], a[76:79], a[24:27], v[44:47]\n"
      "s_add_u32 s47, s54, 38912\n"
      "buffer_load_dwordx4 v[192:195], v248, %[rO], s47 offen sc1\n"
      "v_mfma_f32_16x16x32_bf16 v[76:79], a[76:79], a[28:31], v[76:79]\n"
      "s_waitcnt lgkmcnt(6)\n"
      "v_mfma_f32_16x16x32_bf16 v[16:19], a[80:83], a[20:23], v[16:19]\n"
      "s_waitcnt vmcnt(12) lgkmcnt(0)\n"
      "s_barrier\n"
      "ds_read_b64_tr_b16 a[64:65], v130 offset:0\n"
      "ds_read_b64_tr_b16 a[66:67], v130 offset:4096\n"
      "v_mfma_f32_16x16x32_bf16 v[48:51], a[80:83], a[24:27], v[48:51]\n"
      "ds_read_b64_tr_b16 a[68:69], v131 offset:0\n"
      "ds_read_b64_tr_b16 a[70:71], v131 offset:4096\n"
      "v_mfma_f32_16x16x32_bf16 v[80:83], a[80:83], a[28:31], v[80:83]\n"
      "ds_read_b64_tr_b16 a[72:73], v132 offset:0\n"
      "ds_read_b64_tr_b16 a[74:75], v132 offset:4096\n"
      "v_mfma_f32_16x16x32_bf16 v[20:23], a[84:87], a[20:23], v[20:23]\n"
      "ds_read_b64_tr_b16 a[76:77], v133 offset:0\n"
      "ds_read_b64_tr_b16 a[78:79], v133 offset:4096\n"
      "v_mfma_f32_16x16x32_bf16 v[52:55], a[84:87], a[24:27], v[52:55]\n"
      "ds_read_b64_tr_b16 a[80:81], v134 offset:0\n"
      "ds_read_b64_tr_b16 a[82:83], v134 offset:4096\n"
      "v_mfma_f32_16x16x32_bf16 v[84:87], a[84:87], a[28:31], v[84:87]\n"
      "buffer_load_dwordx4 v[196:199], v248, %[rO], s47 offen offset:1024 sc1\n"
      "ds_read_b64_tr_b16 a[84:85], v135 offset:0\n"
      "v_mfma_f32_16x16x32_bf16 v[24:27], a[88:91], a[20:23], v[24:27]\n"
      "ds_read_b64_tr_b16 a[86:87], v135 offset:4096\n"
      "v_mfma_f32_16x16x32_bf16 v[56:59], a[88:91], a[24:27], v[56:59]\n"
      "v_mfma_f32_16x16x32_bf16 v[88:91], a[88:91], a[28:31], v[88:91]\n"
      "ds_read_b64_tr_b16 a[88:89], v128 offset:0\n"
      "ds_read_b64_tr_b16 a[90:91], v128 offset:4096\n"
      "v_mfma_f32_16x16x32_bf16 v[28:31], a[92:95], a[20:23], v[28:31]\n"
      "buffer_load_dwordx4 v[200:203], v248, %[rO], s47 offen offset:2048 sc1\n"
      "v_mfma_f32_16x16x32_bf16 v[60:63], a[92:95], a[24:27], v[60:63]\n"
      "v_mfma_f32_16x16x32_bf16 v[92:95], a[92:95], a[28:31], v[92:95]\n"
      "s_waitcnt lgkmcnt(12)\n"
      "ds_read_b64_tr_b16 a[92:93], v129 offset:0\n"
      "ds_read_b64_tr_b16 a[94:95], v129 offset:4096\n"
      "v_mfma_f32_16x16x32_bf16 v[8:11], a[64:67], a[36:39], v[8:11]\n"
      "v_mfma_f32_16x16x32_bf16 v[40:43], a[64:67], a[40:43], v[40:43]\n"
      "v_mfma_f32_16x16x32_bf16 v[72:75], a[64:67], a[44:47], v[72:75]\n"
      "s_waitcnt lgkmcnt(12)\n"
      "v_mfma_f32_16x16x32_bf16 v[12:15], a[68:71], a[36:39], v[12:15]\n"
      "s_add_u32 s47, s55, 26624\n"
      "buffer_load_dwordx4 v[208:211], v248, %[rO], s47 offen sc1\n"
      "ds_read_b64_tr_b16 a[64:65], v128 offset:8192\n"
      "v_mfma_f32_16x16x32_bf16 v[44:47], a[68:71], a[40:43], v[44:47]\n"
      "ds_read_b64_tr_b16 a[66:67], v128 offset:12288\n"
      "v_mfma_f32_16x16x32_bf16 v[76:79], a[68:71], a[44:47], v[76:79]\n"
      "s_waitcnt lgkmcnt(12)\n"
      "v_mfma_f32_16x16x32_bf16 v[16:19], a[72:75], a[36:39], v[16:19]\n"
      "v_mfma_f32_16x16x32_bf16 v[48:51], a[72:75], a[40:43], v[48:51]\n"
      "ds_read_b64_tr_b16 a[68:69], v129 offset:8192\n"
      "ds_read_b64_tr_b16 a[70:71], v129 offset:12288\n"
      "v_mfma_f32_16x16x32_bf16 v[80:83], a[72:75], a[44:47], v[80:83]\n"
      "s_waitcnt lgkmcnt(12)\n"
      "buffer_load_dwordx4 v[212:215], v248, %[rO], s47 offen offset:1024 sc1\n"
      "v_mfma_f32_16x16x32_bf16 v[20:23], a[76:79], a[36:39], v[20:23]\n"
      "v_mfma_f32_16x16x32_bf16 v[52:55], a[76:79], a[40:43], v[52:55]\n"
      "v_mfma_f32_16x16x32_bf16 v[84:87], a[76:79], a[44:47], v[84:87]\n"
      "s_waitcnt lgkmcnt(10)\n"
      "ds_read_b64_tr_b16 a[72:73], v130 offset:8192\n"
      "ds_read_b64_tr_b16 a[74:75], v130 offset:12288\n"
      "v_mfma_f32_16x16x32_bf16 v[24:27], a[80:83], a[36:39], v[24:27]\n"
      "v_mfma_f32_16x16x32_bf16 v[56:59], a[80:83], a[40:43], v[56:59]\n"
      "v_mfma_f32_16x16x32_bf16 v[88:91], a[80:83], a[44:47], v[88:91]\n"
      "s_waitcnt lgkmcnt(10)\n"
      "buffer_load_dwordx4 a[116:119], v248, %[rO], s47 offen offset:2048 sc1\n"
      "ds_read_b64_tr_b16 a[76:77], v131 offset:8192\n"
      "v_mfma_f32_16x16x32_bf16 v[28:31], a[84:87], a[36:39], v[28:31]\n"
      "ds_read_b64_tr_b16 a[78:79], v131 offset:12288\n"
      "v_mfma_f32_16x16x32_bf16 v[60:63], a[84:87], a[40:43], v[60:63]\n"
      "v_mfma_f32_16x16x32_bf16 v[92:95], a[84:87], a[44:47], v[92:95]\n"
      "s_waitcnt lgkmcnt(10)\n"
      "ds_read_b64_tr_b16 a[80:81], v132 offset:8192\n"
      "ds_read_b64_tr_b16 a[82:83], v132 offset:12288\n"
      "v_mfma_f32_16x16x32_bf16 v[0:3], a[88:91], a[36:39], v[0:3]\n"
      "v_mfma_f32_16x16x32_bf16 v[32:35], a[88:91], a[40:43], v[32:35]\n"
      "s_add_u32 s47, s55, 38912\n"
      "buffer_load_dwordx4 a[124:127], v248, %[rO], s47 offen sc1\n"
      "v_mfma_f32_16x16x32_bf16 v[64:67], a[88:91], a[44:47], v[64:67]\n"
      "ds_read_b64_tr_b16 a[84:85], v133 offset:8192\n"
      "ds_read_b64_tr_b16 a[86:87], v133 offset:12288\n"
      "v_mfma_f32_16x16x32_bf16 v[96:99], a[88:91], a[48:51], v[96:99]\n"
      "s_waitcnt lgkmcnt(12)\n"
      "v_mfma_f32_16x16x32_bf16 v[4:7], a[92:95], a[36:39], v[4:7]\n"
      "v_mfma_f32_16x16x32_bf16 v[36:39], a[92:95], a[40:43], v[36:39]\n"
      "ds_read_b64_tr_b16 a[88:89], v134 offset:8192\n"
      "ds_read_b64_tr_b16 a[90:91], v134 offset:12288\n"
      "v_mfma_f32_16x16x32_bf16 v[68:71], a[92:95], a[44:47], v[68:71]\n"
      "v_mfma_f32_16x16x32_bf16 v[100:103], a[92:95], a[48:51], v[100:103]\n"
      "s_waitcnt lgkmcnt(12)\n"
      "buffer_load_dwordx4 a[128:131], v248, %[rO], s47 offen offset:1024 sc1\n"
      "v_mfma_f32_16x16x32_bf16 v[0:3], a[64:67], a[96:99], v[0:3]\n"
      "ds_read_b64_tr_b16 a[92:93], v135 offset:8192\n"
      "ds_read_b64_tr_b16 a[94:95], v135 offset:12288\n"
      "v_mfma_f32_16x16x32_bf16 v[32:35], a[64:67], a[100:103], v[32:35]\n"
      "v_mfma_f32_16x16x32_bf16 v[64:67], a[64:67], a[104:107], v[64:67]\n"
      "v_mfma_f32_16x16x32_bf16 v[96:99], a[64:67], a[108:111], v[96:99]\n"
      "s_waitcnt lgkmcnt(12)\n"
      "s_waitcnt vmcnt(18)\n"
      "ds_write_b128 v116, v[156:159] offset:8192\n"
      "s_waitcnt vmcnt(17)\n"
      "ds_write_b128 v116, v[172:175] offset:9216\n"
      "v_mfma_f32_16x16x32_bf16 v[4:7], a[68:71], a[96:99], v[4:7]\n"
      "v_mfma_f32_16x16x32_bf16 v[36:39], a[68:71], a[100:103], v[36:39]\n"
      "buffer_load_dwordx4 a[132:135], v248, %[rO], s47 offen offset:2048 sc1\n"
      "v_mfma_f32_16x16x32_bf16 v[68:71], a[68:71], a[104:107], v[68:71]\n"
      "v_mfma_f32_16x16x32_bf16 v[100:103], a[68:71], a[108:111], v[100:103]\n"
      "s_waitcnt lgkmcnt(12)\n"
      "ds_read_b64_tr_b16 a[64:65], v130 offset:16384\n"
      "ds_read_b64_tr_b16 a[66:67], v130 offset:20480\n"
      "v_mfma_f32_16x16x32_bf16 v[8:11], a[72:75], a[96:99], v[8:11]\n"
      "v_mfma_f32_16x16x32_bf16 v[40:43], a[72:75], a[100:103], v[40:43]\n"
      "v_mfma_f32_16x16x32_bf16 v[72:75], a[72:75], a[104:107], v[72:75]\n"
      "s_waitcnt lgkmcnt(12)\n"
      "ds_read_b64_tr_b16 a[68:69], v131 offset:16384\n"
      "ds_read_b64_tr_b16 a[70:71], v131 offset:20480\n"
      "v_mfma_f32_16x16x32_bf16 v[12:15], a[76:79], a[96:99], v[12:15]\n"
      "v_mfma_f32_16x16x32_bf16 v[44:47], a[76:79], a[100:103], v[44:47]\n"
      "v_mfma_f32_16x16x32_bf16 v[76:79], a[76:79], a[104:107], v[76:79]\n"
      "s_waitcnt lgkmcnt(12)\n"
      "ds_read_b64_tr_b16 a[72:73], v130 offset:32768\n"
      "ds_read_b64_tr_b16 a[74:75], v130 offset:36864\n"
      "v_mfma_f32_16x16x32_bf16 v[16:19], a[80:83], a[96:99], v[16:19]\n"
      "v_mfma_f32_16x16x32_bf16 v[48:51], a[80:83], a[100:103], v[48:51]\n"
      "v_mfma_f32_16x16x32_bf16 v[80:83], a[80:83], a[104:107], v[80:83]\n"
      "s_waitcnt lgkmcnt(6)\n"
      "s_barrier\n"
      "ds_read_b64_tr_b16 a[76:77], v131 offset:32768\n"
      "ds_read_b64_tr_b16 a[78:79], v131 offset:36864\n"
      "v_mfma_f32_16x16x32_bf16 v[20:23], a[84:87], a[96:99], v[20:23]\n"
      "ds_read_b128 v[156:159], v250 offset:8192\n"
      "ds_read_b64_tr_b16 a[80:81], v130 offset:49152\n"
      "v_mfma_f32_16x16x32_bf16 v[52:55], a[84:87], a[100:103], v[52:55]\n"
      "ds_read_b64_tr_b16 a[82:83], v130 offset:53248\n"
      "v_mfma_f32_16x16x32_bf16 v[84:87], a[84:87], a[104:107], v[84:87]\n"
      "ds_read_b64_tr_b16 a[84:85], v131 offset:49152\n"
      "ds_read_b64_tr_b16 a[86:87], v131 offset:53248\n"
      "v_mfma_f32_16x16x32_bf16 v[24:27], a[88:91], a[96:99], v[24:27]\n"
      "ds_read_b128 v[172:175], v250 offset:9216\n"
      "v_mfma_f32_16x16x32_bf16 v[56:59], a[88:91], a[100:103], v[56:59]\n"
      "v_mfma_f32_16x16x32_bf16 v[88:91], a[88:91], a[104:107], v[88:91]\n"
      "v_mfma_f32_16x16x32_bf16 v[28:31], a[92:95], a[96:99], v[28:31]\n"
      "v_mfma_f32_16x16x32_bf16 v[60:63], a[92:95], a[100:103], v[60:63]\n"
      "v_mfma_f32_16x16x32_bf16 v[92:95], a[92:95], a[104:107], v[92:95]\n"
      "s_waitcnt vmcnt(17) lgkmcnt(12)\n"
      "ds_read_b64_tr_b16 a[88:89], v130 offset:24576\n"
      "ds_read_b64_tr_b16 a[90:91], v130 offset:28672\n"
      "v_mfma_f32_16x16x32_bf16 v[8:11], a[64:67], v[144:147], v[8:11]\n"
      "s_waitcnt vmcnt(16)\n"
      "v_mfma_f32_16x16x32_bf16 v[40:43], a[64:67], v[148:151], v[40:43]\n"
      "s_waitcnt vmcnt(15)\n"
      "v_mfma_f32_16x16x32_bf16 v[72:75], a[64:67], v[152:155], v[72:75]\n"
      "s_waitcnt lgkmcnt(12)\n"
      "ds_read_b128 v[188:191], v250 offset:10240\n"
      "ds_read_b64_tr_b16 a[92:93], v131 offset:24576\n"
      "v_mfma_f32_16x16x32_bf16 v[12:15], a[68:71], v[144:147], v[12:15]\n"
      "v_mfma_f32_16x16x32_bf16 v[44:47], a[68:71], v[148:151], v[44:47]\n"
      "v_mfma_f32_16x16x32_bf16 v[76:79], a[68:71], v[152:155], v[76:79]\n"
      "s_waitcnt vmcnt(11) lgkmcnt(12)\n"
      "ds_read_b64_tr_b16 a[94:95], v131 offset:28672\n"
      "ds_read_b128 v[204:207], v250 offset:11264\n"
      "v_mfma_f32_16x16x32_bf16 v[8:11], a[72:75], v[176:179], v[8:11]\n"
      "s_waitcnt vmcnt(10)\n"
      "v_mfma_f32_16x16x32_bf16 v[40:43], a[72:75], v[180:183], v[40:43]\n"
      "s_waitcnt vmcnt(9)\n"
      "v_mfma_f32_16x16x32_bf16 v[72:75], a[72:75], v[184:187], v[72:75]\n"
      "s_waitcnt lgkmcnt(12)\n"
      "ds_read_b64_tr_b16 a[64:65], v130 offset:40960\n"
      "ds_read_b64_tr_b16 a[66:67], v130 offset:45056\n"
      "v_mfma_f32_16x16x32_bf16 v[12:15], a[76:79], v[176:179], v[12:15]\n"
      "v_mfma_f32_16x16x32_bf16 v[44:47], a[76:79], v[180:183], v[44:47]\n"
      "v_mfma_f32_16x16x32_bf16 v[76:79], a[76:79], v[184:187], v[76:79]\n"
      "s_waitcnt vmcnt(5) lgkmcnt(11)\n"
      "ds_read_b64_tr_b16 a[68:69], v131 offset:40960\n"
      "ds_read_b64_tr_b16 a[70:71], v131 offset:45056\n"
      "v_mfma_f32_16x16x32_bf16 v[8:11], a[80:83], v[208:211], v[8:11]\n"
      "s_waitcnt vmcnt(4)\n"
      "ds_read_b128 a[120:123], v250 offset:12288\n"
      "v_mfma_f32_16x16x32_bf16 v[40:43], a[80:83], v[212:215], v[40:43]\n"
      "s_waitcnt vmcnt(3)\n"
      "v_mfma_f32_16x16x32_bf16 v[72:75], a[80:83], a[116:119], v[72:75]\n"
      "s_waitcnt lgkmcnt(12)\n"
      "ds_read_b64_tr_b16 a[72:73], v130 offset:57344\n"
      "ds_read_b64_tr_b16 a[74:75], v130 offset:61440\n"
      "v_mfma_f32_16x16x32_bf16 v[12:15], a[84:87], v[208:211], v[12:15]\n"
      "v_mfma_f32_16x16x32_bf16 v[44:47], a[84:87], v[212:215], v[44:47]\n"
      "v_mfma_f32_16x16x32_bf16 v[76:79], a[84:87], a[116:119], v[76:79]\n"
      "s_waitcnt lgkmcnt(11)\n"
      "ds_read_b128 a[136:139], v250 offset:13312\n"
      "ds_read_b64_tr_b16 a[76:77], v131 offset:57344\n"
      "ds_read_b64_tr_b16 a[78:79], v131 offset:61440\n"
      "v_mfma_f32_16x16x32_bf16 v[8:11], a[88:91], v[160:163], v[8:11]\n"
      "v_mfma_f32_16x16x32_bf16 v[40:43], a[88:91], v[164:167], v[40:43]\n"
      "v_mfma_f32_16x16x32_bf16 v[72:75], a[88:91], v[168:171], v[72:75]\n"
      "s_waitcnt lgkmcnt(11)\n"
      "ds_read_b64_tr_b16 a[80:81], v132 offset:16384\n"
      "ds_read_b64_tr_b16 a[82:83], v132 offset:20480\n"
      "v_mfma_f32_16x16x32_bf16 v[12:15], a[92:95], v[160:163], v[12:15]\n"
      "ds_read_b64_tr_b16 a[84:85], v133 offset:16384\n"
      "v_mfma_f32_16x16x32_bf16 v[44:47], a[92:95], v[164:167], v[44:47]\n"
      "v_mfma_f32_16x16x32_bf16 v[76:79], a[92:95], v[168:171], v[76:79]\n"
      "s_waitcnt lgkmcnt(11)\n"
      "ds_read_b64_tr_b16 a[86:87], v133 offset:20480\n"
      "ds_read_b64_tr_b16 a[88:89], v132 offset:32768\n"
      "v_mfma_f32_16x16x32_bf16 v[8:11], a[64:67], v[192:195], v[8:11]\n"
      "ds_read_b64_tr_b16 a[90:91], v132 offset:36864\n"
      "v_mfma_f32_16x16x32_bf16 v[40:43], a[64:67], v[196:199], v[40:43]\n"
      "v_mfma_f32_16x16x32_bf16 v[72:75], a[64:67], v[200:203], v[72:75]\n"
      "s_waitcnt lgkmcnt(12)\n"
      "ds_read_b64_tr_b16 a[92:93], v133 offset:32768\n"
      "ds_read_b64_tr_b16 a[94:95], v133 offset:36864\n"
      "v_mfma_f32_16x16x32_bf16 v[12:15], a[68:71], v[192:195], v[12:15]\n"
      "v_mfma_f32_16x16x32_bf16 v[44:47], a[68:71], v[196:199], v[44:47]\n"
      "v_mfma_f32_16x16x32_bf16 v[76:79], a[68:71], v[200:203], v[76:79]\n"
      "s_waitcnt vmcnt(2) lgkmcnt(11)\n"
      "ds_read_b64_tr_b16 a[64:65], v132 offset:49152\n"
      "ds_read_b64_tr_b16 a[66:67], v132 offset:53248\n"
      "v_mfma_f32_16x16x32_bf16 v[8:11], a[72:75], a[124:127], v[8:11]\n"
      "s_waitcnt vmcnt(1)\n"
      "ds_read_b64_tr_b16 a[68:69], v133 offset:49152\n"
      "v_mfma_f32_16x16x32_bf16 v[40:43], a[72:75], a[128:131], v[40:43]\n"
      "s_waitcnt vmcnt(0)\n"
      "v_mfma_f32_16x16x32_bf16 v[72:75], a[72:75], a[132:135], v[72:75]\n"
      "s_waitcnt lgkmcnt(11)\n"
      "ds_read_b64_tr_b16 a[70:71], v133 offset:53248\n"
      "ds_read_b64_tr_b16 a[72:73], v132 offset:24576\n"
      "v_mfma_f32_16x16x32_bf16 v[12:15], a[76:79], a[124:127], v[12:15]\n"
      "ds_read_b64_tr_b16 a[74:75], v132 offset:28672\n"
      "v_mfma_f32_16x16x32_bf16 v[44:47], a[76:79], a[128:131], v[44:47]\n"
      "v_mfma_f32_16x16x32_bf16 v[76:79], a[76:79], a[132:135], v[76:79]\n"
      "s_waitcnt lgkmcnt(12)\n"
      "ds_read_b64_tr_b16 a[76:77], v133 offset:24576\n"
      "ds_read_b64_tr_b16 a[78:79], v133 offset:28672\n"
      "v_mfma_f32_16x16x32_bf16 v[16:19], a[80:83], v[144:147], v[16:19]\n"
      "v_mfma_f32_16x16x32_bf16 v[48:51], a[80:83], v[148:151], v[48:51]\n"
      "v_mfma_f32_16x16x32_bf16 v[80:83], a[80:83], v[152:155], v[80:83]\n"
      "s_waitcnt lgkmcnt(12)\n"
      "v_cvt_pk_bf16_f32 v8, v8, v9\n"
      "v_cvt_pk_bf16_f32 v9, v10, v11\n"
      "v_mfma_f32_16x16x32_bf16 v[20:23], a[84:87], v[144:147], v[20:23]\n"
      "v_cvt_pk_bf16_f32 v10, v12, v13\n"
      "v_cvt_pk_bf16_f32 v11, v14, v15\n"
      "v_mfma_f32_16x16x32_bf16 v[52:55], a[84:87], v[148:151], v[52:55]\n"
      "s_sub_u32 s44, %[hq1k], 0\n"
      "ds_read_b64_tr_b16 a[80:81], v132 offset:40960\n"
      "ds_read_b64_tr_b16 a[82:83], v132 offset:45056\n"
      "v_mfma_f32_16x16x32_bf16 v[84:87], a[84:87], v[152:155], v[84:87]\n"
      "s_waitcnt lgkmcnt(12)\n"
      "ds_read_b64_tr_b16 a[84:85], v133 offset:40960\n"
      "ds_read_b64_tr_b16 a[86:87], v133 offset:45056\n"
      "v_mfma_f32_16x16x32_bf16 v[16:19], a[88:91], v[176:179], v[16:19]\n"
      "s_and_b32 s44, s44, 0x1c00\n"
      "s_cselect_b32 s44, s44, 2.0\n"
      "v_mfma_f32_16x16x32_bf16 v[48:51], a[88:91], v[180:183], v[48:51]\n"
      "s_add_u32 s44, s44, 0\n"
      "buffer_store_dwordx4 v[8:11], v252, %[rP], s44 offen sc1\n"
      "v_cvt_pk_bf16_f32 v40, v40, v41\n"
      "v_mfma_f32_16x16x32_bf16 v[80:83], a[88:91], v[184:187], v[80:83]\n"
      "s_waitcnt lgkmcnt(12)\n"
      "v_cvt_pk_bf16_f32 v41, v42, v43\n"
      "v_cvt_pk_bf16_f32 v42, v44, v45\n"
      "v_mfma_f32_16x16x32_bf16 v[20:23], a[92:95], v[176:179], v[20:23]\n"
      "ds_read_b64_tr_b16 a[88:89], v132 offset:57344\n"
      "v_cvt_pk_bf16_f32 v43, v46, v47\n"
      "v_mfma_f32_16x16x32_bf16 v[52:55], a[92:95], v[180:183], v[52:55]\n"
      "s_sub_u32 s44, %[hq1k], 1024\n"
      "ds_read_b64_tr_b16 a[90:91], v132 offset:61440\n"
      "v_mfma_f32_16x16x32_bf16 v[84:87], a[92:95], v[184:187], v[84:87]\n"
      "s_waitcnt lgkmcnt(12)\n"
      "ds_read_b64_tr_b16 a[92:93], v133 offset:57344\n"
      "ds_read_b64_tr_b16 a[94:95], v133 offset:61440\n"
      "v_mfma_f32_16x16x32_bf16 v[16:19], a[64:67], v[208:211], v[16:19]\n"
      "s_and_b32 s44, s44, 0x1c00\n"
      "s_cselect_b32 s44, s44, 2.0\n"
      "v_mfma_f32_16x16x32_bf16 v[48:51], a[64:67], v[212:215], v[48:51]\n"
      "s_add_u32 s44, s44, 8192\n"
      "buffer_store_dwordx4 v[40:43], v252, %[rP], s44 offen sc1\n"
      "v_cvt_pk_bf16_f32 v72, v72, v73\n"
      "v_mfma_f32_16x16x32_bf16 v[80:83], a[64:67], a[116:119], v[80:83]\n"
      "s_waitcnt lgkmcnt(12)\n"
      "v_cvt_pk_bf16_f32 v73, v74, v75\n"
      "v_cvt_pk_bf16_f32 v74, v76, v77\n"
      "v_mfma_f32_16x16x32_bf16 v[20:23], a[68:71], v[208:211], v[20:23]\n"
      "ds_read_b64_tr_b16 a[64:65], v134 offset:16384\n"
      "v_cvt_pk_bf16_f32 v75, v78, v79\n"
      "v_mfma_f32_16x16x32_bf16 v[52:55], a[68:71], v[212:215], v[52:55]\n"
      "s_sub_u32 s44, %[hq1k], 2048\n"
      "ds_read_b64_tr_b16 a[66:67], v134 offset:20480\n"
      "v_mfma_f32_16x16x32_bf16 v[84:87], a[68:71], a[116:119], v[84:87]\n"
      "s_waitcnt lgkmcnt(12)\n"
      "ds_read_b64_tr_b16 a[68:69], v135 offset:16384\n"
      "ds_read_b64_tr_b16 a[70:71], v135 offset:20480\n"
      "v_mfma_f32_16x16x32_bf16 v[16:19], a[72:75], v[160:163], v[16:19]\n"
      "s_and_b32 s44, s44, 0x1c00\n"
      "s_cselect_b32 s44, s44, 2.0\n"
      "v_mfma_f32_16x16x32_bf16 v[48:51], a[72:75], v[164:167], v[48:51]\n"
      "s_add_u32 s44, s44, 16384\n"
      "buffer_store_dwordx4 v[72:75], v252, %[rP], s44 offen sc1\n"
      "v_mfma_f32_16x16x32_bf16 v[80:83], a[72:75], v[168:171], v[80:83]\n"
      "s_waitcnt lgkmcnt(12)\n"
      "ds_read_b64_tr_b16 a[72:73], v134 offset:32768\n"
      "ds_read_b64_tr_b16 a[74:75], v134 offset:36864\n"
      "v_mfma_f32_16x16x32_bf16 v[20:23], a[76:79], v[160:163], v[20:23]\n"
      "v_mfma_f32_16x16x32_bf16 v[52:55], a[76:79], v[164:167], v[52:55]\n"
      "v_mfma_f32_16x16x32_bf16 v[84:87], a[76:79], v[168:171], v[84:87]\n"
      "s_waitcnt lgkmcnt(12)\n"
      "ds_read_b64_tr_b16 a[76:77], v135 offset:32768\n"
      "ds_read_b64_tr_b16 a[78:79], v135 offset:36864\n"
      "v_mfma_f32_16x16x32_bf16 v[16:19], a[80:83], v[192:195], v[16:19]\n"
      "v_mfma_f32_16x16x32_bf16 v[48:51], a[80:83], v[196:199], v[48:51]\n"
      "v_mfma_f32_16x16x32_bf16 v[80:83], a[80:83], v[200:203], v[80:83]\n"
      "s_waitcnt lgkmcnt(12)\n"
      "ds_read_b64_tr_b16 a[80:81], v134 offset:49152\n"
      "ds_read_b64_tr_b16 a[82:83], v134 offset:53248\n"
      "v_mfma_f32_16x16x32_bf16 v[20:23], a[84:87], v[192:195], v[20:23]\n"
      "v_mfma_f32_16x16x32_bf16 v[52:55], a[84:87], v[196:199], v[52:55]\n"
      "v_mfma_f32_16x16x32_bf16 v[84:87], a[84:87], v[200:203], v[84:87]\n"
      "s_waitcnt lgkmcnt(12)\n"
      "ds_read_b64_tr_b16 a[84:85], v135 offset:49152\n"
      "ds_read_b64_tr_b16 a[86:87], v135 offset:53248\n"
      "v_mfma_f32_16x16x32_bf16 v[16:19], a[88:91], a[124:127], v[16:19]\n"
      "v_mfma_f32_16x16x32_bf16 v[48:51], a[88:91], a[128:131], v[48:51]\n"
      "v_mfma_f32_16x16x32_bf16 v[80:83], a[88:91], a[132:135], v[80:83]\n"
      "s_waitcnt lgkmcnt(12)\n"
      "ds_read_b64_tr_b16 a[88:89], v134 offset:24576\n"
      "ds_read_b64_tr_b16 a[90:91], v134 offset:28672\n"
      "v_mfma_f32_16x16x32_bf16 v[20:23], a[92:95], a[124:127], v[20:23]\n"
      "v_mfma_f32_16x16x32_bf16 v[52:55], a[92:95], a[128:131], v[52:55]\n"
      "v_mfma_f32_16x16x32_bf16 v[84:87], a[92:95], a[132:135], v[84:87]\n"
      "s_waitcnt lgkmcnt(12)\n"
      "ds_read_b64_tr_b16 a[92:93], v135 offset:24576\n"
      "ds_read_b64_tr_b16 a[94:95], v135 offset:28672\n"
      "v_mfma_f32_16x16x32_bf16 v[24:27], a[64:67], v[144:147], v[24:27]\n"
      "v_mfma_f32_16x16x32_bf16 v[56:59], a[64:67], v[148:151], v[56:59]\n"
      "v_mfma_f32_16x16x32_bf16 v[88:91], a[64:67], v[152:155], v[88:91]\n"
      "s_waitcnt lgkmcnt(12)\n"
      "v_cvt_pk_bf16_f32 v16, v16, v17\n"
      "v_cvt_pk_bf16_f32 v17, v18, v19\n"
      "v_mfma_f32_16x16x32_bf16 v[28:31], a[68:71], v[144:147], v[28:31]\n"
      "v_cvt_pk_bf16_f32 v18, v20, v21\n"
      "v_cvt_pk_bf16_f32 v19, v22, v23\n"
      "v_mfma_f32_16x16x32_bf16 v[60:63], a[68:71], v[148:151], v[60:63]\n"
      "s_sub_u32 s44, %[hq1k], 3072\n"
      "ds_read_b64_tr_b16 a[64:65], v134 offset:40960\n"
      "ds_read_b64_tr_b16 a[66:67], v134 offset:45056\n"
      "v_mfma_f32_16x16x32_bf16 v[92:95], a[68:71], v[152:155], v[92:95]\n"
      "s_waitcnt lgkmcnt(12)\n"
      "ds_read_b64_tr_b16 a[68:69], v135 offset:40960\n"
      "ds_read_b64_tr_b16 a[70:71], v135 offset:45056\n"
      "v_mfma_f32_16x16x32_bf16 v[24:27], a[72:75], v[176:179], v[24:27]\n"
      "s_and_b32 s44, s44, 0x1c00\n"
      "s_cselect_b32 s44, s44, 2.0\n"
      "v_mfma_f32_16x16x32_bf16 v[56:59], a[72:75], v[180:183], v[56:59]\n"
      "s_add_u32 s44, s44, 24576\n"
      "buffer_store_dwordx4 v[16:19], v252, %[rP], s44 offen sc1\n"
      "v_cvt_pk_bf16_f32 v48, v48, v49\n"
      "v_mfma_f32_16x16x32_bf16 v[88:91], a[72:75], v[184:187], v[88:91]\n"
      "s_waitcnt lgkmcnt(12)\n"
      "v_cvt_pk_bf16_f32 v49, v50, v51\n"
      "v_cvt_pk_bf16_f32 v50, v52, v53\n"
      "v_mfma_f32_16x16x32_bf16 v[28:31], a[76:79], v[176:179], v[28:31]\n"
      "ds_read_b64_tr_b16 a[72:73], v134 offset:57344\n"
      "v_cvt_pk_bf16_f32 v51, v54, v55\n"
      "v_mfma_f32_16x16x32_bf16 v[60:63], a[76:79], v[180:183], v[60:63]\n"
      "s_sub_u32 s44, %[hq1k], 4096\n"
      "ds_read_b64_tr_b16 a[74:75], v134 offset:61440\n"
      "v_mfma_f32_16x16x32_bf16 v[92:95], a[76:79], v[184:187], v[92:95]\n"
      "s_waitcnt lgkmcnt(12)\n"
      "ds_read_b64_tr_b16 a[76:77], v135 offset:57344\n"
      "ds_read_b64_tr_b16 a[78:79], v135 offset:61440\n"
      "v_mfma_f32_16x16x32_bf16 v[24:27], a[80:83], v[208:211], v[24:27]\n"
      "s_and_b32 s44, s44, 0x1c00\n"
      "s_cselect_b32 s44, s44, 2.0\n"
      "v_mfma_f32_16x16x32_bf16 v[56:59], a[80:83], v[212:215], v[56:59]\n"
      "s_add_u32 s44, s44, 32768\n"
      "buffer_store_dwordx4 v[48:51], v252, %[rP], s44 offen sc1\n"
      "v_cvt_pk_bf16_f32 v80, v80, v81\n"
      "v_mfma_f32_16x16x32_bf16 v[88:91], a[80:83], a[116:119], v[88:91]\n"
      "s_waitcnt lgkmcnt(12)\n"
      "v_cvt_pk_bf16_f32 v81, v82, v83\n"
      "v_cvt_pk_bf16_f32 v82, v84, v85\n"
      "v_mfma_f32_16x16x32_bf16 v[28:31], a[84:87], v[208:211], v[28:31]\n"
      "ds_read_b64_tr_b16 a[80:81], v128 offset:16384\n"
      "v_cvt_pk_bf16_f32 v83, v86, v87\n"
      "v_mfma_f32_16x16x32_bf16 v[60:63], a[84:87], v[212:215], v[60:63]\n"
      "s_sub_u32 s44, %[hq1k], 5120\n"
      "ds_read_b64_tr_b16 a[82:83], v128 offset:20480\n"
      "v_mfma_f32_16x16x32_bf16 v[92:95], a[84:87], a[116:119], v[92:95]\n"
      "s_waitcnt lgkmcnt(12)\n"
      "ds_read_b64_tr_b16 a[84:85], v129 offset:16384\n"
      "ds_read_b64_tr_b16 a[86:87], v129 offset:20480\n"
      "v_mfma_f32_16x16x32_bf16 v[24:27], a[88:91], v[160:163], v[24:27]\n"
      "s_and_b32 s44, s44, 0x1c00\n"
      "s_cselect_b32 s44, s44, 2.0\n"
      "v_mfma_f32_16x16x32_bf16 v[56:59], a[88:91], v[164:167], v[56:59]\n"
      "s_add_u32 s44, s44, 40960\n"
      "buffer_store_dwordx4 v[80:83], v252, %[rP], s44 offen sc1\n"
      "v_mfma_f32_16x16x32_bf16 v[88:91], a[88:91], v[168:171], v[88:91]\n"
      "s_waitcnt lgkmcnt(12)\n"
      "v_mfma_f32_16x16x32_bf16 v[28:31], a[92:95], v[160:163], v[28:31]\n"
      "ds_read_b64_tr_b16 a[88:89], v128 offset:32768\n"
      "ds_read_b64_tr_b16 a[90:91], v128 offset:36864\n"
      "v_mfma_f32_16x16x32_bf16 v[60:63], a[92:95], v[164:167], v[60:63]\n"
      "v_mfma_f32_16x16x32_bf16 v[92:95], a[92:95], v[168:171], v[92:95]\n"
      "s_waitcnt lgkmcnt(12)\n"
      "v_mfma_f32_16x16x32_bf16 v[24:27], a[64:67], v[192:195], v[24:27]\n"
      "v_mfma_f32_16x16x32_bf16 v[56:59], a[64:67], v[196:199], v[56:59]\n"
      "ds_read_b64_tr_b16 a[92:93], v129 offset:32768\n"
      "ds_read_b64_tr_b16 a[94:95], v129 offset:36864\n"
      "v_mfma_f32_16x16x32_bf16 v[88:91], a[64:67], v[200:203], v[88:91]\n"
      "s_waitcnt lgkmcnt(12)\n"
      "v_mfma_f32_16x16x32_bf16 v[28:31], a[68:71], v[192:195], v[28:31]\n"
      "v_mfma_f32_16x16x32_bf16 v[60:63], a[68:71], v[196:199], v[60:63]\n"
      "v_mfma_f32_16x16x32_bf16 v[92:95], a[68:71], v[200:203], v[92:95]\n"
      "s_waitcnt lgkmcnt(10)\n"
      "ds_read_b64_tr_b16 a[64:65], v128 offset:49152\n"
      "ds_read_b64_tr_b16 a[66:67], v128 offset:53248\n"
      "v_mfma_f32_16x16x32_bf16 v[24:27], a[72:75], a[124:127], v[24:27]\n"
      "v_mfma_f32_16x16x32_bf16 v[56:59], a[72:75], a[128:131], v[56:59]\n"
      "v_mfma_f32_16x16x32_bf16 v[88:91], a[72:75], a[132:135], v[88:91]\n"
      "s_waitcnt lgkmcnt(10)\n"
      "v_mfma_f32_16x16x32_bf16 v[28:31], a[76:79], a[124:127], v[28:31]\n"
      "ds_read_b64_tr_b16 a[68:69], v129 offset:49152\n"
      "ds_read_b64_tr_b16 a[70:71], v129 offset:53248\n"
      "v_mfma_f32_16x16x32_bf16 v[60:63], a[76:79], a[128:131], v[60:63]\n"
      "v_mfma_f32_16x16x32_bf16 v[92:95], a[76:79], a[132:135], v[92:95]\n"
      "s_waitcnt lgkmcnt(10)\n"
      "v_mfma_f32_16x16x32_bf16 v[0:3], a[80:83], v[144:147], v[0:3]\n"
      "v_mfma_f32_16x16x32_bf16 v[32:35], a[80:83], v[148:151], v[32:35]\n"
      "ds_read_b64_tr_b16 a[72:73], v128 offset:24576\n"
      "ds_read_b64_tr_b16 a[74:75], v128 offset:28672\n"
      "v_mfma_f32_16x16x32_bf16 v[64:67], a[80:83], v[152:155], v[64:67]\n"
      "v_cvt_pk_bf16_f32 v24, v24, v25\n"
      "v_cvt_pk_bf16_f32 v25, v26, v27\n"
      "v_mfma_f32_16x16x32_bf16 v[96:99], a[80:83], v[156:159], v[96:99]\n"
      "s_waitcnt lgkmcnt(10)\n"
      "v_cvt_pk_bf16_f32 v26, v28, v29\n"
      "v_cvt_pk_bf16_f32 v27, v30, v31\n"
      "v_mfma_f32_16x16x32_bf16 v[4:7], a[84:87], v[144:147], v[4:7]\n"
      "s_sub_u32 s44, %[hq1k], 6144\n"
      "v_mfma_f32_16x16x32_bf16 v[36:39], a[84:87], v[148:151], v[36:39]\n"
      "s_and_b32 s44, s44, 0x1c00\n"
      "s_cselect_b32 s44, s44, 2.0\n"
      "ds_read_b64_tr_b16 a[76:77], v129 offset:24576\n"
      "ds_read_b64_tr_b16 a[78:79], v129 offset:28672\n"
      "v_mfma_f32_16x16x32_bf16 v[68:71], a[84:87], v[152:155], v[68:71]\n"
      "s_add_u32 s44, s44, 49152\n"
      "buffer_store_dwordx4 v[24:27], v252, %[rP], s44 offen sc1\n"
      "v_cvt_pk_bf16_f32 v56, v56, v57\n"
      "v_mfma_f32_16x16x32_bf16 v[100:103], a[84:87], v[156:159], v[100:103]\n"
      "s_waitcnt lgkmcnt(10)\n"
      "v_cvt_pk_bf16_f32 v57, v58, v59\n"
      "v_cvt_pk_bf16_f32 v58, v60, v61\n"
      "v_mfma_f32_16x16x32_bf16 v[0:3], a[88:91], v[176:179], v[0:3]\n"
      "v_cvt_pk_bf16_f32 v59, v62, v63\n"
      "v_mfma_f32_16x16x32_bf16 v[32:35], a[88:91], v[180:183], v[32:35]\n"
      "s_sub_u32 s44, %[hq1k], 7168\n"
      "ds_read_b64_tr_b16 a[80:81], v128 offset:40960\n"
      "ds_read_b64_tr_b16 a[82:83], v128 offset:45056\n"
      "v_mfma_f32_16x16x32_bf16 v[64:67], a[88:91], v[184:187], v[64:67]\n"
      "s_and_b32 s44, s44, 0x1c00\n"
      "s_cselect_b32 s44, s44, 2.0\n"
      "v_mfma_f32_16x16x32_bf16 v[96:99], a[88:91], v[188:191], v[96:99]\n"
      "s_waitcnt lgkmcnt(10)\n"
      "v_mfma_f32_16x16x32_bf16 v[4:7], a[92:95], v[176:179], v[4:7]\n"
      "s_add_u32 s44, s44, 57344\n"
      "buffer_store_dwordx4 v[56:59], v252, %[rP], s44 offen sc1\n"
      "v_cvt_pk_bf16_f32 v88, v88, v89\n"
      "v_mfma_f32_16x16x32_bf16 v[36:39], a[92:95], v[180:183], v[36:39]\n"
      "v_cvt_pk_bf16_f32 v89, v90, v91\n"
      "v_cvt_pk_bf16_f32 v90, v92, v93\n"
      "v_mfma_f32_16x16x32_bf16 v[68:71], a[92:95], v[184:187], v[68:71]\n"
      "ds_read_b64_tr_b16 a[84:85], v129 offset:40960\n"
      "v_cvt_pk_bf16_f32 v91, v94, v95\n"
      "v_mfma_f32_16x16x32_bf16 v[100:103], a[92:95], v[188:191], v[100:103]\n"
      "s_waitcnt lgkmcnt(9)\n"
      "ds_read_b64_tr_b16 a[86:87], v129 offset:45056\n"
      "v_mfma_f32_16x16x32_bf16 v[0:3], a[64:67], v[208:211], v[0:3]\n"
      "s_sub_u32 s44, %[hq1k], 0\n"
      "v_mfma_f32_16x16x32_bf16 v[32:35], a[64:67], v[212:215], v[32:35]\n"
      "s_and_b32 s44, s44, 0x1c00\n"
      "s_cselect_b32 s44, s44, 2.0\n"
      "ds_read_b64_tr_b16 a[88:89], v128 offset:57344\n"
      "ds_read_b64_tr_b16 a[90:91], v128 offset:61440\n"
      "v_mfma_f32_16x16x32_bf16 v[64:67], a[64:67], a[116:119], v[64:67]\n"
      "s_add_u32 s44, s44, 65536\n"
      "buffer_store_dwordx4 v[88:91], v252, %[rP], s44 offen sc1\n"
      "v_mfma_f32_16x16x32_bf16 v[96:99], a[64:67], a[120:123], v[96:99]\n"
      "s_waitcnt lgkmcnt(10)\n"
      "v_mfma_f32_16x16x32_bf16 v[4:7], a[68:71], v[208:211], v[4:7]\n"
      "v_mfma_f32_16x16x32_bf16 v[36:39], a[68:71], v[212:215], v[36:39]\n"
      "ds_read_b64_tr_b16 a[92:93], v129 offset:57344\n"
      "ds_read_b64_tr_b16 a[94:95], v129 offset:61440\n"
      "v_mfma_f32_16x16x32_bf16 v[68:71], a[68:71], a[116:119], v[68:71]\n"
      "v_mfma_f32_16x16x32_bf16 v[100:103], a[68:71], a[120:123], v[100:103]\n"
      "s_waitcnt lgkmcnt(10)\n"
      "v_mfma_f32_16x16x32_bf16 v[0:3], a[72:75], v[160:163], v[0:3]\n"
      "v_mfma_f32_16x16x32_bf16 v[32:35], a[72:75], v[164:167], v[32:35]\n"
      "v_mfma_f32_16x16x32_bf16 v[64:67], a[72:75], v[168:171], v[64:67]\n"
      "v_mfma_f32_16x16x32_bf16 v[96:99], a[72:75], v[172:175], v[96:99]\n"
      "s_waitcnt lgkmcnt(8)\n"
      "v_mfma_f32_16x16x32_bf16 v[4:7], a[76:79], v[160:163], v[4:7]\n"
      "v_mfma_f32_16x16x32_bf16 v[36:39], a[76:79], v[164:167], v[36:39]\n"
      "v_mfma_f32_16x16x32_bf16 v[68:71], a[76:79], v[168:171], v[68:71]\n"
      "v_mfma_f32_16x16x32_bf16 v[100:103], a[76:79], v[172:175], v[100:103]\n"
      "s_waitcnt lgkmcnt(6)\n"
      "v_mfma_f32_16x16x32_bf16 v[0:3], a[80:83], v[192:195], v[0:3]\n"
      "v_mfma_f32_16x16x32_bf16 v[32:35], a[80:83], v[196:199], v[32:35]\n"
      "v_mfma_f32_16x16x32_bf16 v[64:67], a[80:83], v[200:203], v[64:67]\n"
      "v_mfma_f32_16x16x32_bf16 v[96:99], a[80:83], v[204:207], v[96:99]\n"
      "s_waitcnt lgkmcnt(4)\n"
      "v_mfma_f32_16x16x32_bf16 v[4:7], a[84:87], v[192:195], v[4:7]\n"
      "v_mfma_f32_16x16x32_bf16 v[36:39], a[84:87], v[196:199], v[36:39]\n"
      "v_mfma_f32_16x16x32_bf16 v[68:71], a[84:87], v[200:203], v[68:71]\n"
      "v_mfma_f32_16x16x32_bf16 v[100:103], a[84:87], v[204:207], v[100:103]\n"
      "s_waitcnt lgkmcnt(2)\n"
      "v_mfma_f32_16x16x32_bf16 v[0:3], a[88:91], a[124:127], v[0:3]\n"
      "v_mfma_f32_16x16x32_bf16 v[32:35], a[88:91], a[128:131], v[32:35]\n"
      "v_mfma_f32_16x16x32_bf16 v[64:67], a[88:91], a[132:135], v[64:67]\n"
      "v_mfma_f32_16x16x32_bf16 v[96:99], a[88:91], a[136:139], v[96:99]\n"
      "s_waitcnt lgkmcnt(0)\n"
      "v_mfma_f32_16x16x32_bf16 v[4:7], a[92:95], a[124:127], v[4:7]\n"
      "v_mfma_f32_16x16x32_bf16 v[36:39], a[92:95], a[128:131], v[36:39]\n"
      "v_mfma_f32_16x16x32_bf16 v[68:71], a[92:95], a[132:135], v[68:71]\n"
      "v_mfma_f32_16x16x32_bf16 v[100:103], a[92:95], a[136:139], v[100:103]\n"
      "v_cvt_pk_bf16_f32 v0, v0, v1\n"
      "v_cvt_pk_bf16_f32 v1, v2, v3\n"
      "s_nop 2\n"
      "v_cvt_pk_bf16_f32 v2, v4, v5\n"
      "v_cvt_pk_bf16_f32 v3, v6, v7\n"
      "s_sub_u32 s44, %[hq1k], 1024\n"
      "s_and_b32 s44, s44, 0x1c00\n"
      "s_cselect_b32 s44, s44, 2.0\n"
      "s_add_u32 s44, s44, 73728\n"
      "buffer_store_dwordx4 v[0:3], v252, %[rP], s44 offen sc1\n"
      "v_cvt_pk_bf16_f32 v32, v32, v33\n"
      "v_cvt_pk_bf16_f32 v33, v34, v35\n"
      "v_cvt_pk_bf16_f32 v34, v36, v37\n"
      "v_cvt_pk_bf16_f32 v35, v38, v39\n"
      "s_sub_u32 s44, %[hq1k], 2048\n"
      "s_and_b32 s44, s44, 0x1c00\n"
      "s_cselect_b32 s44, s44, 2.0\n"
      "s_add_u32 s44, s44, 81920\n"
      "buffer_store_dwordx4 v[32:35], v252, %[rP], s44 offen sc1\n"
      "v_cvt_pk_bf16_f32 v64, v64, v65\n"
      "v_cvt_pk_bf16_f32 v65, v66, v67\n"
      "v_cvt_pk_bf16_f32 v66, v68, v69\n"
      "v_cvt_pk_bf16_f32 v67, v70, v71\n"
      "s_sub_u32 s44, %[hq1k], 3072\n"
      "s_and_b32 s44, s44, 0x1c00\n"
      "s_cselect_b32 s44, s44, 2.0\n"
      "s_add_u32 s44, s44, 90112\n"
      "buffer_store_dwordx4 v[64:67], v252, %[rP], s44 offen sc1\n"
      "v_cvt_pk_bf16_f32 v96, v96, v97\n"
      "v_cvt_pk_bf16_f32 v97, v98, v99\n"
      "v_cvt_pk_bf16_f32 v98, v100, v101\n"
      "v_cvt_pk_bf16_f32 v99, v102, v103\n"
      "s_sub_u32 s44, %[hq1k], 4096\n"
      "s_and_b32 s44, s44, 0x1c00\n"
      "s_cselect_b32 s44, s44, 2.0\n"
      "s_add_u32 s44, s44, 98304\n"
      "buffer_store_dwordx4 v[96:99], v252, %[rP], s44 offen sc1\n"
      "s_cmp_lg_u32 %[hq1k], 0\n"
      "s_cbranch_scc1 .Lmha_ok0\n"
      "v_mov_b32 v104, v8\n"
      "v_mov_b32 v105, v9\n"
      "v_mov_b32 v106, v10\n"
      "v_mov_b32 v107, v11\n"
      "v_mov_b32 v136, v88\n"
      "v_mov_b32 v137, v89\n"
      "v_mov_b32 v138, v90\n"
      "v_mov_b32 v139, v91\n"
      ".Lmha_ok0:\n"
      "s_cmp_lg_u32 %[hq1k], 1024\n"
      "s_cbranch_scc1 .Lmha_ok1\n"
      "v_mov_b32 v104, v40\n"
      "v_mov_b32 v105, v41\n"
      "v_mov_b32 v106, v42\n"
      "v_mov_b32 v107, v43\n"
      "v_mov_b32 v136, v0\n"
      "v_mov_b32 v137, v1\n"
      "v_mov_b32 v138, v2\n"
      "v_mov_b32 v139, v3\n"
      ".Lmha_ok1:\n"
      "s_cmp_lg_u32 %[hq1k], 2048\n"
      "s_cbranch_scc1 .Lmha_ok2\n"
      "v_mov_b32 v104, v72\n"
      "v_mov_b32 v105, v73\n"
      "v_mov_b32 v106, v74\n"
      "v_mov_b32 v107, v75\n"
      "v_mov_b32 v136, v32\n"
      "v_mov_b32 v137, v33\n"
      "v_mov_b32 v138, v34\n"
      "v_mov_b32 v139, v35\n"
      ".Lmha_ok2:\n"
      "s_cmp_lg_u32 %[hq1k], 3072\n"
      "s_cbranch_scc1 .Lmha_ok3\n"
      "v_mov_b32 v104, v16\n"
      "v_mov_b32 v105, v17\n"
      "v_mov_b32 v106, v18\n"
      "v_mov_b32 v107, v19\n"
      "v_mov_b32 v136, v64\n"
      "v_mov_b32 v137, v65\n"
      "v_mov_b32 v138, v66\n"
      "v_mov_b32 v139, v67\n"
      ".Lmha_ok3:\n"
      "s_cmp_lg_u32 %[hq1k], 4096\n"
      "s_cbranch_scc1 .Lmha_ok4\n"
      "v_mov_b32 v104, v48\n"
      "v_mov_b32 v105, v49\n"
      "v_mov_b32 v106, v50\n"
      "v_mov_b32 v107, v51\n"
      "v_mov_b32 v136, v96\n"
      "v_mov_b32 v137, v97\n"
      "v_mov_b32 v138, v98\n"
      "v_mov_b32 v139, v99\n"
      ".Lmha_ok4:\n"
      "s_cmp_lg_u32 %[hq1k], 5120\n"
      "s_cbranch_scc1 .Lmha_ok5\n"
      "v_mov_b32 v104, v80\n"
      "v_mov_b32 v105, v81\n"
      "v_mov_b32 v106, v82\n"
      "v_mov_b32 v107, v83\n"
      ".Lmha_ok5:\n"
      "s_cmp_lg_u32 %[hq1k], 6144\n"
      "s_cbranch_scc1 .Lmha_ok6\n"
      "v_mov_b32 v104, v24\n"
      "v_mov_b32 v105, v25\n"
      "v_mov_b32 v106, v26\n"
      "v_mov_b32 v107, v27\n"
      ".Lmha_ok6:\n"
      "s_cmp_lg_u32 %[hq1k], 7168\n"
      "s_cbranch_scc1 .Lmha_ok7\n"
      "v_mov_b32 v104, v56\n"
      "v_mov_b32 v105, v57\n"
      "v_mov_b32 v106, v58\n"
      "v_mov_b32 v107, v59\n"
      ".Lmha_ok7:\n"
      "v_lshlrev_b32 v168, 16, v104\n"
      "v_and_b32 v169, 0xffff0000, v104\n"
      "v_lshlrev_b32 v170, 16, v105\n"
      "v_and_b32 v171, 0xffff0000, v105\n"
      "v_lshlrev_b32 v172, 16, v106\n"
      "v_and_b32 v173, 0xffff0000, v106\n"
      "v_lshlrev_b32 v174, 16, v107\n"
      "v_and_b32 v175, 0xffff0000, v107\n"
      "v_lshlrev_b32 v176, 16, v136\n"
      "v_and_b32 v177, 0xffff0000, v136\n"
      "v_lshlrev_b32 v178, 16, v137\n"
      "v_and_b32 v179, 0xffff0000, v137\n"
      "v_lshlrev_b32 v180, 16, v138\n"
      "v_and_b32 v181, 0xffff0000, v138\n"
      "v_lshlrev_b32 v182, 16, v139\n"
      "v_and_b32 v183, 0xffff0000, v139\n"
      "s_add_u32 s52, %[red0], 4096\n"
      "s_add_u32 s53, %[red1], 4096\n"
      "s_mov_b32 s50, 1\n"
      "s_mov_b32 s45, 0x40000\n"
      "buffer_load_dword v251, v254, %[rC], 0 offen offset:80 sc1\n"
      "s_waitcnt vmcnt(0)\n"
      "s_barrier\n"
      "s_cmp_lg_u32 %[w], 0\n"
      "s_cbranch_scc1 .Lmha_6\n"
      "s_mov_b64 exec, s[56:57]\n"
      "buffer_store_dword v243, v242, %[rC], 0 offen offset:80 sc1\n"
      "s_mov_b64 exec, -1\n"
      ".Lmha_6:\n"
      "s_branch .Lmha_r1\n"
      ".Lmha_rp:\n"
      "buffer_load_dword v251, v254, %[rC], 0 offen offset:80 sc1\n"
      "s_waitcnt vmcnt(0)\n"
      ".Lmha_r1:\n"
      "v_cmp_eq_u32 vcc, v251, v255\n"
      "s_sub_u32 s45, s45, 1\n"
      "s_cselect_b32 s51, -1, vcc_lo\n"
      "s_andn2_b32 s51, s51, s50\n"
      "s_or_b32 s50, s50, s51\n"
      "s_bitcmp1_b32 s51, 1\n"
      "s_cbranch_scc0 .Lmha_rk1\n"
      "buffer_load_dwordx4 v[108:111], v252, %[rP], %[red0] offen offset:1024 sc1\n"
      "buffer_load_dwordx4 v[140:143], v252, %[rP], %[red1] offen offset:1024 sc1\n"
      ".Lmha_rk1:\n"
      "s_bitcmp1_b32 s51, 2\n"
      "s_cbranch_scc0 .Lmha_rk2\n"
      "buffer_load_dwordx4 v[112:115], v252, %[rP], %[red0] offen offset:2048 sc1\n"
      "buffer_load_dwordx4 v[144:147], v252, %[rP], %[red1] offen offset:2048 sc1\n"
      ".Lmha_rk2:\n"
      "s_bitcmp1_b32 s51, 3\n"
      "s_cbranch_scc0 .Lmha_rk3\n"
      "buffer_load_dwordx4 v[116:119], v252, %[rP], %[red0] offen offset:3072 sc1\n"
      "buffer_load_dwordx4 v[148:151], v252, %[rP], %[red1] offen offset:3072 sc1\n"
      ".Lmha_rk3:\n"
      "s_bitcmp1_b32 s51, 4\n"
      "s_cbranch_scc0 .Lmha_rk4\n"
      "buffer_load_dwordx4 v[120:123], v252, %[rP], s52 offen sc1\n"
      "buffer_load_dwordx4 v[152:155], v252, %[rP], s53 offen sc1\n"
      ".Lmha_rk4:\n"
      "s_bitcmp1_b32 s51, 5\n"
      "s_cbranch_scc0 .Lmha_rk5\n"
      "buffer_load_dwordx4 v[124:127], v252, %[rP], s52 offen offset:1024 sc1\n"
      "buffer_load_dwordx4 v[156:159], v252, %[rP], s53 offen offset:1024 sc1\n"
      ".Lmha_rk5:\n"
      "s_bitcmp1_b32 s51, 6\n"
      "s_cbranch_scc0 .Lmha_rk6\n"
      "buffer_load_dwordx4 v[128:131], v252, %[rP], s52 offen offset:2048 sc1\n"
      "buffer_load_dwordx4 v[160:163], v252, %[rP], s53 offen offset:2048 sc1\n"
      ".Lmha_rk6:\n"
      "s_bitcmp1_b32 s51, 7\n"
      "s_cbranch_scc0 .Lmha_rk7\n"
      "buffer_load_dwordx4 v[132:135], v252, %[rP], s52 offen offset:3072 sc1\n"
      "buffer_load_dwordx4 v[164:167], v252, %[rP], s53 offen offset:3072 sc1\n"
      ".Lmha_rk7:\n"
      "s_and_b32 s51, s50, 0xff\n"
      "s_cmp_lg_u32 s51, 0xff\n"
      "s_cbranch_scc1 .Lmha_rp\n"
      "s_cmp_lg_u32 %[w], 0\n"
      "s_cbranch_scc1 .Lmha_nb\n"
      "s_mov_b64 exec, s[56:57]\n"
      "buffer_store_dword v243, v242, %[rC], 0 offen sc1\n"
      "s_mov_b64 exec, -1\n"
      ".Lmha_nb:\n"
      "s_mov_b32 s48, 0x3f80\n"
      "s_mov_b32 s49, 0x3f800000\n"
      "s_waitcnt vmcnt(0)\n"
      "v_dot2c_f32_bf16 v168, s48, v108\n"
      "v_dot2c_f32_bf16 v169, s49, v108\n"
      "v_dot2c_f32_bf16 v170, s48, v109\n"
      "v_dot2c_f32_bf16 v171, s49, v109\n"
      "v_dot2c_f32_bf16 v172, s48, v110\n"
      "v_dot2c_f32_bf16 v173, s49, v110\n"
      "v_dot2c_f32_bf16 v174, s48, v111\n"
      "v_dot2c_f32_bf16 v175, s49, v111\n"
      "v_dot2c_f32_bf16 v168, s48, v112\n"
      "v_dot2c_f32_bf16 v169, s49, v112\n"
      "v_dot2c_f32_bf16 v170, s48, v113\n"
      "v_dot2c_f32_bf16 v171, s49, v113\n"
      "v_dot2c_f32_bf16 v172, s48, v114\n"
      "v_dot2c_f32_bf16 v173, s49, v114\n"
      "v_dot2c_f32_bf16 v174, s48, v115\n"
      "v_dot2c_f32_bf16 v175, s49, v115\n"
      "v_dot2c_f32_bf16 v168, s48, v116\n"
      "v_dot2c_f32_bf16 v169, s49, v116\n"
      "v_dot2c_f32_bf16 v170, s48, v117\n"
      "v_dot2c_f32_bf16 v171, s49, v117\n"
      "v_dot2c_f32_bf16 v172, s48, v118\n"
      "v_dot2c_f32_bf16 v173, s49, v118\n"
      "v_dot2c_f32_bf16 v174, s48, v119\n"
      "v_dot2c_f32_bf16 v175, s49, v119\n"
      "v_dot2c_f32_bf16 v168, s48, v120\n"
      "v_dot2c_f32_bf16 v169, s49, v120\n"
      "v_dot2c_f32_bf16 v170, s48, v121\n"
      "v_dot2c_f32_bf16 v171, s49, v121\n"
      "v_dot2c_f32_bf16 v172, s48, v122\n"
      "v_dot2c_f32_bf16 v173, s49, v122\n"
      "v_dot2c_f32_bf16 v174, s48, v123\n"
      "v_dot2c_f32_bf16 v175, s49, v123\n"
      "v_dot2c_f32_bf16 v168, s48, v124\n"
      "v_dot2c_f32_bf16 v169, s49, v124\n"
      "v_dot2c_f32_bf16 v170, s48, v125\n"
      "v_dot2c_f32_bf16 v171, s49, v125\n"
      "v_dot2c_f32_bf16 v172, s48, v126\n"
      "v_dot2c_f32_bf16 v173, s49, v126\n"
      "v_dot2c_f32_bf16 v174, s48, v127\n"
      "v_dot2c_f32_bf16 v175, s49, v127\n"
      "v_dot2c_f32_bf16 v168, s48, v128\n"
      "v_dot2c_f32_bf16 v169, s49, v128\n"
      "v_dot2c_f32_bf16 v170, s48, v129\n"
      "v_dot2c_f32_bf16 v171, s49, v129\n"
      "v_dot2c_f32_bf16 v172, s48, v130\n"
      "v_dot2c_f32_bf16 v173, s49, v130\n"
      "v_dot2c_f32_bf16 v174, s48, v131\n"
      "v_dot2c_f32_bf16 v175, s49, v131\n"
      "v_dot2c_f32_bf16 v168, s48, v132\n"
      "v_dot2c_f32_bf16 v169, s49, v132\n"
      "v_dot2c_f32_bf16 v170, s48, v133\n"
      "v_dot2c_f32_bf16 v171, s49, v133\n"
      "v_dot2c_f32_bf16 v172, s48, v134\n"
      "v_dot2c_f32_bf16 v173, s49, v134\n"
      "v_dot2c_f32_bf16 v174, s48, v135\n"
      "v_dot2c_f32_bf16 v175, s49, v135\n"
      "s_nop 2\n"
      "v_cvt_pk_bf16_f32 v184, v168, v169\n"
      "v_cvt_pk_bf16_f32 v185, v170, v171\n"
      "v_cvt_pk_bf16_f32 v186, v172, v173\n"
      "v_cvt_pk_bf16_f32 v187, v174, v175\n"
      "s_nop 0\n"
      "v_permlane16_swap_b32 v184, v186\n"
      "v_permlane16_swap_b32 v185, v187\n"
      "buffer_store_dwordx4 v[184:187], v253, %[rOut], %[out0] offen sc1\n"
      "v_dot2c_f32_bf16 v176, s48, v140\n"
      "v_dot2c_f32_bf16 v177, s49, v140\n"
      "v_dot2c_f32_bf16 v178, s48, v141\n"
      "v_dot2c_f32_bf16 v179, s49, v141\n"
      "v_dot2c_f32_bf16 v180, s48, v142\n"
      "v_dot2c_f32_bf16 v181, s49, v142\n"
      "v_dot2c_f32_bf16 v182, s48, v143\n"
      "v_dot2c_f32_bf16 v183, s49, v143\n"
      "v_dot2c_f32_bf16 v176, s48, v144\n"
      "v_dot2c_f32_bf16 v177, s49, v144\n"
      "v_dot2c_f32_bf16 v178, s48, v145\n"
      "v_dot2c_f32_bf16 v179, s49, v145\n"
      "v_dot2c_f32_bf16 v180, s48, v146\n"
      "v_dot2c_f32_bf16 v181, s49, v146\n"
      "v_dot2c_f32_bf16 v182, s48, v147\n"
      "v_dot2c_f32_bf16 v183, s49, v147\n"
      "v_dot2c_f32_bf16 v176, s48, v148\n"
      "v_dot2c_f32_bf16 v177, s49, v148\n"
      "v_dot2c_f32_bf16 v178, s48, v149\n"
      "v_dot2c_f32_bf16 v179, s49, v149\n"
      "v_dot2c_f32_bf16 v180, s48, v150\n"
      "v_dot2c_f32_bf16 v181, s49, v150\n"
      "v_dot2c_f32_bf16 v182, s48, v151\n"
      "v_dot2c_f32_bf16 v183, s49, v151\n"
      "v_dot2c_f32_bf16 v176, s48, v152\n"
      "v_dot2c_f32_bf16 v177, s49, v152\n"
      "v_dot2c_f32_bf16 v178, s48, v153\n"
      "v_dot2c_f32_bf16 v179, s49, v153\n"
      "v_dot2c_f32_bf16 v180, s48, v154\n"
      "v_dot2c_f32_bf16 v181, s49, v154\n"
      "v_dot2c_f32_bf16 v182, s48, v155\n"
      "v_dot2c_f32_bf16 v183, s49, v155\n"
      "v_dot2c_f32_bf16 v176, s48, v156\n"
      "v_dot2c_f32_bf16 v177, s49, v156\n"
      "v_dot2c_f32_bf16 v178, s48, v157\n"
      "v_dot2c_f32_bf16 v179, s49, v157\n"
      "v_dot2c_f32_bf16 v180, s48, v158\n"
      "v_dot2c_f32_bf16 v181, s49, v158\n"
      "v_dot2c_f32_bf16 v182, s48, v159\n"
      "v_dot2c_f32_bf16 v183, s49, v159\n"
      "v_dot2c_f32_bf16 v176, s48, v160\n"
      "v_dot2c_f32_bf16 v177, s49, v160\n"
      "v_dot2c_f32_bf16 v178, s48, v161\n"
      "v_dot2c_f32_bf16 v179, s49, v161\n"
      "v_dot2c_f32_bf16 v180, s48, v162\n"
      "v_dot2c_f32_bf16 v181, s49, v162\n"
      "v_dot2c_f32_bf16 v182, s48, v163\n"
      "v_dot2c_f32_bf16 v183, s49, v163\n"
      "v_dot2c_f32_bf16 v176, s48, v164\n"
      "v_dot2c_f32_bf16 v177, s49, v164\n"
      "v_dot2c_f32_bf16 v178, s48, v165\n"
      "v_dot2c_f32_bf16 v179, s49, v165\n"
      "v_dot2c_f32_bf16 v180, s48, v166\n"
      "v_dot2c_f32_bf16 v181, s49, v166\n"
      "v_dot2c_f32_bf16 v182, s48, v167\n"
      "v_dot2c_f32_bf16 v183, s49, v167\n"
      "s_nop 2\n"
      "v_cvt_pk_bf16_f32 v188, v176, v177\n"
      "v_cvt_pk_bf16_f32 v189, v178, v179\n"
      "v_cvt_pk_bf16_f32 v190, v180, v181\n"
      "v_cvt_pk_bf16_f32 v191, v182, v183\n"
      "s_nop 0\n"
      "v_permlane16_swap_b32 v188, v190\n"
      "v_permlane16_swap_b32 v189, v191\n"
      "buffer_store_dwordx4 v[188:191], v253, %[rOut], %[out1] offen sc1\n"
      "s_branch .Lmha_end\n"
      ".Lmha_redo:\n"
      "s_nop 7\n"
      "s_waitcnt vmcnt(0) lgkmcnt(0)\n"
      "s_barrier\n"
      "v_accvgpr_read_b32 v240, a255\n"
      "v_accvgpr_read_b32 v153, a254\n"
      "v_accvgpr_read_b32 v152, a246\n"
      "v_xor_b32 v241, 64, v240\n"
      "v_xor_b32 v242, 128, v240\n"
      "v_xor_b32 v243, 192, v240\n"
      "s_lshl_b32 s58, s43, 3\n"
      "v_add_u32 v244, s58, v240\n"
      "v_add_u32 v245, s58, v241\n"
      "v_add_u32 v246, s58, v242\n"
      "v_add_u32 v247, s58, v243\n"
      "v_xor_b32 v248, v152, v153\n"
      "v_add_u32 v248, s43, v248\n"
      "v_add_u32 v251, s43, v250\n"
      "v_add_u32 v252, 131072, v252\n"
      "v_add_u32 v253, 131072, v253\n"
      "v_add_u32 v254, 131072, v254\n"
      "v_add_u32 v255, 131072, v255\n"
      "v_mov_b32 v40, 0xff800000\n"
      "v_mov_b32 v41, 0xff800000\n"
      "v_mov_b32 v42, 0xff800000\n"
      "v_mov_b32 v43, 0xff800000\n"
      "s_mov_b32 s58, 0\n"
      ".Lmha_mx:\n"
      "s_add_u32 m0, s43, 0\n"
      "s_add_u32 s46, s58, 0\n"
      "buffer_load_dwordx4 v248, %[rK], s46 offen lds\n"
      "s_add_u32 m0, s43, 4096\n"
      "s_add_u32 s46, s58, 4096\n"
      "buffer_load_dwordx4 v248, %[rK], s46 offen lds\n"
      "s_add_u32 m0, s43, 8192\n"
      "s_add_u32 s46, s58, 8192\n"
      "buffer_load_dwordx4 v248, %[rK], s46 offen lds\n"
      "s_add_u32 m0, s43, 12288\n"
      "s_add_u32 s46, s58, 12288\n"
      "buffer_load_dwordx4 v248, %[rK], s46 offen lds\n"
      "s_add_u32 m0, s43, 16384\n"
      "s_add_u32 s46, s58, 16384\n"
      "buffer_load_dwordx4 v248, %[rK], s46 offen lds\n"
      "s_add_u32 m0, s43, 20480\n"
      "s_add_u32 s46, s58, 20480\n"
      "buffer_load_dwordx4 v248, %[rK], s46 offen lds\n"
      "s_add_u32 m0, s43, 24576\n"
      "s_add_u32 s46, s58, 24576\n"
      "buffer_load_dwordx4 v248, %[rK], s46 offen lds\n"
      "s_add_u32 m0, s43, 28672\n"
      "s_add_u32 s46, s58, 28672\n"
      "buffer_load_dwordx4 v248, %[rK], s46 offen lds\n"
      "s_waitcnt vmcnt(0)\n"
      "s_barrier\n"
      "ds_read_b128 a[64:67], v240 offset:0\n"
      "ds_read_b128 a[68:71], v241 offset:0\n"
      "ds_read_b128 a[72:75], v242 offset:0\n"
      "ds_read_b128 a[76:79], v243 offset:0\n"
      "s_waitcnt lgkmcnt(3)\n"
      "v_mfma_f32_16x16x32_bf16 v[0:3], a[64:67], a[0:3], 0\n"
      "v_mfma_f32_16x16x32_bf16 v[4:7], a[64:67], a[16:19], 0\n"
      "v_mfma_f32_16x16x32_bf16 v[8:11], a[64:67], a[32:35], 0\n"
      "v_mfma_f32_16x16x32_bf16 v[12:15], a[64:67], a[48:51], 0\n"
      "s_waitcnt lgkmcnt(2)\n"
      "v_mfma_f32_16x16x32_bf16 v[0:3], a[68:71], a[4:7], v[0:3]\n"
      "v_mfma_f32_16x16x32_bf16 v[4:7], a[68:71], a[20:23], v[4:7]\n"
      "v_mfma_f32_16x16x32_bf16 v[8:11], a[68:71], a[36:39], v[8:11]\n"
      "v_mfma_f32_16x16x32_bf16 v[12:15], a[68:71], a[52:55], v[12:15]\n"
      "s_waitcnt lgkmcnt(1)\n"
      "v_mfma_f32_16x16x32_bf16 v[0:3], a[72:75], a[8:11], v[0:3]\n"
      "v_mfma_f32_16x16x32_bf16 v[4:7], a[72:75], a[24:27], v[4:7]\n"
      "v_mfma_f32_16x16x32_bf16 v[8:11], a[72:75], a[40:43], v[8:11]\n"
      "v_mfma_f32_16x16x32_bf16 v[12:15], a[72:75], a[56:59], v[12:15]\n"
      "s_waitcnt lgkmcnt(0)\n"
      "v_mfma_f32_16x16x32_bf16 v[0:3], a[76:79], a[12:15], v[0:3]\n"
      "v_mfma_f32_16x16x32_bf16 v[4:7], a[76:79], a[28:31], v[4:7]\n"
      "v_mfma_f32_16x16x32_bf16 v[8:11], a[76:79], a[44:47], v[8:11]\n"
      "v_mfma_f32_16x16x32_bf16 v[12:15], a[76:79], a[60:63], v[12:15]\n"
      "s_nop 4\n"
      "v_max3_f32 v40, v40, v0, v1\n"
      "v_max3_f32 v40, v40, v2, v3\n"
      "v_max3_f32 v41, v41, v4, v5\n"
      "v_max3_f32 v41, v41, v6, v7\n"
      "v_max3_f32 v42, v42, v8, v9\n"
      "v_max3_f32 v42, v42, v10, v11\n"
      "v_max3_f32 v43, v43, v12, v13\n"
      "v_max3_f32 v43, v43, v14, v15\n"
      "ds_read_b128 a[64:67], v240 offset:4096\n"
      "ds_read_b128 a[68:71], v241 offset:4096\n"
      "ds_read_b128 a[72:75], v242 offset:4096\n"
      "ds_read_b128 a[76:79], v243 offset:4096\n"
      "s_waitcnt lgkmcnt(3)\n"
      "v_mfma_f32_16x16x32_bf16 v[0:3], a[64:67], a[0:3], 0\n"
      "v_mfma_f32_16x16x32_bf16 v[4:7], a[64:67], a[16:19], 0\n"
      "v_mfma_f32_16x16x32_bf16 v[8:11], a[64:67], a[32:35], 0\n"
      "v_mfma_f32_16x16x32_bf16 v[12:15], a[64:67], a[48:51], 0\n"
      "s_waitcnt lgkmcnt(2)\n"
      "v_mfma_f32_16x16x32_bf16 v[0:3], a[68:71], a[4:7], v[0:3]\n"
      "v_mfma_f32_16x16x32_bf16 v[4:7], a[68:71], a[20:23], v[4:7]\n"
      "v_mfma_f32_16x16x32_bf16 v[8:11], a[68:71], a[36:39], v[8:11]\n"
      "v_mfma_f32_16x16x32_bf16 v[12:15], a[68:71], a[52:55], v[12:15]\n"
      "s_waitcnt lgkmcnt(1)\n"
      "v_mfma_f32_16x16x32_bf16 v[0:3], a[72:75], a[8:11], v[0:3]\n"
      "v_mfma_f32_16x16x32_bf16 v[4:7], a[72:75], a[24:27], v[4:7]\n"
      "v_mfma_f32_16x16x32_bf16 v[8:11], a[72:75], a[40:43], v[8:11]\n"
      "v_mfma_f32_16x16x32_bf16 v[12:15], a[72:75], a[56:59], v[12:15]\n"
      "s_waitcnt lgkmcnt(0)\n"
      "v_mfma_f32_16x16x32_bf16 v[0:3], a[76:79], a[12:15], v[0:3]\n"
      "v_mfma_f32_16x16x32_bf16 v[4:7], a[76:79], a[28:31], v[4:7]\n"
      "v_mfma_f32_16x16x32_bf16 v[8:11], a[76:79], a[44:47], v[8:11]\n"
      "v_mfma_f32_16x16x32_bf16 v[12:15], a[76:79], a[60:63], v[12:15]\n"
      "s_nop 4\n"
      "v_max3_f32 v40, v40, v0, v1\n"
      "v_max3_f32 v40, v40, v2, v3\n"
      "v_max3_f32 v41, v41, v4, v5\n"
      "v_max3_f32 v41, v41, v6, v7\n"
      "v_max3_f32 v42, v42, v8, v9\n"
      "v_max3_f32 v42, v42, v10, v11\n"
      "v_max3_f32 v43, v43, v12, v13\n"
      "v_max3_f32 v43, v43, v14, v15\n"
      "ds_read_b128 a[64:67], v240 offset:8192\n"
      "ds_read_b128 a[68:71], v241 offset:8192\n"
      "ds_read_b128 a[72:75], v242 offset:8192\n"
      "ds_read_b128 a[76:79], v243 offset:8192\n"
      "s_waitcnt lgkmcnt(3)\n"
      "v_mfma_f32_16x16x32_bf16 v[0:3], a[64:67], a[0:3], 0\n"
      "v_mfma_f32_16x16x32_bf16 v[4:7], a[64:67], a[16:19], 0\n"
      "v_mfma_f32_16x16x32_bf16 v[8:11], a[64:67], a[32:35], 0\n"
      "v_mfma_f32_16x16x32_bf16 v[12:15], a[64:67], a[48:51], 0\n"
      "s_waitcnt lgkmcnt(2)\n"
      "v_mfma_f32_16x16x32_bf16 v[0:3], a[68:71], a[4:7], v[0:3]\n"
      "v_mfma_f32_16x16x32_bf16 v[4:7], a[68:71], a[20:23], v[4:7]\n"
      "v_mfma_f32_16x16x32_bf16 v[8:11], a[68:71], a[36:39], v[8:11]\n"
      "v_mfma_f32_16x16x32_bf16 v[12:15], a[68:71], a[52:55], v[12:15]\n"
      "s_waitcnt lgkmcnt(1)\n"
      "v_mfma_f32_16x16x32_bf16 v[0:3], a[72:75], a[8:11], v[0:3]\n"
      "v_mfma_f32_16x16x32_bf16 v[4:7], a[72:75], a[24:27], v[4:7]\n"
      "v_mfma_f32_16x16x32_bf16 v[8:11], a[72:75], a[40:43], v[8:11]\n"
      "v_mfma_f32_16x16x32_bf16 v[12:15], a[72:75], a[56:59], v[12:15]\n"
      "s_waitcnt lgkmcnt(0)\n"
      "v_mfma_f32_16x16x32_bf16 v[0:3], a[76:79], a[12:15], v[0:3]\n"
      "v_mfma_f32_16x16x32_bf16 v[4:7], a[76:79], a[28:31], v[4:7]\n"
      "v_mfma_f32_16x16x32_bf16 v[8:11], a[76:79], a[44:47], v[8:11]\n"
      "v_mfma_f32_16x16x32_bf16 v[12:15], a[76:79], a[60:63], v[12:15]\n"
      "s_nop 4\n"
      "v_max3_f32 v40, v40, v0, v1\n"
      "v_max3_f32 v40, v40, v2, v3\n"
      "v_max3_f32 v41, v41, v4, v5\n"
      "v_max3_f32 v41, v41, v6, v7\n"
      "v_max3_f32 v42, v42, v8, v9\n"
      "v_max3_f32 v42, v42, v10, v11\n"
      "v_max3_f32 v43, v43, v12, v13\n"
      "v_max3_f32 v43, v43, v14, v15\n"
      "ds_read_b128 a[64:67], v240 offset:12288\n"
      "ds_read_b128 a[68:71], v241 offset:12288\n"
      "ds_read_b128 a[72:75], v242 offset:12288\n"
      "ds_read_b128 a[76:79], v243 offset:12288\n"
      "s_waitcnt lgkmcnt(3)\n"
      "v_mfma_f32_16x16x32_bf16 v[0:3], a[64:67], a[0:3], 0\n"
      "v_mfma_f32_16x16x32_bf16 v[4:7], a[64:67], a[16:19], 0\n"
      "v_mfma_f32_16x16x32_bf16 v[8:11], a[64:67], a[32:35], 0\n"
      "v_mfma_f32_16x16x32_bf16 v[12:15], a[64:67], a[48:51], 0\n"
      "s_waitcnt lgkmcnt(2)\n"
      "v_mfma_f32_16x16x32_bf16 v[0:3], a[68:71], a[4:7], v[0:3]\n"
      "v_mfma_f32_16x16x32_bf16 v[4:7], a[68:71], a[20:23], v[4:7]\n"
      "v_mfma_f32_16x16x32_bf16 v[8:11], a[68:71], a[36:39], v[8:11]\n"
      "v_mfma_f32_16x16x32_bf16 v[12:15], a[68:71], a[52:55], v[12:15]\n"
      "s_waitcnt lgkmcnt(1)\n"
      "v_mfma_f32_16x16x32_bf16 v[0:3], a[72:75], a[8:11], v[0:3]\n"
      "v_mfma_f32_16x16x32_bf16 v[4:7], a[72:75], a[24:27], v[4:7]\n"
      "v_mfma_f32_16x16x32_bf16 v[8:11], a[72:75], a[40:43], v[8:11]\n"
      "v_mfma_f32_16x16x32_bf16 v[12:15], a[72:75], a[56:59], v[12:15]\n"
      "s_waitcnt lgkmcnt(0)\n"
      "v_mfma_f32_16x16x32_bf16 v[0:3], a[76:79], a[12:15], v[0:3]\n"
      "v_mfma_f32_16x16x32_bf16 v[4:7], a[76:79], a[28:31], v[4:7]\n"
      "v_mfma_f32_16x16x32_bf16 v[8:11], a[76:79], a[44:47], v[8:11]\n"
      "v_mfma_f32_16x16x32_bf16 v[12:15], a[76:79], a[60:63], v[12:15]\n"
      "s_nop 4\n"
      "v_max3_f32 v40, v40, v0, v1\n"
      "v_max3_f32 v40, v40, v2, v3\n"
      "v_max3_f32 v41, v41, v4, v5\n"
      "v_max3_f32 v41, v41, v6, v7\n"
      "v_max3_f32 v42, v42, v8, v9\n"
      "v_max3_f32 v42, v42, v10, v11\n"
      "v_max3_f32 v43, v43, v12, v13\n"
      "v_max3_f32 v43, v43, v14, v15\n"
      "ds_read_b128 a[64:67], v240 offset:16384\n"
      "ds_read_b128 a[68:71], v241 offset:16384\n"
      "ds_read_b128 a[72:75], v242 offset:16384\n"
      "ds_read_b128 a[76:79], v243 offset:16384\n"
      "s_waitcnt lgkmcnt(3)\n"
      "v_mfma_f32_16x16x32_bf16 v[0:3], a[64:67], a[0:3], 0\n"
      "v_mfma_f32_16x16x32_bf16 v[4:7], a[64:67], a[16:19], 0\n"
      "v_mfma_f32_16x16x32_bf16 v[8:11], a[64:67], a[32:35], 0\n"
      "v_mfma_f32_16x16x32_bf16 v[12:15], a[64:67], a[48:51], 0\n"
      "s_waitcnt lgkmcnt(2)\n"
      "v_mfma_f32_16x16x32_bf16 v[0:3], a[68:71], a[4:7], v[0:3]\n"
      "v_mfma_f32_16x16x32_bf16 v[4:7], a[68:71], a[20:23], v[4:7]\n"
      "v_mfma_f32_16x16x32_bf16 v[8:11], a[68:71], a[36:39], v[8:11]\n"
      "v_mfma_f32_16x16x32_bf16 v[12:15], a[68:71], a[52:55], v[12:15]\n"
      "s_waitcnt lgkmcnt(1)\n"
      "v_mfma_f32_16x16x32_bf16 v[0:3], a[72:75], a[8:11], v[0:3]\n"
      "v_mfma_f32_16x16x32_bf16 v[4:7], a[72:75], a[24:27], v[4:7]\n"
      "v_mfma_f32_16x16x32_bf16 v[8:11], a[72:75], a[40:43], v[8:11]\n"
      "v_mfma_f32_16x16x32_bf16 v[12:15], a[72:75], a[56:59], v[12:15]\n"
      "s_waitcnt lgkmcnt(0)\n"
      "v_mfma_f32_16x16x32_bf16 v[0:3], a[76:79], a[12:15], v[0:3]\n"
      "v_mfma_f32_16x16x32_bf16 v[4:7], a[76:79], a[28:31], v[4:7]\n"
      "v_mfma_f32_16x16x32_bf16 v[8:11], a[76:79], a[44:47], v[8:11]\n"
      "v_mfma_f32_16x16x32_bf16 v[12:15], a[76:79], a[60:63], v[12:15]\n"
      "s_nop 4\n"
      "v_max3_f32 v40, v40, v0, v1\n"
      "v_max3_f32 v40, v40, v2, v3\n"
      "v_max3_f32 v41, v41, v4, v5\n"
      "v_max3_f32 v41, v41, v6, v7\n"
      "v_max3_f32 v42, v42, v8, v9\n"
      "v_max3_f32 v42, v42, v10, v11\n"
      "v_max3_f32 v43, v43, v12, v13\n"
      "v_max3_f32 v43, v43, v14, v15\n"
      "ds_read_b128 a[64:67], v240 offset:20480\n"
      "ds_read_b128 a[68:71], v241 offset:20480\n"
      "ds_read_b128 a[72:75], v242 offset:20480\n"
      "ds_read_b128 a[76:79], v243 offset:20480\n"
      "s_waitcnt lgkmcnt(3)\n"
      "v_mfma_f32_16x16x32_bf16 v[0:3], a[64:67], a[0:3], 0\n"
      "v_mfma_f32_16x16x32_bf16 v[4:7], a[64:67], a[16:19], 0\n"
      "v_mfma_f32_16x16x32_bf16 v[8:11], a[64:67], a[32:35], 0\n"
      "v_mfma_f32_16x16x32_bf16 v[12:15], a[64:67], a[48:51], 0\n"
      "s_waitcnt lgkmcnt(2)\n"
      "v_mfma_f32_16x16x32_bf16 v[0:3], a[68:71], a[4:7], v[0:3]\n"
      "v_mfma_f32_16x16x32_bf16 v[4:7], a[68:71], a[20:23], v[4:7]\n"
      "v_mfma_f32_16x16x32_bf16 v[8:11], a[68:71], a[36:39], v[8:11]\n"
      "v_mfma_f32_16x16x32_bf16 v[12:15], a[68:71], a[52:55], v[12:15]\n"
      "s_waitcnt lgkmcnt(1)\n"
      "v_mfma_f32_16x16x32_bf16 v[0:3], a[72:75], a[8:11], v[0:3]\n"
      "v_mfma_f32_16x16x32_bf16 v[4:7], a[72:75], a[24:27], v[4:7]\n"
      "v_mfma_f32_16x16x32_bf16 v[8:11], a[72:75], a[40:43], v[8:11]\n"
      "v_mfma_f32_16x16x32_bf16 v[12:15], a[72:75], a[56:59], v[12:15]\n"
      "s_waitcnt lgkmcnt(0)\n"
      "v_mfma_f32_16x16x32_bf16 v[0:3], a[76:79], a[12:15], v[0:3]\n"
      "v_mfma_f32_16x16x32_bf16 v[4:7], a[76:79], a[28:31], v[4:7]\n"
      "v_mfma_f32_16x16x32_bf16 v[8:11], a[76:79], a[44:47], v[8:11]\n"
      "v_mfma_f32_16x16x32_bf16 v[12:15], a[76:79], a[60:63], v[12:15]\n"
      "s_nop 4\n"
      "v_max3_f32 v40, v40, v0, v1\n"
      "v_max3_f32 v40, v40, v2, v3\n"
      "v_max3_f32 v41, v41, v4, v5\n"
      "v_max3_f32 v41, v41, v6, v7\n"
      "v_max3_f32 v42, v42, v8, v9\n"
      "v_max3_f32 v42, v42, v10, v11\n"
      "v_max3_f32 v43, v43, v12, v13\n"
      "v_max3_f32 v43, v43, v14, v15\n"
      "ds_read_b128 a[64:67], v240 offset:24576\n"
      "ds_read_b128 a[68:71], v241 offset:24576\n"
      "ds_read_b128 a[72:75], v242 offset:24576\n"
      "ds_read_b128 a[76:79], v243 offset:24576\n"
      "s_waitcnt lgkmcnt(3)\n"
      "v_mfma_f32_16x16x32_bf16 v[0:3], a[64:67], a[0:3], 0\n"
      "v_mfma_f32_16x16x32_bf16 v[4:7], a[64:67], a[16:19], 0\n"
      "v_mfma_f32_16x16x32_bf16 v[8:11], a[64:67], a[32:35], 0\n"
      "v_mfma_f32_16x16x32_bf16 v[12:15], a[64:67], a[48:51], 0\n"
      "s_waitcnt lgkmcnt(2)\n"
      "v_mfma_f32_16x16x32_bf16 v[0:3], a[68:71], a[4:7], v[0:3]\n"
      "v_mfma_f32_16x16x32_bf16 v[4:7], a[68:71], a[20:23], v[4:7]\n"
      "v_mfma_f32_16x16x32_bf16 v[8:11], a[68:71], a[36:39], v[8:11]\n"
      "v_mfma_f32_16x16x32_bf16 v[12:15], a[68:71], a[52:55], v[12:15]\n"
      "s_waitcnt lgkmcnt(1)\n"
      "v_mfma_f32_16x16x32_bf16 v[0:3], a[72:75], a[8:11], v[0:3]\n"
      "v_mfma_f32_16x16x32_bf16 v[4:7], a[72:75], a[24:27], v[4:7]\n"
      "v_mfma_f32_16x16x32_bf16 v[8:11], a[72:75], a[40:43], v[8:11]\n"
      "v_mfma_f32_16x16x32_bf16 v[12:15], a[72:75], a[56:59], v[12:15]\n"
      "s_waitcnt lgkmcnt(0)\n"
      "v_mfma_f32_16x16x32_bf16 v[0:3], a[76:79], a[12:15], v[0:3]\n"
      "v_mfma_f32_16x16x32_bf16 v[4:7], a[76:79], a[28:31], v[4:7]\n"
      "v_mfma_f32_16x16x32_bf16 v[8:11], a[76:79], a[44:47], v[8:11]\n"
      "v_mfma_f32_16x16x32_bf16 v[12:15], a[76:79], a[60:63], v[12:15]\n"
      "s_nop 4\n"
      "v_max3_f32 v40, v40, v0, v1\n"
      "v_max3_f32 v40, v40, v2, v3\n"
      "v_max3_f32 v41, v41, v4, v5\n"
      "v_max3_f32 v41, v41, v6, v7\n"
      "v_max3_f32 v42, v42, v8, v9\n"
      "v_max3_f32 v42, v42, v10, v11\n"
      "v_max3_f32 v43, v43, v12, v13\n"
      "v_max3_f32 v43, v43, v14, v15\n"
      "ds_read_b128 a[64:67], v240 offset:28672\n"
      "ds_read_b128 a[68:71], v241 offset:28672\n"
      "ds_read_b128 a[72:75], v242 offset:28672\n"
      "ds_read_b128 a[76:79], v243 offset:28672\n"
      "s_waitcnt lgkmcnt(3)\n"
      "v_mfma_f32_16x16x32_bf16 v[0:3], a[64:67], a[0:3], 0\n"
      "v_mfma_f32_16x16x32_bf16 v[4:7], a[64:67], a[16:19], 0\n"
      "v_mfma_f32_16x16x32_bf16 v[8:11], a[64:67], a[32:35], 0\n"
      "v_mfma_f32_16x16x32_bf16 v[12:15], a[64:67], a[48:51], 0\n"
      "s_waitcnt lgkmcnt(2)\n"
      "v_mfma_f32_16x16x32_bf16 v[0:3], a[68:71], a[4:7], v[0:3]\n"
      "v_mfma_f32_16x16x32_bf16 v[4:7], a[68:71], a[20:23], v[4:7]\n"
      "v_mfma_f32_16x16x32_bf16 v[8:11], a[68:71], a[36:39], v[8:11]\n"
      "v_mfma_f32_16x16x32_bf16 v[12:15], a[68:71], a[52:55], v[12:15]\n"
      "s_waitcnt lgkmcnt(1)\n"
      "v_mfma_f32_16x16x32_bf16 v[0:3], a[72:75], a[8:11], v[0:3]\n"
      "v_mfma_f32_16x16x32_bf16 v[4:7], a[72:75], a[24:27], v[4:7]\n"
      "v_mfma_f32_16x16x32_bf16 v[8:11], a[72:75], a[40:43], v[8:11]\n"
      "v_mfma_f32_16x16x32_bf16 v[12:15], a[72:75], a[56:59], v[12:15]\n"
      "s_waitcnt lgkmcnt(0)\n"
      "v_mfma_f32_16x16x32_bf16 v[0:3], a[76:79], a[12:15], v[0:3]\n"
      "v_mfma_f32_16x16x32_bf16 v[4:7], a[76:79], a[28:31], v[4:7]\n"
      "v_mfma_f32_16x16x32_bf16 v[8:11], a[76:79], a[44:47], v[8:11]\n"
      "v_mfma_f32_16x16x32_bf16 v[12:15], a[76:79], a[60:63], v[12:15]\n"
      "s_nop 4\n"
      "v_max3_f32 v40, v40, v0, v1\n"
      "v_max3_f32 v40, v40, v2, v3\n"
      "v_max3_f32 v41, v41, v4, v5\n"
      "v_max3_f32 v41, v41, v6, v7\n"
      "v_max3_f32 v42, v42, v8, v9\n"
      "v_max3_f32 v42, v42, v10, v11\n"
      "v_max3_f32 v43, v43, v12, v13\n"
      "v_max3_f32 v43, v43, v14, v15\n"
      "s_barrier\n"
      "s_add_u32 s58, s58, 32768\n"
      "s_cmp_lt_u32 s58, 131072\n"
      "s_cbranch_scc1 .Lmha_mx\n"
      "v_mov_b32 v44, v40\n"
      "v_mov_b32 v45, v41\n"
      "v_mov_b32 v46, v42\n"
      "v_mov_b32 v47, v43\n"
      "v_permlane32_swap_b32 v40, v44\n"
      "v_permlane32_swap_b32 v41, v45\n"
      "v_permlane32_swap_b32 v42, v46\n"
      "v_permlane32_swap_b32 v43, v47\n"
      "v_max_f32 v40, v40, v44\n"
      "v_max_f32 v41, v41, v45\n"
      "v_max_f32 v42, v42, v46\n"
      "v_max_f32 v43, v43, v47\n"
      "v_mov_b32 v44, v40\n"
      "v_mov_b32 v45, v41\n"
      "v_mov_b32 v46, v42\n"
      "v_mov_b32 v47, v43\n"
      "v_permlane16_swap_b32 v40, v44\n"
      "v_permlane16_swap_b32 v41, v45\n"
      "v_permlane16_swap_b32 v42, v46\n"
      "v_permlane16_swap_b32 v43, v47\n"
      "v_max_f32 v40, v40, v44\n"
      "v_max_f32 v41, v41, v45\n"
      "v_max_f32 v42, v42, v46\n"
      "v_max_f32 v43, v43, v47\n"
      "ds_write_b32 v251, v43 offset:0\n"
      "s_waitcnt lgkmcnt(0)\n"
      "s_barrier\n"
      "ds_read_b32 v43, v250 offset:0\n"
      "v_sub_f32 v32, 0, v40\n"
      "v_sub_f32 v33, 0, v41\n"
      "v_sub_f32 v34, 0, v42\n"
      "s_waitcnt lgkmcnt(0)\n"
      "v_sub_f32 v35, 0, v43\n"
      "s_add_u32 m0, s43, 0\n"
      "s_mov_b32 s46, 0\n"
      "buffer_load_dwordx4 v248, %[rK], s46 offen nt lds\n"
      "s_add_u32 m0, s43, 4096\n"
      "s_mov_b32 s46, 4096\n"
      "buffer_load_dwordx4 v248, %[rK], s46 offen nt lds\n"
      "s_add_u32 m0, s43, 8192\n"
      "s_mov_b32 s46, 8192\n"
      "buffer_load_dwordx4 v248, %[rK], s46 offen nt lds\n"
      "s_add_u32 m0, s43, 12288\n"
      "s_mov_b32 s46, 12288\n"
      "buffer_load_dwordx4 v248, %[rK], s46 offen nt lds\n"
      "s_add_u32 m0, s43, 16384\n"
      "s_mov_b32 s46, 16384\n"
      "buffer_load_dwordx4 v248, %[rK], s46 offen nt lds\n"
      "s_add_u32 m0, s43, 20480\n"
      "s_mov_b32 s46, 20480\n"
      "buffer_load_dwordx4 v248, %[rK], s46 offen nt lds\n"
      "s_add_u32 m0, s43, 24576\n"
      "s_mov_b32 s46, 24576\n"
      "buffer_load_dwordx4 v248, %[rK], s46 offen nt lds\n"
      "s_add_u32 m0, s43, 28672\n"
      "s_mov_b32 s46, 28672\n"
      "buffer_load_dwordx4 v248, %[rK], s46 offen nt lds\n"
      "s_add_u32 m0, s43, 65536\n"
      "s_mov_b32 s46, 32768\n"
      "buffer_load_dwordx4 v248, %[rK], s46 offen nt lds\n"
      "s_add_u32 m0, s43, 69632\n"
      "s_mov_b32 s46, 36864\n"
      "buffer_load_dwordx4 v248, %[rK], s46 offen nt lds\n"
      "s_add_u32 m0, s43, 73728\n"
      "s_mov_b32 s46, 40960\n"
      "buffer_load_dwordx4 v248, %[rK], s46 offen nt lds\n"
      "s_add_u32 m0, s43, 77824\n"
      "s_mov_b32 s46, 45056\n"
      "buffer_load_dwordx4 v248, %[rK], s46 offen nt lds\n"
      "s_add_u32 m0, s43, 81920\n"
      "s_mov_b32 s46, 49152\n"
      "buffer_load_dwordx4 v248, %[rK], s46 offen nt lds\n"
      "s_add_u32 m0, s43, 86016\n"
      "s_mov_b32 s46, 53248\n"
      "buffer_load_dwordx4 v248, %[rK], s46 offen nt lds\n"
      "s_add_u32 m0, s43, 90112\n"
      "s_mov_b32 s46, 57344\n"
      "buffer_load_dwordx4 v248, %[rK], s46 offen nt lds\n"
      "s_add_u32 m0, s43, 94208\n"
      "s_mov_b32 s46, 61440\n"
      "buffer_load_dwordx4 v248, %[rK], s46 offen nt lds\n"
      "s_add_u32 m0, s43, 131072\n"
      "s_mov_b32 s46, 0\n"
      "buffer_load_dwordx4 v249, %[rV], s46 offen nt lds\n"
      "s_add_u32 m0, s43, 135168\n"
      "s_mov_b32 s46, 8192\n"
      "buffer_load_dwordx4 v249, %[rV], s46 offen nt lds\n"
      "s_add_u32 m0, s43, 139264\n"
      "s_mov_b32 s46, 16384\n"
      "buffer_load_dwordx4 v249, %[rV], s46 offen nt lds\n"
      "s_add_u32 m0, s43, 143360\n"
      "s_mov_b32 s46, 24576\n"
      "buffer_load_dwordx4 v249, %[rV], s46 offen nt lds\n"
      "s_add_u32 m0, s43, 147456\n"
      "s_mov_b32 s46, 32768\n"
      "buffer_load_dwordx4 v249, %[rV], s46 offen nt lds\n"
      "s_add_u32 m0, s43, 151552\n"
      "s_mov_b32 s46, 40960\n"
      "buffer_load_dwordx4 v249, %[rV], s46 offen nt lds\n"
      "s_add_u32 m0, s43, 155648\n"
      "s_mov_b32 s46, 49152\n"
      "buffer_load_dwordx4 v249, %[rV], s46 offen nt lds\n"
      "s_add_u32 m0, s43, 159744\n"
      "s_mov_b32 s46, 57344\n"
      "buffer_load_dwordx4 v249, %[rV], s46 offen nt lds\n"
      "s_waitcnt vmcnt(0)\n"
      "s_barrier\n"
      "v_xor_b32 v161, 0, v153\n"
      "v_xor_b32 v162, 64, v153\n"
      "v_xor_b32 v163, 128, v153\n"
      "v_xor_b32 v164, 192, v153\n"
      "ds_read_b128 a[168:171], v161 offset:0\n"
      "ds_read_b128 a[172:175], v162 offset:0\n"
      "ds_read_b128 a[176:179], v163 offset:0\n"
      "ds_read_b128 a[180:183], v164 offset:0\n"
      "s_waitcnt lgkmcnt(0)\n"
      "s_mov_b32 s42, -1\n"
      "s_branch .Lmha_S\n"
      ".Lmha_end:\n"
      : "+{v[136:151]}"(qa), "+{v251}"(xs)
      : [rX] "s"(rX), [rW] "s"(rW), [rK] "s"(rK), [rV] "s"(rV), [rWo] "s"(rWo), [rO] "s"(rO), [rP] "s"(rP), [rC] "s"(rC), [rOut] "s"(rOut),
        [w] "s"(w), [cq] "s"((b * NH + hq * 4) * 1024), [cg] "s"((b * NH + nq) * 1024), [tw] "s"((((b & 3) * 4 + nq) * 32 + w * 8) * 1024), [rot] "s"(rot), [nq] "s"(nq), [hq1k] "s"(hq * 1024), [red0] "s"(red0), [red1] "s"(red1), [out0] "s"(out0), [out1] "s"(out1)
      : "v0", "v1", "v2", "v3", "v4", "v5", "v6", "v7", "v8", "v9", "v10", "v11", "v12", "v13", "v14", "v15", "v16", "v17", "v18", "v19", "v20", "v21", "v22", "v23", "v24", "v25", "v26", "v27", "v28", "v29", "v30", "v31", "v32", "v33", "v34", "v35", "v36", "v37", "v38", "v39", "v40", "v41", "v42", "v43", "v44", "v45", "v46", "v47", "v48", "v49", "v50", "v51", "v52", "v53", "v54", "v55", "v56", "v57", "v58", "v59", "v60", "v61", "v62", "v63", "v64", "v65", "v66", "v67", "v68", "v69", "v70", "v71", "v72", "v73", "v74", "v75", "v76", "v77", "v78", "v79", "v80", "v81", "v82", "v83", "v84", "v85", "v86", "v87", "v88", "v89", "v90", "v91", "v92", "v93", "v94", "v95", "v96", "v97", "v98", "v99", "v100", "v101", "v102", "v103", "v104", "v105", "v106", "v107", "v108", "v109", "v110", "v111", "v112", "v113", "v114", "v115", "v116", "v117", "v118", "v119", "v120", "v121", "v122", "v123", "v124", "v125", "v126", "v127", "v128", "v129", "v130", "v131", "v132", "v133", "v134", "v135", "v152", "v153", "v154", "v155", "v156", "v157", "v158", "v159", "v160", "v161", "v162", "v163", "v164", "v165", "v166", "v167", "v168", "v169", "v170", "v171", "v172", "v173", "v174", "v175", "v176", "v177", "v178", "v179", "v180", "v181", "v182", "v183", "v184", "v185", "v186", "v187", "v188", "v189", "v190", "v191", "v192", "v193", "v194", "v195", "v196", "v197", "v198", "v199", "v200", "v201", "v202", "v203", "v204", "v205", "v206", "v207", "v208", "v209", "v210", "v211", "v212", "v213", "v214", "v215", "v216", "v217", "v218", "v219", "v220", "v221", "v222", "v223", "v224", "v225", "v226", "v227", "v228", "v229", "v230", "v231", "v232", "v233", "v234", "v235", "v236", "v237", "v238", "v239", "v240", "v241", "v242", "v243", "v244", "v245", "v246", "v247", "v248", "v249", "v250", "v252", "v253", "v254", "v255", "a0", "a1", "a2", "a3", "a4", "a5", "a6", "a7", "a8", "a9", "a10", "a11", "a12", "a13", "a14", "a15", "a16", "a17", "a18", "a19", "a20", "a21", "a22", "a23", "a24", "a25", "a26", "a27", "a28", "a29", "a30", "a31", "a32", "a33", "a34", "a35", "a36", "a37", "a38", "a39", "a40", "a41", "a42", "a43", "a44", "a45", "a46", "a47", "a48", "a49", "a50", "a51", "a52", "a53", "a54", "a55", "a56", "a57", "a58", "a59", "a60", "a61", "a62", "a63", "a64", "a65", "a66", "a67", "a68", "a69", "a70", "a71", "a72", "a73", "a74", "a75", "a76", "a77", "a78", "a79", "a80", "a81", "a82", "a83", "a84", "a85", "a86", "a87", "a88", "a89", "a90", "a91", "a92", "a93", "a94", "a95", "a96", "a97", "a98", "a99", "a100", "a101", "a102", "a103", "a104", "a105", "a106", "a107", "a108", "a109", "a110", "a111", "a112", "a113", "a114", "a115", "a116", "a117", "a118", "a119", "a120", "a121", "a122", "a123", "a124", "a125", "a126", "a127", "a128", "a129", "a130", "a131", "a132", "a133", "a134", "a135", "a136", "a137", "a138", "a139", "a140", "a141", "a142", "a143", "a144", "a145", "a146", "a147", "a148", "a149", "a150", "a151", "a152", "a153", "a154", "a155", "a156", "a157", "a158", "a159", "a160", "a161", "a162", "a163", "a164", "a165", "a166", "a167", "a168", "a169", "a170", "a171", "a172", "a173", "a174", "a175", "a176", "a177", "a178", "a179", "a180", "a181", "a182", "a183", "a184", "a185", "a186", "a187", "a188", "a189", "a190", "a191", "a192", "a193", "a194", "a195", "a196", "a197", "a198", "a199", "a200", "a201", "a202", "a203", "a204", "a205", "a206", "a207", "a208", "a209", "a210", "a211", "a212", "a213", "a214", "a215", "a216", "a217", "a218", "a219", "a220", "a221", "a222", "a223", "a224", "a225", "a226", "a227", "a228", "a229", "a230", "a231", "a232", "a233", "a234", "a235", "a236", "a237", "a238", "a239", "a240", "a241", "a242", "a243", "a244", "a245", "a246", "a247", "a248", "a249", "a250", "a251", "a252", "a253", "a254", "a255", "s36", "s37", "s38", "s39", "s40", "s41", "s42", "s43", "s44", "s45", "s46", "s47", "s48", "s49", "s50", "s51", "s52", "s53", "s54", "s55", "s56", "s57", "s58", "s59", "vcc", "scc", "memory");
}

extern "C" void kernel_launch(void* const* d_in, const int* in_sizes, int n_in, void* d_out, int out_size, void* d_ws, size_t ws_size, hipStream_t stream) {
    mha_kernel<<<256, 256, 0, stream>>>((const char*)d_in[0], (const char*)d_in[1], (const char*)d_in[2], (const char*)d_in[3], (const char*)d_in[4], (char*)d_out, (char*)d_ws);
}
```
